```python
import torch

def _softmax(x, axis=-1):
    return torch.softmax(x, dim=axis)
def _gelu(x):
    return torch.nn.functional.gelu(x, approximate="tanh")
def _silu(x):
    return torch.nn.functional.silu(x)
def _sigmoid(x):
    return torch.sigmoid(x)
def _layernorm(x, axis=-1, eps=1e-5):
    m = x.mean(dim=axis, keepdim=True)
    v = x.var(dim=axis, keepdim=True, unbiased=False)
    return (x - m) / torch.sqrt(v + eps)
def _scatter_add(tgt, idx, src, axis=0):
    return tgt.clone().index_add_(axis, idx, src)
def _segment_sum(src, seg, n):
    out = torch.zeros((n,) + src.shape[1:], dtype=src.dtype)
    return out.index_add_(0, seg, src)
def _pad(x, pw):
    flat = tuple(v for pair in reversed(pw) for v in pair)
    return torch.nn.functional.pad(x, flat)

kernel_name = 'sequential_preLN__spectral_fnet__glu_gated_glu__prompt'

def setup_inputs():

    _w = (lambda a: (lambda t: t.to(torch.bfloat16) if t.is_floating_point() else t)(torch.as_tensor(a)))
    def _A_setup():
        B, S, D = 1, 2048, 576
        ns = torch.arange(S)[:, None] * torch.arange(S)[None, :]
        nd = torch.arange(D)[:, None] * torch.arange(D)[None, :]
        Ws_re = torch.cos(-2.0 * torch.pi * ns / S).to(float)
        Ws_im = torch.sin(-2.0 * torch.pi * ns / S).to(float)
        Wd_re = torch.cos(-2.0 * torch.pi * nd / D).to(float)
        Wd_im = torch.sin(-2.0 * torch.pi * nd / D).to(float)
        return {
            "x": _w((lambda *s: torch.randn(*s, dtype=torch.bfloat16) * 0.05)(B, S, D)),
            "dft_seq_re": _w(Ws_re),
            "dft_seq_im": _w(Ws_im),
            "dft_hid_re": _w(Wd_re),
            "dft_hid_im": _w(Wd_im),
        }
    _A_in = _A_setup()

    def _B_setup():
        B, S, D, F = 1, 2048, 576, 4608
        return {
            "x": _w((lambda *s: torch.randn(*s, dtype=torch.bfloat16) * 0.05)(B, S, D)),
            "w_gate": _w((lambda *s: torch.randn(*s, dtype=torch.bfloat16) * 1.6)(D, F)),
            "w_up": _w((lambda *s: torch.randn(*s, dtype=torch.bfloat16) * 0.4)(D, F)),
            "w_down": _w((lambda *s: torch.randn(*s, dtype=torch.bfloat16) * 0.4)(F, D)),
        }
    _B_in = _B_setup()

    return {
        "A_x": _A_in["x"],
        "A_dft_seq_re": _A_in["dft_seq_re"],
        "A_dft_seq_im": _A_in["dft_seq_im"],
        "A_dft_hid_re": _A_in["dft_hid_re"],
        "A_dft_hid_im": _A_in["dft_hid_im"],
        "B_x": _B_in["x"],
        "B_w_gate": _B_in["w_gate"],
        "B_w_up": _B_in["w_up"],
        "B_w_down": _B_in["w_down"],
    }

def reference(A_x, A_dft_seq_re, A_dft_seq_im, A_dft_hid_re, A_dft_hid_im, B_x, B_w_gate, B_w_up, B_w_down):
    def _A_ref(x, dft_seq_re, dft_seq_im, dft_hid_re, dft_hid_im):
        a = torch.einsum("bsd,ed->bse", x, dft_hid_re)
        b = torch.einsum("bsd,ed->bse", x, dft_hid_im)
        y = torch.einsum("ts,bse->bte", dft_seq_re, a) - torch.einsum("ts,bse->bte", dft_seq_im, b)
        return y

    def _B_ref(x, w_gate, w_up, w_down):
        g = torch.einsum("bsd,df->bsf", x, w_gate)
        u = torch.einsum("bsd,df->bsf", x, w_up)
        h = _sigmoid(g) * u
        return torch.einsum("bsf,fd->bsd", h, w_down)

    x = A_x
    h = x + _A_ref(_layernorm(x, axis=-1), A_dft_seq_re, A_dft_seq_im, A_dft_hid_re, A_dft_hid_im)
    h2 = h + _B_ref(_layernorm(h, axis=-1), B_w_gate, B_w_up, B_w_down)
    return h2
```

```cpp
#include <hip/hip_runtime.h>
#include <hip/hip_bf16.h>
#include <stdint.h>

typedef unsigned short u16;
using bf16x8 = __attribute__((ext_vector_type(8))) short;
using f32x4 = __attribute__((ext_vector_type(4))) float;

constexpr int S = 2048, D = 576, F = 4608;

__device__ __forceinline__ float bf2f(u16 v) { return __uint_as_float(((unsigned)v) << 16); }
__device__ __forceinline__ u16 f2bf(float f) {
  unsigned u = __float_as_uint(f);
  u += 0x7fffu + ((u >> 16) & 1u);
  return (u16)(u >> 16);
}

__global__ void ln_kernel(const u16* __restrict__ x, u16* __restrict__ y) {
  int row = blockIdx.x * 4 + (threadIdx.x >> 6);
  int lane = threadIdx.x & 63;
  float v[9];
  float s = 0.f;
  for (int i = 0; i < 9; ++i) { v[i] = bf2f(x[row * D + lane + 64 * i]); s += v[i]; }
  for (int o = 32; o; o >>= 1) s += __shfl_xor(s, o);
  float m = s / D;
  float q = 0.f;
  for (int i = 0; i < 9; ++i) { float d = v[i] - m; q += d * d; }
  for (int o = 32; o; o >>= 1) q += __shfl_xor(q, o);
  float r = rsqrtf(q / D + 1e-5f);
  for (int i = 0; i < 9; ++i) y[row * D + lane + 64 * i] = f2bf((v[i] - m) * r);
}

template <int MODE>
__global__ void gemm_naive(const u16* __restrict__ A, int lda, const u16* __restrict__ B, int sbk, int sbn,
                           const u16* __restrict__ A2, const u16* __restrict__ B2,
                           int K, int N, const u16* __restrict__ res, u16* __restrict__ C, int ldc) {
  int lane = threadIdx.x & 63;
  int tile = blockIdx.x * 4 + (threadIdx.x >> 6);
  int nt = N / 16;
  int tm = tile / nt, tn = tile % nt;
  int r = lane & 15, q = lane >> 4;
  f32x4 acc = {0, 0, 0, 0}, acc2 = {0, 0, 0, 0};
  for (int k0 = 0; k0 < K; k0 += 32) {
    bf16x8 a = *(const bf16x8*)(A + (size_t)(tm * 16 + r) * lda + k0 + q * 8);
    bf16x8 b;
    for (int j = 0; j < 8; ++j) b[j] = B[(size_t)(k0 + q * 8 + j) * sbk + (size_t)(tn * 16 + r) * sbn];
    acc = __builtin_amdgcn_mfma_f32_16x16x32_bf16(a, b, acc, 0, 0, 0);
    if (MODE == 1 || MODE == 2) {
      bf16x8 a2 = (MODE == 1) ? *(const bf16x8*)(A2 + (size_t)(tm * 16 + r) * lda + k0 + q * 8) : a;
      bf16x8 b2;
      for (int j = 0; j < 8; ++j) b2[j] = B2[(size_t)(k0 + q * 8 + j) * sbk + (size_t)(tn * 16 + r) * sbn];
      acc2 = __builtin_amdgcn_mfma_f32_16x16x32_bf16(a2, b2, acc2, 0, 0, 0);
    }
  }
  for (int j = 0; j < 4; ++j) {
    int row = tm * 16 + q * 4 + j, col = tn * 16 + r;
    float v;
    if (MODE == 0) v = acc[j];
    else if (MODE == 1) v = bf2f(res[(size_t)row * ldc + col]) + (acc[j] - acc2[j]);
    else if (MODE == 2) v = acc2[j] / (1.f + __expf(-acc[j]));
    else v = bf2f(res[(size_t)row * ldc + col]) + acc[j];
    C[(size_t)row * ldc + col] = f2bf(v);
  }
}

extern "C" void kernel_launch(void* const* d_in, const int* in_sizes, int n_in,
                              void* d_out, int out_size, void* d_ws, size_t ws_size,
                              hipStream_t stream) {
  const u16* x = (const u16*)d_in[0];
  const u16* cr = (const u16*)d_in[1];
  const u16* ci = (const u16*)d_in[2];
  const u16* wr = (const u16*)d_in[3];
  const u16* wi = (const u16*)d_in[4];
  const u16* wg = (const u16*)d_in[6];
  const u16* wu = (const u16*)d_in[7];
  const u16* wd = (const u16*)d_in[8];
  u16* out = (u16*)d_out;
  u16* ws = (u16*)d_ws;
  u16* xn = ws;
  u16* a = xn + S * D;
  u16* b = a + S * D;
  u16* h = b + S * D;
  u16* hn = h + S * D;
  u16* hh = hn + S * D;
  ln_kernel<<<S / 4, 256, 0, stream>>>(x, xn);
  gemm_naive<0><<<(S / 16) * (D / 16) / 4, 256, 0, stream>>>(xn, D, wr, 1, D, nullptr, nullptr, D, D, nullptr, a, D);
  gemm_naive<0><<<(S / 16) * (D / 16) / 4, 256, 0, stream>>>(xn, D, wi, 1, D, nullptr, nullptr, D, D, nullptr, b, D);
  gemm_naive<1><<<(S / 16) * (D / 16) / 4, 256, 0, stream>>>(cr, S, a, D, 1, ci, b, S, D, x, h, D);
  ln_kernel<<<S / 4, 256, 0, stream>>>(h, hn);
  gemm_naive<2><<<(S / 16) * (F / 16) / 4, 256, 0, stream>>>(hn, D, wg, F, 1, nullptr, wu, D, F, nullptr, hh, F);
  gemm_naive<3><<<(S / 16) * (D / 16) / 4, 256, 0, stream>>>(hh, F, wd, D, 1, nullptr, nullptr, F, D, h, out, D);
}
```

```cpp
#include <hip/hip_runtime.h>
#include <hip/hip_bf16.h>

extern "C" __device__ unsigned long long fnet_dispatch_id() __asm("llvm.amdgcn.dispatch.id");

__global__ __launch_bounds__(256, 1) __attribute__((aligned(4096))) void fnet_glu_kernel(const void* x, const void* cr, const void* ci, const void* wre, const void* wim,
                                                          const void* wg, const void* wu, const void* wd, void* out, void* ws) {
  __shared__ __attribute__((aligned(16))) char lds[163840];
  asm volatile("" ::"s"((unsigned)(size_t)lds));
  asm volatile(
      "Lstart_%=:\n"
      "s_load_dwordx16 s[8:23], %0, 0\n"
      "s_load_dwordx4 s[24:27], %0, 64\n"
      "s_mov_b32 s73, %1\n"
      "s_mov_b64 s[76:77], %3\n"
      "s_getpc_b64 s[94:95]\n"
      "Lpc_1_%=:\n"
      "s_sub_u32 s94, s94, Lpc_1_%=-Lstart_%=\n"
      "s_subb_u32 s95, s95, 0\n"
      "v_and_b32 v253, 7, v0\n"
      "v_lshrrev_b32 v252, 6, v0\n"
      "v_lshl_add_u32 v252, v253, 7, v252\n"
      "s_lshr_b32 s97, s73, 3\n"
      "s_lshl_b32 s97, s97, 2\n"
      "v_add_u32 v252, s97, v252\n"
      "v_lshlrev_b32 v252, 7, v252\n"
      "v_min_u32 v252, Lend_%=-Lstart_%=, v252\n"
      "global_load_dword v253, v252, s[94:95]\n"
      "v_lshrrev_b32 v28, 6, v0\n"
      "v_and_b32 v2, 63, v0\n"
      "v_readfirstlane_b32 s72, v28\n"
      "v_lshlrev_b32 v1, 4, v2\n"
      "s_and_b32 s74, s73, 7\n"
      "s_lshr_b32 s75, s73, 3\n"
      "s_and_b32 s29, s75, 3\n"
      "v_lshrrev_b32 v216, 5, v2\n"
      "v_mul_u32_u24 v216, 9216, v216\n"
      "v_bfe_u32 v228, v2, 3, 2\n"
      "v_mul_u32_u24 v228, 1152, v228\n"
      "v_add_u32 v216, v216, v228\n"
      "v_and_b32 v228, 7, v2\n"
      "v_lshl_add_u32 v216, v228, 4, v216\n"
      "s_bfe_u32 s31, s74, 0x20001\n"
      "s_lshl_b32 s31, s31, 3\n"
      "s_bfe_u32 s97, s75, 0x30002\n"
      "s_add_u32 s31, s31, s97\n"
      "s_mul_i32 s92, s31, 73728\n"
      "s_bfe_u32 s97, s72, 0x10001\n"
      "s_mul_i32 s97, s97, 36864\n"
      "s_add_u32 s92, s92, s97\n"
      "s_bfe_u32 s97, s72, 0x10000\n"
      "s_mul_i32 s97, s97, 4608\n"
      "s_add_u32 s92, s92, s97\n"
      "s_add_u32 s93, s92, 18432\n"
      "s_mul_i32 s90, s72, 41\n"
      "v_add_u32 v229, s90, v2\n"
      "s_mul_i32 s90, s72, 41\n"
      "s_add_u32 s90, s90, 0x28\n"
      "s_min_u32 s90, s90, 161\n"
      "v_min_u32 v229, s90, v229\n"
      "v_lshlrev_b32 v229, 7, v229\n"
      "s_mul_i32 s91, s75, 20736\n"
      "s_waitcnt lgkmcnt(0)\n"
      "s_bitcmp1_b32 s74, 0\n"
      "s_cselect_b32 s44, s16, s14\n"
      "s_cselect_b32 s45, s17, s15\n"
      "s_and_b32 s45, s45, 0xffff\n"
      "s_mov_b32 s46, 0xa2000\n"
      "s_mov_b32 s47, 0x20000\n"
      "s_mov_b32 s40, s8\n"
      "s_and_b32 s41, s9, 0xffff\n"
      "s_mov_b32 s42, 0x240000\n"
      "s_mov_b32 s43, 0x20000\n"
      "buffer_load_dwordx4 v[32:35], v216, s[40:43], s92 offen\n"
      "buffer_load_dwordx4 v[36:39], v216, s[40:43], s92 offen offset:128\n"
      "buffer_load_dwordx4 v[40:43], v216, s[40:43], s92 offen offset:256\n"
      "buffer_load_dwordx4 v[44:47], v216, s[40:43], s92 offen offset:384\n"
      "buffer_load_dwordx4 v[48:51], v216, s[40:43], s92 offen offset:512\n"
      "buffer_load_dwordx4 v[52:55], v216, s[40:43], s92 offen offset:640\n"
      "buffer_load_dwordx4 v[56:59], v216, s[40:43], s92 offen offset:768\n"
      "buffer_load_dwordx4 v[60:63], v216, s[40:43], s92 offen offset:896\n"
      "buffer_load_dwordx4 v[64:67], v216, s[40:43], s92 offen offset:1024\n"
      "buffer_load_dword v251, v229, s[44:47], s91 offen\n"
      "s_mul_i32 s78, s72, 1024\n"
      "s_mul_i32 s79, s72, 2048\n"
      "s_mul_i32 s80, s72, 3072\n"
      "s_mul_i32 s81, s72, 4096\n"
      "s_mov_b32 s36, s26\n"
      "s_and_b32 s37, s27, 0xffff\n"
      "s_mov_b32 s38, 0x4000000\n"
      "s_mov_b32 s39, 0x20000\n"
      "v_lshrrev_b32 v228, 4, v2\n"
      "v_xor_b32 v228, v228, v2\n"
      "v_and_b32 v228, 7, v228\n"
      "v_lshlrev_b32 v228, 4, v228\n"
      "s_bfe_u32 s90, s72, 0x10000\n"
      "s_lshl_b32 s90, s90, 6\n"
      "v_lshrrev_b32 v217, 3, v2\n"
      "v_mul_u32_u24 v217, 1152, v217\n"
      "v_xor_b32 v229, s90, v228\n"
      "v_add_u32 v217, v217, v229\n"
      "s_mul_i32 s59, s29, 18432\n"
      "s_bfe_u32 s97, s72, 0x10000\n"
      "s_mul_i32 s97, s97, 9216\n"
      "s_add_u32 s59, s59, s97\n"
      "s_mov_b32 s35, s59\n"
      "s_bfe_u32 s97, s72, 0x10001\n"
      "s_lshl_b32 s97, s97, 7\n"
      "s_add_u32 s35, s35, s97\n"
      "s_mov_b32 s34, s59\n"
      "s_bfe_u32 s97, s72, 0x10001\n"
      "s_mul_i32 s97, s97, 73728\n"
      "s_add_u32 s34, s34, s97\n"
      "s_lshl_b32 s90, s72, 11\n"
      "s_add_u32 s90, s90, 0x17000\n"
      "v_lshrrev_b32 v218, 3, v2\n"
      "v_lshlrev_b32 v218, 7, v218\n"
      "v_add_u32 v218, s90, v218\n"
      "v_add_u32 v218, v218, v228\n"
      "v_xor_b32 v219, 64, v218\n"
      "v_add_u32 v219, 1024, v219\n"
      "v_add_u32 v247, s78, v1\n"
      "v_add_u32 v248, 36864, v247\n"
      "v_lshrrev_b32 v240, 5, v2\n"
      "v_lshlrev_b32 v240, 15, v240\n"
      "v_bfe_u32 v229, v2, 3, 2\n"
      "v_lshl_add_u32 v240, v229, 12, v240\n"
      "v_add_u32 v240, v240, v228\n"
      "v_xor_b32 v241, 64, v240\n"
      "v_add_u32 v241, 64512, v241\n"
      "v_lshrrev_b32 v228, 4, v2\n"
      "v_bfe_u32 v229, v2, 1, 3\n"
      "v_xor_b32 v228, v228, v229\n"
      "v_and_b32 v220, 15, v2\n"
      "v_lshlrev_b32 v220, 7, v220\n"
      "v_lshl_add_u32 v220, v228, 4, v220\n"
      "v_xor_b32 v221, 64, v220\n"
      "s_bfe_u32 s90, s72, 0x10001\n"
      "s_lshl_b32 s90, s90, 12\n"
      "s_add_u32 s90, s90, 0x17000\n"
      "v_add_u32 v222, s90, v220\n"
      "v_add_u32 v223, s90, v221\n"
      "s_bfe_u32 s90, s72, 0x10000\n"
      "s_lshl_b32 s90, s90, 14\n"
      "v_add_u32 v224, s90, v220\n"
      "v_add_u32 v225, s90, v221\n"
      "s_bfe_u32 s90, s72, 0x10000\n"
      "s_lshl_b32 s90, s90, 13\n"
      "s_add_u32 s90, s90, 0x12000\n"
      "v_add_u32 v226, s90, v220\n"
      "v_add_u32 v227, s90, v221\n"
      "s_lshl_b32 s90, s72, 11\n"
      "s_add_u32 s90, s90, 0x1c000\n"
      "v_add_u32 v242, s90, v220\n"
      "v_add_u32 v243, s90, v221\n"
      "s_lshl_b32 s90, s72, 6\n"
      "s_add_u32 s90, s90, 0x21000\n"
      "v_lshrrev_b32 v244, 3, v2\n"
      "v_lshlrev_b32 v244, 2, v244\n"
      "v_add_u32 v244, s90, v244\n"
      "s_bfe_u32 s90, s72, 0x10001\n"
      "s_lshl_b32 s90, s90, 7\n"
      "s_add_u32 s90, s90, 0x21000\n"
      "v_lshrrev_b32 v245, 4, v2\n"
      "v_lshlrev_b32 v245, 4, v245\n"
      "v_add_u32 v245, s90, v245\n"
      "s_bfe_u32 s30, s74, 0x20001\n"
      "s_bfe_u32 s28, s74, 0x10000\n"
      "s_lshl_b32 s28, s28, 3\n"
      "s_bfe_u32 s97, s75, 0x30002\n"
      "s_add_u32 s28, s28, s97\n"
      "s_cmp_ge_u32 s30, 2\n"
      "s_cselect_b32 s48, s12, s10\n"
      "s_cselect_b32 s49, s13, s11\n"
      "s_and_b32 s49, s49, 0xffff\n"
      "s_mov_b32 s50, 0x800000\n"
      "s_mov_b32 s51, 0x20000\n"
      "s_lshl_b32 s56, s28, 19\n"
      "s_lshl_b32 s97, s72, 17\n"
      "s_add_u32 s56, s56, s97\n"
      "s_bfe_u32 s97, s30, 0x10000\n"
      "s_lshl_b32 s97, s97, 11\n"
      "s_add_u32 s56, s56, s97\n"
      "v_lshrrev_b32 v246, 3, v2\n"
      "v_lshlrev_b32 v246, 12, v246\n"
      "v_and_b32 v229, 7, v2\n"
      "v_lshl_add_u32 v246, v229, 7, v246\n"
      "v_and_b32 v229, 7, v2\n"
      "v_min_u32 v254, 3, v229\n"
      "v_max_u32 v255, 4, v229\n"
      "v_and_b32 v229, 0xfffff000, v246\n"
      "v_lshl_add_u32 v254, v254, 7, v229\n"
      "v_lshl_add_u32 v255, v255, 7, v229\n"
      "s_lshl_b32 s4, s28, 19\n"
      "s_lshl_b32 s97, s29, 17\n"
      "s_add_u32 s4, s4, s97\n"
      "s_lshl_b32 s97, s72, 15\n"
      "s_add_u32 s4, s4, s97\n"
      "s_bfe_u32 s97, s30, 0x10000\n"
      "s_lshl_b32 s97, s97, 11\n"
      "s_add_u32 s4, s4, s97\n"
      "s_bfe_u32 s90, s74, 0x10000\n"
      "s_mul_i32 s90, s90, 2359296\n"
      "s_bfe_u32 s97, s31, 0x10004\n"
      "s_mul_i32 s97, s97, 1179648\n"
      "s_add_u32 s90, s90, s97\n"
      "s_mul_i32 s97, s29, 294912\n"
      "s_add_u32 s90, s90, s97\n"
      "s_bfe_u32 s97, s72, 0x10001\n"
      "s_lshl_b32 s97, s97, 10\n"
      "s_add_u32 s90, s90, s97\n"
      "s_add_u32 s90, s90, 0x100000\n"
      "s_mov_b32 s57, s90\n"
      "s_bfe_u32 s97, s31, 0x40000\n"
      "s_lshl_b32 s97, s97, 11\n"
      "s_add_u32 s57, s57, s97\n"
      "s_bfe_u32 s97, s72, 0x10000\n"
      "s_lshl_b32 s97, s97, 13\n"
      "s_add_u32 s57, s57, s97\n"
      "s_mov_b32 s58, s90\n"
      "s_bfe_u32 s97, s31, 0x40000\n"
      "s_lshl_b32 s97, s97, 11\n"
      "s_add_u32 s58, s58, s97\n"
      "s_bfe_u32 s97, s72, 0x10000\n"
      "s_lshl_b32 s97, s97, 17\n"
      "s_add_u32 s58, s58, s97\n"
      "s_bitcmp1_b32 s74, 0\n"
      "s_cselect_b32 s9, 0x80000000, 0\n"
      "s_getpc_b64 s[14:15]\n"
      "Lwh_2_%=:\n"
      "s_add_u32 s14, s14, Lwb_3_%=-Lwh_2_%=\n"
      "s_addc_u32 s15, s15, 0\n"
      "s_mov_b64 exec, 0\n"
      "s_cmp_eq_u32 s72, 0\n"
      "s_cbranch_scc1 Lret_%=\n"
      "s_cmp_eq_u32 s72, 1\n"
      "s_cbranch_scc1 Lpad0_%=\n"
      "s_cmp_eq_u32 s72, 2\n"
      "s_cbranch_scc1 Lret_%=\n"
      "s_branch Lpad1_%=\n"
      "Lret_%=:\n"
      "s_setpc_b64 s[14:15]\n"
      "Lwb_3_%=:\n"
      "s_mov_b64 exec, -1\n"
      "v_mov_b32 v16, -1\n"
      "v_mov_b32 v17, -1\n"
      "v_mov_b32 v18, -1\n"
      "v_mov_b32 v19, -1\n"
      "s_mul_i32 s90, s28, 589824\n"
      "s_mul_i32 s97, s30, 147456\n"
      "s_add_u32 s90, s90, s97\n"
      "s_mul_i32 s97, s29, 36864\n"
      "s_add_u32 s90, s90, s97\n"
      "s_mul_i32 s97, s72, 9216\n"
      "s_add_u32 s90, s90, s97\n"
      "s_add_u32 s90, s90, 0x601800\n"
      "s_waitcnt vmcnt(9)\n"
      "buffer_load_dwordx4 v[68:71], v216, s[40:43], s93 offen\n"
      "buffer_load_dwordx4 v[72:75], v216, s[40:43], s93 offen offset:128\n"
      "v_lshlrev_b32 v104, 16, v32\n"
      "v_lshlrev_b32 v105, 16, v33\n"
      "v_lshlrev_b32 v106, 16, v34\n"
      "v_lshlrev_b32 v107, 16, v35\n"
      "v_and_b32 v32, 0xffff0000, v32\n"
      "v_and_b32 v33, 0xffff0000, v33\n"
      "v_and_b32 v34, 0xffff0000, v34\n"
      "v_and_b32 v35, 0xffff0000, v35\n"
      "v_pk_add_f32 v[232:233], v[104:105], v[106:107]\n"
      "v_pk_add_f32 v[234:235], v[32:33], v[34:35]\n"
      "s_waitcnt vmcnt(10)\n"
      "buffer_load_dwordx4 v[76:79], v216, s[40:43], s93 offen offset:256\n"
      "buffer_load_dwordx4 v[80:83], v216, s[40:43], s93 offen offset:384\n"
      "v_lshlrev_b32 v108, 16, v36\n"
      "v_lshlrev_b32 v109, 16, v37\n"
      "v_lshlrev_b32 v110, 16, v38\n"
      "v_lshlrev_b32 v111, 16, v39\n"
      "v_and_b32 v36, 0xffff0000, v36\n"
      "v_and_b32 v37, 0xffff0000, v37\n"
      "v_and_b32 v38, 0xffff0000, v38\n"
      "v_and_b32 v39, 0xffff0000, v39\n"
      "v_pk_add_f32 v[232:233], v[232:233], v[108:109]\n"
      "v_pk_add_f32 v[234:235], v[234:235], v[36:37]\n"
      "v_pk_add_f32 v[232:233], v[232:233], v[110:111]\n"
      "v_pk_add_f32 v[234:235], v[234:235], v[38:39]\n"
      "s_waitcnt vmcnt(11)\n"
      "buffer_load_dwordx4 v[84:87], v216, s[40:43], s93 offen offset:512\n"
      "buffer_load_dwordx4 v[88:91], v216, s[40:43], s93 offen offset:640\n"
      "v_lshlrev_b32 v112, 16, v40\n"
      "v_lshlrev_b32 v113, 16, v41\n"
      "v_lshlrev_b32 v114, 16, v42\n"
      "v_lshlrev_b32 v115, 16, v43\n"
      "v_and_b32 v40, 0xffff0000, v40\n"
      "v_and_b32 v41, 0xffff0000, v41\n"
      "v_and_b32 v42, 0xffff0000, v42\n"
      "v_and_b32 v43, 0xffff0000, v43\n"
      "v_pk_add_f32 v[232:233], v[232:233], v[112:113]\n"
      "v_pk_add_f32 v[234:235], v[234:235], v[40:41]\n"
      "v_pk_add_f32 v[232:233], v[232:233], v[114:115]\n"
      "v_pk_add_f32 v[234:235], v[234:235], v[42:43]\n"
      "s_waitcnt vmcnt(12)\n"
      "buffer_load_dwordx4 v[92:95], v216, s[40:43], s93 offen offset:768\n"
      "v_lshlrev_b32 v116, 16, v44\n"
      "v_lshlrev_b32 v117, 16, v45\n"
      "v_lshlrev_b32 v118, 16, v46\n"
      "v_lshlrev_b32 v119, 16, v47\n"
      "v_and_b32 v44, 0xffff0000, v44\n"
      "v_and_b32 v45, 0xffff0000, v45\n"
      "v_and_b32 v46, 0xffff0000, v46\n"
      "v_and_b32 v47, 0xffff0000, v47\n"
      "v_pk_add_f32 v[232:233], v[232:233], v[116:117]\n"
      "v_pk_add_f32 v[234:235], v[234:235], v[44:45]\n"
      "v_pk_add_f32 v[232:233], v[232:233], v[118:119]\n"
      "v_pk_add_f32 v[234:235], v[234:235], v[46:47]\n"
      "s_waitcnt vmcnt(12)\n"
      "buffer_load_dwordx4 v[96:99], v216, s[40:43], s93 offen offset:896\n"
      "v_lshlrev_b32 v120, 16, v48\n"
      "v_lshlrev_b32 v121, 16, v49\n"
      "v_lshlrev_b32 v122, 16, v50\n"
      "v_lshlrev_b32 v123, 16, v51\n"
      "v_and_b32 v48, 0xffff0000, v48\n"
      "v_and_b32 v49, 0xffff0000, v49\n"
      "v_and_b32 v50, 0xffff0000, v50\n"
      "v_and_b32 v51, 0xffff0000, v51\n"
      "v_pk_add_f32 v[232:233], v[232:233], v[120:121]\n"
      "v_pk_add_f32 v[234:235], v[234:235], v[48:49]\n"
      "v_pk_add_f32 v[232:233], v[232:233], v[122:123]\n"
      "v_pk_add_f32 v[234:235], v[234:235], v[50:51]\n"
      "s_waitcnt vmcnt(12)\n"
      "buffer_load_dwordx4 v[100:103], v216, s[40:43], s93 offen offset:1024\n"
      "v_lshlrev_b32 v124, 16, v52\n"
      "v_lshlrev_b32 v125, 16, v53\n"
      "v_lshlrev_b32 v126, 16, v54\n"
      "v_lshlrev_b32 v127, 16, v55\n"
      "v_and_b32 v52, 0xffff0000, v52\n"
      "v_and_b32 v53, 0xffff0000, v53\n"
      "v_and_b32 v54, 0xffff0000, v54\n"
      "v_and_b32 v55, 0xffff0000, v55\n"
      "v_pk_add_f32 v[232:233], v[232:233], v[124:125]\n"
      "v_pk_add_f32 v[234:235], v[234:235], v[52:53]\n"
      "v_pk_add_f32 v[232:233], v[232:233], v[126:127]\n"
      "v_pk_add_f32 v[234:235], v[234:235], v[54:55]\n"
      "s_waitcnt vmcnt(12)\n"
      "v_mov_b32 v228, 0\n"
      "buffer_load_dwordx2 v[230:231], v228, s[36:39], 0 offen sc1\n"
      "v_lshlrev_b32 v128, 16, v56\n"
      "v_lshlrev_b32 v129, 16, v57\n"
      "v_lshlrev_b32 v130, 16, v58\n"
      "v_lshlrev_b32 v131, 16, v59\n"
      "v_and_b32 v56, 0xffff0000, v56\n"
      "v_and_b32 v57, 0xffff0000, v57\n"
      "v_and_b32 v58, 0xffff0000, v58\n"
      "v_and_b32 v59, 0xffff0000, v59\n"
      "v_pk_add_f32 v[232:233], v[232:233], v[128:129]\n"
      "v_pk_add_f32 v[234:235], v[234:235], v[56:57]\n"
      "v_pk_add_f32 v[232:233], v[232:233], v[130:131]\n"
      "v_pk_add_f32 v[234:235], v[234:235], v[58:59]\n"
      "s_waitcnt vmcnt(12)\n"
      "s_add_u32 m0, s78, 0\n"
      "s_add_u32 s96, s35, 0\n"
      "buffer_load_dwordx4 v217, s[44:47], s96 offen lds\n"
      "v_lshlrev_b32 v132, 16, v60\n"
      "v_lshlrev_b32 v133, 16, v61\n"
      "v_lshlrev_b32 v134, 16, v62\n"
      "v_lshlrev_b32 v135, 16, v63\n"
      "v_and_b32 v60, 0xffff0000, v60\n"
      "v_and_b32 v61, 0xffff0000, v61\n"
      "v_and_b32 v62, 0xffff0000, v62\n"
      "v_and_b32 v63, 0xffff0000, v63\n"
      "v_pk_add_f32 v[232:233], v[232:233], v[132:133]\n"
      "v_pk_add_f32 v[234:235], v[234:235], v[60:61]\n"
      "v_pk_add_f32 v[232:233], v[232:233], v[134:135]\n"
      "v_pk_add_f32 v[234:235], v[234:235], v[62:63]\n"
      "s_waitcnt vmcnt(12)\n"
      "s_add_u32 m0, s78, 4096\n"
      "s_add_u32 s96, s35, 73728\n"
      "buffer_load_dwordx4 v217, s[44:47], s96 offen lds\n"
      "v_lshlrev_b32 v136, 16, v64\n"
      "v_lshlrev_b32 v137, 16, v65\n"
      "v_lshlrev_b32 v138, 16, v66\n"
      "v_lshlrev_b32 v139, 16, v67\n"
      "v_and_b32 v64, 0xffff0000, v64\n"
      "v_and_b32 v65, 0xffff0000, v65\n"
      "v_and_b32 v66, 0xffff0000, v66\n"
      "v_and_b32 v67, 0xffff0000, v67\n"
      "v_pk_add_f32 v[232:233], v[232:233], v[136:137]\n"
      "v_pk_add_f32 v[234:235], v[234:235], v[64:65]\n"
      "v_pk_add_f32 v[232:233], v[232:233], v[138:139]\n"
      "v_pk_add_f32 v[234:235], v[234:235], v[66:67]\n"
      "v_pk_add_f32 v[232:233], v[232:233], v[234:235]\n"
      "v_add_f32 v232, v232, v233\n"
      "s_nop 1\n"
      "v_add_f32_dpp v232, v232, v232 quad_perm:[1,0,3,2] row_mask:0xf bank_mask:0xf\n"
      "s_nop 1\n"
      "v_add_f32_dpp v232, v232, v232 quad_perm:[2,3,0,1] row_mask:0xf bank_mask:0xf\n"
      "s_nop 1\n"
      "v_add_f32_dpp v232, v232, v232 row_half_mirror row_mask:0xf bank_mask:0xf\n"
      "v_mul_f32 v232, 0xbae38e39, v232\n"
      "v_pk_add_f32 v[104:105], v[104:105], v[232:233] op_sel_hi:[1,0]\n"
      "v_pk_add_f32 v[106:107], v[106:107], v[232:233] op_sel_hi:[1,0]\n"
      "v_pk_add_f32 v[32:33], v[32:33], v[232:233] op_sel_hi:[1,0]\n"
      "v_pk_add_f32 v[34:35], v[34:35], v[232:233] op_sel_hi:[1,0]\n"
      "v_pk_mul_f32 v[234:235], v[104:105], v[104:105]\n"
      "v_pk_mul_f32 v[228:229], v[106:107], v[106:107]\n"
      "v_pk_fma_f32 v[234:235], v[32:33], v[32:33], v[234:235]\n"
      "v_pk_fma_f32 v[228:229], v[34:35], v[34:35], v[228:229]\n"
      "v_cvt_pk_bf16_f32 v104, v104, v32\n"
      "v_cvt_pk_bf16_f32 v105, v105, v33\n"
      "v_cvt_pk_bf16_f32 v106, v106, v34\n"
      "v_cvt_pk_bf16_f32 v107, v107, v35\n"
      "ds_write_b128 v218, v[104:107]\n"
      "s_add_u32 m0, s78, 8192\n"
      "s_add_u32 s96, s35, 147456\n"
      "buffer_load_dwordx4 v217, s[44:47], s96 offen lds\n"
      "v_pk_add_f32 v[108:109], v[108:109], v[232:233] op_sel_hi:[1,0]\n"
      "v_pk_add_f32 v[110:111], v[110:111], v[232:233] op_sel_hi:[1,0]\n"
      "v_pk_add_f32 v[36:37], v[36:37], v[232:233] op_sel_hi:[1,0]\n"
      "v_pk_add_f32 v[38:39], v[38:39], v[232:233] op_sel_hi:[1,0]\n"
      "v_pk_fma_f32 v[234:235], v[108:109], v[108:109], v[234:235]\n"
      "v_pk_fma_f32 v[228:229], v[110:111], v[110:111], v[228:229]\n"
      "v_pk_fma_f32 v[234:235], v[36:37], v[36:37], v[234:235]\n"
      "v_pk_fma_f32 v[228:229], v[38:39], v[38:39], v[228:229]\n"
      "v_cvt_pk_bf16_f32 v108, v108, v36\n"
      "v_cvt_pk_bf16_f32 v109, v109, v37\n"
      "v_cvt_pk_bf16_f32 v110, v110, v38\n"
      "v_cvt_pk_bf16_f32 v111, v111, v39\n"
      "ds_write_b128 v218, v[108:111] offset:8192\n"
      "s_add_u32 m0, s78, 12288\n"
      "s_add_u32 s96, s35, 221184\n"
      "buffer_load_dwordx4 v217, s[44:47], s96 offen lds\n"
      "v_pk_add_f32 v[112:113], v[112:113], v[232:233] op_sel_hi:[1,0]\n"
      "v_pk_add_f32 v[114:115], v[114:115], v[232:233] op_sel_hi:[1,0]\n"
      "v_pk_add_f32 v[40:41], v[40:41], v[232:233] op_sel_hi:[1,0]\n"
      "v_pk_add_f32 v[42:43], v[42:43], v[232:233] op_sel_hi:[1,0]\n"
      "v_pk_fma_f32 v[234:235], v[112:113], v[112:113], v[234:235]\n"
      "v_pk_fma_f32 v[228:229], v[114:115], v[114:115], v[228:229]\n"
      "v_pk_fma_f32 v[234:235], v[40:41], v[40:41], v[234:235]\n"
      "v_pk_fma_f32 v[228:229], v[42:43], v[42:43], v[228:229]\n"
      "v_cvt_pk_bf16_f32 v112, v112, v40\n"
      "v_cvt_pk_bf16_f32 v113, v113, v41\n"
      "v_cvt_pk_bf16_f32 v114, v114, v42\n"
      "v_cvt_pk_bf16_f32 v115, v115, v43\n"
      "ds_write_b128 v218, v[112:115] offset:16384\n"
      "s_add_u32 m0, s78, 16384\n"
      "s_add_u32 s96, s35, 294912\n"
      "buffer_load_dwordx4 v217, s[44:47], s96 offen lds\n"
      "v_pk_add_f32 v[116:117], v[116:117], v[232:233] op_sel_hi:[1,0]\n"
      "v_pk_add_f32 v[118:119], v[118:119], v[232:233] op_sel_hi:[1,0]\n"
      "v_pk_add_f32 v[44:45], v[44:45], v[232:233] op_sel_hi:[1,0]\n"
      "v_pk_add_f32 v[46:47], v[46:47], v[232:233] op_sel_hi:[1,0]\n"
      "v_pk_fma_f32 v[234:235], v[116:117], v[116:117], v[234:235]\n"
      "v_pk_fma_f32 v[228:229], v[118:119], v[118:119], v[228:229]\n"
      "v_pk_fma_f32 v[234:235], v[44:45], v[44:45], v[234:235]\n"
      "v_pk_fma_f32 v[228:229], v[46:47], v[46:47], v[228:229]\n"
      "v_cvt_pk_bf16_f32 v116, v116, v44\n"
      "v_cvt_pk_bf16_f32 v117, v117, v45\n"
      "v_cvt_pk_bf16_f32 v118, v118, v46\n"
      "v_cvt_pk_bf16_f32 v119, v119, v47\n"
      "ds_write_b128 v218, v[116:119] offset:24576\n"
      "s_add_u32 m0, s78, 20480\n"
      "s_add_u32 s96, s35, 368640\n"
      "buffer_load_dwordx4 v217, s[44:47], s96 offen lds\n"
      "v_pk_add_f32 v[136:137], v[136:137], v[232:233] op_sel_hi:[1,0]\n"
      "v_pk_add_f32 v[138:139], v[138:139], v[232:233] op_sel_hi:[1,0]\n"
      "v_pk_add_f32 v[64:65], v[64:65], v[232:233] op_sel_hi:[1,0]\n"
      "v_pk_add_f32 v[66:67], v[66:67], v[232:233] op_sel_hi:[1,0]\n"
      "v_pk_fma_f32 v[234:235], v[136:137], v[136:137], v[234:235]\n"
      "v_pk_fma_f32 v[228:229], v[138:139], v[138:139], v[228:229]\n"
      "v_pk_fma_f32 v[234:235], v[64:65], v[64:65], v[234:235]\n"
      "v_pk_fma_f32 v[228:229], v[66:67], v[66:67], v[228:229]\n"
      "v_cvt_pk_bf16_f32 v136, v136, v64\n"
      "v_cvt_pk_bf16_f32 v137, v137, v65\n"
      "v_cvt_pk_bf16_f32 v138, v138, v66\n"
      "v_cvt_pk_bf16_f32 v139, v139, v67\n"
      "ds_write_b128 v218, v[136:139] offset:32768\n"
      "s_add_u32 m0, s78, 24576\n"
      "s_add_u32 s96, s35, 442368\n"
      "buffer_load_dwordx4 v217, s[44:47], s96 offen lds\n"
      "v_pk_add_f32 v[120:121], v[120:121], v[232:233] op_sel_hi:[1,0]\n"
      "v_pk_add_f32 v[122:123], v[122:123], v[232:233] op_sel_hi:[1,0]\n"
      "v_pk_add_f32 v[48:49], v[48:49], v[232:233] op_sel_hi:[1,0]\n"
      "v_pk_add_f32 v[50:51], v[50:51], v[232:233] op_sel_hi:[1,0]\n"
      "v_pk_fma_f32 v[234:235], v[120:121], v[120:121], v[234:235]\n"
      "v_pk_fma_f32 v[228:229], v[122:123], v[122:123], v[228:229]\n"
      "v_pk_fma_f32 v[234:235], v[48:49], v[48:49], v[234:235]\n"
      "v_pk_fma_f32 v[228:229], v[50:51], v[50:51], v[228:229]\n"
      "v_cvt_pk_bf16_f32 v120, v120, v48\n"
      "v_cvt_pk_bf16_f32 v121, v121, v49\n"
      "v_cvt_pk_bf16_f32 v122, v122, v50\n"
      "v_cvt_pk_bf16_f32 v123, v123, v51\n"
      "s_add_u32 m0, s78, 28672\n"
      "s_add_u32 s96, s35, 516096\n"
      "buffer_load_dwordx4 v217, s[44:47], s96 offen lds\n"
      "v_pk_add_f32 v[124:125], v[124:125], v[232:233] op_sel_hi:[1,0]\n"
      "v_pk_add_f32 v[126:127], v[126:127], v[232:233] op_sel_hi:[1,0]\n"
      "v_pk_add_f32 v[52:53], v[52:53], v[232:233] op_sel_hi:[1,0]\n"
      "v_pk_add_f32 v[54:55], v[54:55], v[232:233] op_sel_hi:[1,0]\n"
      "v_pk_fma_f32 v[234:235], v[124:125], v[124:125], v[234:235]\n"
      "v_pk_fma_f32 v[228:229], v[126:127], v[126:127], v[228:229]\n"
      "v_pk_fma_f32 v[234:235], v[52:53], v[52:53], v[234:235]\n"
      "v_pk_fma_f32 v[228:229], v[54:55], v[54:55], v[228:229]\n"
      "v_cvt_pk_bf16_f32 v124, v124, v52\n"
      "v_cvt_pk_bf16_f32 v125, v125, v53\n"
      "v_cvt_pk_bf16_f32 v126, v126, v54\n"
      "v_cvt_pk_bf16_f32 v127, v127, v55\n"
      "s_add_u32 m0, s78, 32768\n"
      "s_add_u32 s96, s35, 589824\n"
      "buffer_load_dwordx4 v217, s[44:47], s96 offen lds\n"
      "v_pk_add_f32 v[128:129], v[128:129], v[232:233] op_sel_hi:[1,0]\n"
      "v_pk_add_f32 v[130:131], v[130:131], v[232:233] op_sel_hi:[1,0]\n"
      "v_pk_add_f32 v[56:57], v[56:57], v[232:233] op_sel_hi:[1,0]\n"
      "v_pk_add_f32 v[58:59], v[58:59], v[232:233] op_sel_hi:[1,0]\n"
      "v_pk_fma_f32 v[234:235], v[128:129], v[128:129], v[234:235]\n"
      "v_pk_fma_f32 v[228:229], v[130:131], v[130:131], v[228:229]\n"
      "v_pk_fma_f32 v[234:235], v[56:57], v[56:57], v[234:235]\n"
      "v_pk_fma_f32 v[228:229], v[58:59], v[58:59], v[228:229]\n"
      "v_cvt_pk_bf16_f32 v128, v128, v56\n"
      "v_cvt_pk_bf16_f32 v129, v129, v57\n"
      "v_cvt_pk_bf16_f32 v130, v130, v58\n"
      "v_cvt_pk_bf16_f32 v131, v131, v59\n"
      "s_add_u32 m0, s78, 36864\n"
      "s_add_u32 s96, s35, 256\n"
      "buffer_load_dwordx4 v217, s[44:47], s96 offen lds\n"
      "v_pk_add_f32 v[132:133], v[132:133], v[232:233] op_sel_hi:[1,0]\n"
      "v_pk_add_f32 v[134:135], v[134:135], v[232:233] op_sel_hi:[1,0]\n"
      "v_pk_add_f32 v[60:61], v[60:61], v[232:233] op_sel_hi:[1,0]\n"
      "v_pk_add_f32 v[62:63], v[62:63], v[232:233] op_sel_hi:[1,0]\n"
      "v_pk_fma_f32 v[234:235], v[132:133], v[132:133], v[234:235]\n"
      "v_pk_fma_f32 v[228:229], v[134:135], v[134:135], v[228:229]\n"
      "v_pk_fma_f32 v[234:235], v[60:61], v[60:61], v[234:235]\n"
      "v_pk_fma_f32 v[228:229], v[62:63], v[62:63], v[228:229]\n"
      "v_cvt_pk_bf16_f32 v132, v132, v60\n"
      "v_cvt_pk_bf16_f32 v133, v133, v61\n"
      "v_cvt_pk_bf16_f32 v134, v134, v62\n"
      "v_cvt_pk_bf16_f32 v135, v135, v63\n"
      "s_add_u32 m0, s78, 40960\n"
      "s_add_u32 s96, s35, 73984\n"
      "buffer_load_dwordx4 v217, s[44:47], s96 offen lds\n"
      "v_pk_add_f32 v[234:235], v[234:235], v[228:229]\n"
      "v_add_f32 v234, v234, v235\n"
      "s_nop 1\n"
      "v_add_f32_dpp v234, v234, v234 quad_perm:[1,0,3,2] row_mask:0xf bank_mask:0xf\n"
      "s_nop 1\n"
      "v_add_f32_dpp v234, v234, v234 quad_perm:[2,3,0,1] row_mask:0xf bank_mask:0xf\n"
      "s_nop 1\n"
      "v_add_f32_dpp v234, v234, v234 row_half_mirror row_mask:0xf bank_mask:0xf\n"
      "v_mov_b32 v235, 0x3727c5ac\n"
      "v_fmac_f32 v235, 0x3ae38e39, v234\n"
      "v_rsq_f32 v234, v235\n"
      "s_nop 0\n"
      "v_xor_b32 v234, s9, v234\n"
      "ds_write_b32 v244, v234\n"
      "s_waitcnt vmcnt(20)\n"
      "s_add_u32 m0, s78, 45056\n"
      "s_add_u32 s96, s35, 147712\n"
      "buffer_load_dwordx4 v217, s[44:47], s96 offen lds\n"
      "v_lshlrev_b32 v140, 16, v68\n"
      "v_lshlrev_b32 v141, 16, v69\n"
      "v_lshlrev_b32 v142, 16, v70\n"
      "v_lshlrev_b32 v143, 16, v71\n"
      "v_and_b32 v68, 0xffff0000, v68\n"
      "v_and_b32 v69, 0xffff0000, v69\n"
      "v_and_b32 v70, 0xffff0000, v70\n"
      "v_and_b32 v71, 0xffff0000, v71\n"
      "v_pk_add_f32 v[236:237], v[140:141], v[142:143]\n"
      "v_pk_add_f32 v[238:239], v[68:69], v[70:71]\n"
      "s_waitcnt vmcnt(20)\n"
      "s_add_u32 m0, s78, 49152\n"
      "s_add_u32 s96, s35, 221440\n"
      "buffer_load_dwordx4 v217, s[44:47], s96 offen lds\n"
      "v_lshlrev_b32 v144, 16, v72\n"
      "v_lshlrev_b32 v145, 16, v73\n"
      "v_lshlrev_b32 v146, 16, v74\n"
      "v_lshlrev_b32 v147, 16, v75\n"
      "v_and_b32 v72, 0xffff0000, v72\n"
      "v_and_b32 v73, 0xffff0000, v73\n"
      "v_and_b32 v74, 0xffff0000, v74\n"
      "v_and_b32 v75, 0xffff0000, v75\n"
      "v_pk_add_f32 v[236:237], v[236:237], v[144:145]\n"
      "v_pk_add_f32 v[238:239], v[238:239], v[72:73]\n"
      "v_pk_add_f32 v[236:237], v[236:237], v[146:147]\n"
      "v_pk_add_f32 v[238:239], v[238:239], v[74:75]\n"
      "s_waitcnt vmcnt(20)\n"
      "s_add_u32 m0, s78, 53248\n"
      "s_add_u32 s96, s35, 295168\n"
      "buffer_load_dwordx4 v217, s[44:47], s96 offen lds\n"
      "v_lshlrev_b32 v148, 16, v76\n"
      "v_lshlrev_b32 v149, 16, v77\n"
      "v_lshlrev_b32 v150, 16, v78\n"
      "v_lshlrev_b32 v151, 16, v79\n"
      "v_and_b32 v76, 0xffff0000, v76\n"
      "v_and_b32 v77, 0xffff0000, v77\n"
      "v_and_b32 v78, 0xffff0000, v78\n"
      "v_and_b32 v79, 0xffff0000, v79\n"
      "v_pk_add_f32 v[236:237], v[236:237], v[148:149]\n"
      "v_pk_add_f32 v[238:239], v[238:239], v[76:77]\n"
      "v_pk_add_f32 v[236:237], v[236:237], v[150:151]\n"
      "v_pk_add_f32 v[238:239], v[238:239], v[78:79]\n"
      "s_waitcnt vmcnt(20)\n"
      "s_add_u32 m0, s78, 57344\n"
      "s_add_u32 s96, s35, 368896\n"
      "buffer_load_dwordx4 v217, s[44:47], s96 offen lds\n"
      "v_lshlrev_b32 v152, 16, v80\n"
      "v_lshlrev_b32 v153, 16, v81\n"
      "v_lshlrev_b32 v154, 16, v82\n"
      "v_lshlrev_b32 v155, 16, v83\n"
      "v_and_b32 v80, 0xffff0000, v80\n"
      "v_and_b32 v81, 0xffff0000, v81\n"
      "v_and_b32 v82, 0xffff0000, v82\n"
      "v_and_b32 v83, 0xffff0000, v83\n"
      "v_pk_add_f32 v[236:237], v[236:237], v[152:153]\n"
      "v_pk_add_f32 v[238:239], v[238:239], v[80:81]\n"
      "v_pk_add_f32 v[236:237], v[236:237], v[154:155]\n"
      "v_pk_add_f32 v[238:239], v[238:239], v[82:83]\n"
      "s_waitcnt vmcnt(20)\n"
      "s_add_u32 m0, s78, 61440\n"
      "s_add_u32 s96, s35, 442624\n"
      "buffer_load_dwordx4 v217, s[44:47], s96 offen lds\n"
      "v_lshlrev_b32 v156, 16, v84\n"
      "v_lshlrev_b32 v157, 16, v85\n"
      "v_lshlrev_b32 v158, 16, v86\n"
      "v_lshlrev_b32 v159, 16, v87\n"
      "v_and_b32 v84, 0xffff0000, v84\n"
      "v_and_b32 v85, 0xffff0000, v85\n"
      "v_and_b32 v86, 0xffff0000, v86\n"
      "v_and_b32 v87, 0xffff0000, v87\n"
      "v_pk_add_f32 v[236:237], v[236:237], v[156:157]\n"
      "v_pk_add_f32 v[238:239], v[238:239], v[84:85]\n"
      "v_pk_add_f32 v[236:237], v[236:237], v[158:159]\n"
      "v_pk_add_f32 v[238:239], v[238:239], v[86:87]\n"
      "s_waitcnt vmcnt(20)\n"
      "s_add_u32 m0, s78, 65536\n"
      "s_add_u32 s96, s35, 516352\n"
      "buffer_load_dwordx4 v217, s[44:47], s96 offen lds\n"
      "v_lshlrev_b32 v160, 16, v88\n"
      "v_lshlrev_b32 v161, 16, v89\n"
      "v_lshlrev_b32 v162, 16, v90\n"
      "v_lshlrev_b32 v163, 16, v91\n"
      "v_and_b32 v88, 0xffff0000, v88\n"
      "v_and_b32 v89, 0xffff0000, v89\n"
      "v_and_b32 v90, 0xffff0000, v90\n"
      "v_and_b32 v91, 0xffff0000, v91\n"
      "v_pk_add_f32 v[236:237], v[236:237], v[160:161]\n"
      "v_pk_add_f32 v[238:239], v[238:239], v[88:89]\n"
      "Lpad0_%=:\n"
      "s_cbranch_execz Lret_%=\n"
      "v_pk_add_f32 v[236:237], v[236:237], v[162:163]\n"
      "v_pk_add_f32 v[238:239], v[238:239], v[90:91]\n"
      "s_waitcnt vmcnt(20)\n"
      "s_add_u32 m0, s78, 69632\n"
      "s_add_u32 s96, s35, 590080\n"
      "buffer_load_dwordx4 v217, s[44:47], s96 offen lds\n"
      "v_lshlrev_b32 v164, 16, v92\n"
      "v_lshlrev_b32 v165, 16, v93\n"
      "v_lshlrev_b32 v166, 16, v94\n"
      "v_lshlrev_b32 v167, 16, v95\n"
      "v_and_b32 v92, 0xffff0000, v92\n"
      "v_and_b32 v93, 0xffff0000, v93\n"
      "v_and_b32 v94, 0xffff0000, v94\n"
      "v_and_b32 v95, 0xffff0000, v95\n"
      "v_pk_add_f32 v[236:237], v[236:237], v[164:165]\n"
      "v_pk_add_f32 v[238:239], v[238:239], v[92:93]\n"
      "v_pk_add_f32 v[236:237], v[236:237], v[166:167]\n"
      "v_pk_add_f32 v[238:239], v[238:239], v[94:95]\n"
      "s_waitcnt vmcnt(20)\n"
      "s_add_u32 m0, s78, 73728\n"
      "s_add_u32 s96, s34, 1024\n"
      "buffer_load_dwordx4 v217, s[44:47], s96 offen lds\n"
      "v_lshlrev_b32 v168, 16, v96\n"
      "v_lshlrev_b32 v169, 16, v97\n"
      "v_lshlrev_b32 v170, 16, v98\n"
      "v_lshlrev_b32 v171, 16, v99\n"
      "v_and_b32 v96, 0xffff0000, v96\n"
      "v_and_b32 v97, 0xffff0000, v97\n"
      "v_and_b32 v98, 0xffff0000, v98\n"
      "v_and_b32 v99, 0xffff0000, v99\n"
      "v_pk_add_f32 v[236:237], v[236:237], v[168:169]\n"
      "v_pk_add_f32 v[238:239], v[238:239], v[96:97]\n"
      "v_pk_add_f32 v[236:237], v[236:237], v[170:171]\n"
      "v_pk_add_f32 v[238:239], v[238:239], v[98:99]\n"
      "s_waitcnt vmcnt(20)\n"
      "s_add_u32 m0, s78, 77824\n"
      "s_add_u32 s96, s34, 148480\n"
      "buffer_load_dwordx4 v217, s[44:47], s96 offen lds\n"
      "v_lshlrev_b32 v172, 16, v100\n"
      "v_lshlrev_b32 v173, 16, v101\n"
      "v_lshlrev_b32 v174, 16, v102\n"
      "v_lshlrev_b32 v175, 16, v103\n"
      "v_and_b32 v100, 0xffff0000, v100\n"
      "v_and_b32 v101, 0xffff0000, v101\n"
      "v_and_b32 v102, 0xffff0000, v102\n"
      "v_and_b32 v103, 0xffff0000, v103\n"
      "v_pk_add_f32 v[236:237], v[236:237], v[172:173]\n"
      "v_pk_add_f32 v[238:239], v[238:239], v[100:101]\n"
      "v_pk_add_f32 v[236:237], v[236:237], v[174:175]\n"
      "v_pk_add_f32 v[238:239], v[238:239], v[102:103]\n"
      "v_pk_add_f32 v[236:237], v[236:237], v[238:239]\n"
      "v_add_f32 v236, v236, v237\n"
      "s_nop 1\n"
      "v_add_f32_dpp v236, v236, v236 quad_perm:[1,0,3,2] row_mask:0xf bank_mask:0xf\n"
      "s_nop 1\n"
      "v_add_f32_dpp v236, v236, v236 quad_perm:[2,3,0,1] row_mask:0xf bank_mask:0xf\n"
      "s_nop 1\n"
      "v_add_f32_dpp v236, v236, v236 row_half_mirror row_mask:0xf bank_mask:0xf\n"
      "v_mul_f32 v236, 0xbae38e39, v236\n"
      "v_pk_add_f32 v[140:141], v[140:141], v[236:237] op_sel_hi:[1,0]\n"
      "v_pk_add_f32 v[142:143], v[142:143], v[236:237] op_sel_hi:[1,0]\n"
      "v_pk_add_f32 v[68:69], v[68:69], v[236:237] op_sel_hi:[1,0]\n"
      "v_pk_add_f32 v[70:71], v[70:71], v[236:237] op_sel_hi:[1,0]\n"
      "v_pk_mul_f32 v[238:239], v[140:141], v[140:141]\n"
      "v_pk_mul_f32 v[228:229], v[142:143], v[142:143]\n"
      "v_pk_fma_f32 v[238:239], v[68:69], v[68:69], v[238:239]\n"
      "v_pk_fma_f32 v[228:229], v[70:71], v[70:71], v[228:229]\n"
      "v_cvt_pk_bf16_f32 v140, v140, v68\n"
      "v_cvt_pk_bf16_f32 v141, v141, v69\n"
      "v_cvt_pk_bf16_f32 v142, v142, v70\n"
      "v_cvt_pk_bf16_f32 v143, v143, v71\n"
      "ds_write_b128 v219, v[140:143]\n"
      "s_add_u32 m0, s78, 81920\n"
      "s_add_u32 s96, s34, 295936\n"
      "buffer_load_dwordx4 v217, s[44:47], s96 offen lds\n"
      "v_pk_add_f32 v[144:145], v[144:145], v[236:237] op_sel_hi:[1,0]\n"
      "v_pk_add_f32 v[146:147], v[146:147], v[236:237] op_sel_hi:[1,0]\n"
      "v_pk_add_f32 v[72:73], v[72:73], v[236:237] op_sel_hi:[1,0]\n"
      "v_pk_add_f32 v[74:75], v[74:75], v[236:237] op_sel_hi:[1,0]\n"
      "v_pk_fma_f32 v[238:239], v[144:145], v[144:145], v[238:239]\n"
      "v_pk_fma_f32 v[228:229], v[146:147], v[146:147], v[228:229]\n"
      "v_pk_fma_f32 v[238:239], v[72:73], v[72:73], v[238:239]\n"
      "v_pk_fma_f32 v[228:229], v[74:75], v[74:75], v[228:229]\n"
      "v_cvt_pk_bf16_f32 v144, v144, v72\n"
      "v_cvt_pk_bf16_f32 v145, v145, v73\n"
      "v_cvt_pk_bf16_f32 v146, v146, v74\n"
      "v_cvt_pk_bf16_f32 v147, v147, v75\n"
      "ds_write_b128 v219, v[144:147] offset:8192\n"
      "s_add_u32 m0, s78, 86016\n"
      "s_add_u32 s96, s34, 443392\n"
      "buffer_load_dwordx4 v217, s[44:47], s96 offen lds\n"
      "v_pk_add_f32 v[148:149], v[148:149], v[236:237] op_sel_hi:[1,0]\n"
      "v_pk_add_f32 v[150:151], v[150:151], v[236:237] op_sel_hi:[1,0]\n"
      "v_pk_add_f32 v[76:77], v[76:77], v[236:237] op_sel_hi:[1,0]\n"
      "v_pk_add_f32 v[78:79], v[78:79], v[236:237] op_sel_hi:[1,0]\n"
      "v_pk_fma_f32 v[238:239], v[148:149], v[148:149], v[238:239]\n"
      "v_pk_fma_f32 v[228:229], v[150:151], v[150:151], v[228:229]\n"
      "v_pk_fma_f32 v[238:239], v[76:77], v[76:77], v[238:239]\n"
      "v_pk_fma_f32 v[228:229], v[78:79], v[78:79], v[228:229]\n"
      "v_cvt_pk_bf16_f32 v148, v148, v76\n"
      "v_cvt_pk_bf16_f32 v149, v149, v77\n"
      "v_cvt_pk_bf16_f32 v150, v150, v78\n"
      "v_cvt_pk_bf16_f32 v151, v151, v79\n"
      "ds_write_b128 v219, v[148:151] offset:16384\n"
      "s_add_u32 m0, s78, 90112\n"
      "s_add_u32 s96, s59, 590848\n"
      "buffer_load_dwordx4 v217, s[44:47], s96 offen lds\n"
      "v_pk_add_f32 v[152:153], v[152:153], v[236:237] op_sel_hi:[1,0]\n"
      "v_pk_add_f32 v[154:155], v[154:155], v[236:237] op_sel_hi:[1,0]\n"
      "v_pk_add_f32 v[80:81], v[80:81], v[236:237] op_sel_hi:[1,0]\n"
      "v_pk_add_f32 v[82:83], v[82:83], v[236:237] op_sel_hi:[1,0]\n"
      "v_pk_fma_f32 v[238:239], v[152:153], v[152:153], v[238:239]\n"
      "v_pk_fma_f32 v[228:229], v[154:155], v[154:155], v[228:229]\n"
      "v_pk_fma_f32 v[238:239], v[80:81], v[80:81], v[238:239]\n"
      "v_pk_fma_f32 v[228:229], v[82:83], v[82:83], v[228:229]\n"
      "v_cvt_pk_bf16_f32 v152, v152, v80\n"
      "v_cvt_pk_bf16_f32 v153, v153, v81\n"
      "v_cvt_pk_bf16_f32 v154, v154, v82\n"
      "v_cvt_pk_bf16_f32 v155, v155, v83\n"
      "ds_write_b128 v219, v[152:155] offset:24576\n"
      "buffer_store_dwordx4 v[16:19], v1, s[36:39], s90 offen sc1\n"
      "buffer_store_dwordx4 v[16:19], v1, s[36:39], s90 offen offset:1024 sc1\n"
      "v_pk_add_f32 v[172:173], v[172:173], v[236:237] op_sel_hi:[1,0]\n"
      "v_pk_add_f32 v[174:175], v[174:175], v[236:237] op_sel_hi:[1,0]\n"
      "v_pk_add_f32 v[100:101], v[100:101], v[236:237] op_sel_hi:[1,0]\n"
      "v_pk_add_f32 v[102:103], v[102:103], v[236:237] op_sel_hi:[1,0]\n"
      "v_pk_fma_f32 v[238:239], v[172:173], v[172:173], v[238:239]\n"
      "v_pk_fma_f32 v[228:229], v[174:175], v[174:175], v[228:229]\n"
      "v_pk_fma_f32 v[238:239], v[100:101], v[100:101], v[238:239]\n"
      "v_pk_fma_f32 v[228:229], v[102:103], v[102:103], v[228:229]\n"
      "v_cvt_pk_bf16_f32 v172, v172, v100\n"
      "v_cvt_pk_bf16_f32 v173, v173, v101\n"
      "v_cvt_pk_bf16_f32 v174, v174, v102\n"
      "v_cvt_pk_bf16_f32 v175, v175, v103\n"
      "ds_write_b128 v219, v[172:175] offset:32768\n"
      "buffer_store_dwordx4 v[16:19], v1, s[36:39], s90 offen offset:2048 sc1\n"
      "s_add_u32 s96, s35, 512\n"
      "v_pk_add_f32 v[156:157], v[156:157], v[236:237] op_sel_hi:[1,0]\n"
      "v_pk_add_f32 v[158:159], v[158:159], v[236:237] op_sel_hi:[1,0]\n"
      "v_pk_add_f32 v[84:85], v[84:85], v[236:237] op_sel_hi:[1,0]\n"
      "v_pk_add_f32 v[86:87], v[86:87], v[236:237] op_sel_hi:[1,0]\n"
      "v_pk_fma_f32 v[238:239], v[156:157], v[156:157], v[238:239]\n"
      "v_pk_fma_f32 v[228:229], v[158:159], v[158:159], v[228:229]\n"
      "v_pk_fma_f32 v[238:239], v[84:85], v[84:85], v[238:239]\n"
      "v_pk_fma_f32 v[228:229], v[86:87], v[86:87], v[228:229]\n"
      "v_cvt_pk_bf16_f32 v156, v156, v84\n"
      "v_cvt_pk_bf16_f32 v157, v157, v85\n"
      "v_cvt_pk_bf16_f32 v158, v158, v86\n"
      "v_cvt_pk_bf16_f32 v159, v159, v87\n"
      "buffer_load_dwordx4 a[0:3], v217, s[44:47], s96 offen\n"
      "s_add_u32 s96, s35, 74240\n"
      "v_pk_add_f32 v[160:161], v[160:161], v[236:237] op_sel_hi:[1,0]\n"
      "v_pk_add_f32 v[162:163], v[162:163], v[236:237] op_sel_hi:[1,0]\n"
      "v_pk_add_f32 v[88:89], v[88:89], v[236:237] op_sel_hi:[1,0]\n"
      "v_pk_add_f32 v[90:91], v[90:91], v[236:237] op_sel_hi:[1,0]\n"
      "v_pk_fma_f32 v[238:239], v[160:161], v[160:161], v[238:239]\n"
      "v_pk_fma_f32 v[228:229], v[162:163], v[162:163], v[228:229]\n"
      "v_pk_fma_f32 v[238:239], v[88:89], v[88:89], v[238:239]\n"
      "v_pk_fma_f32 v[228:229], v[90:91], v[90:91], v[228:229]\n"
      "v_cvt_pk_bf16_f32 v160, v160, v88\n"
      "v_cvt_pk_bf16_f32 v161, v161, v89\n"
      "v_cvt_pk_bf16_f32 v162, v162, v90\n"
      "v_cvt_pk_bf16_f32 v163, v163, v91\n"
      "buffer_load_dwordx4 a[4:7], v217, s[44:47], s96 offen\n"
      "s_add_u32 s96, s35, 147968\n"
      "v_pk_add_f32 v[164:165], v[164:165], v[236:237] op_sel_hi:[1,0]\n"
      "v_pk_add_f32 v[166:167], v[166:167], v[236:237] op_sel_hi:[1,0]\n"
      "v_pk_add_f32 v[92:93], v[92:93], v[236:237] op_sel_hi:[1,0]\n"
      "v_pk_add_f32 v[94:95], v[94:95], v[236:237] op_sel_hi:[1,0]\n"
      "v_pk_fma_f32 v[238:239], v[164:165], v[164:165], v[238:239]\n"
      "v_pk_fma_f32 v[228:229], v[166:167], v[166:167], v[228:229]\n"
      "v_pk_fma_f32 v[238:239], v[92:93], v[92:93], v[238:239]\n"
      "v_pk_fma_f32 v[228:229], v[94:95], v[94:95], v[228:229]\n"
      "v_cvt_pk_bf16_f32 v164, v164, v92\n"
      "v_cvt_pk_bf16_f32 v165, v165, v93\n"
      "v_cvt_pk_bf16_f32 v166, v166, v94\n"
      "v_cvt_pk_bf16_f32 v167, v167, v95\n"
      "buffer_load_dwordx4 a[8:11], v217, s[44:47], s96 offen\n"
      "s_add_u32 s96, s35, 221696\n"
      "v_pk_add_f32 v[168:169], v[168:169], v[236:237] op_sel_hi:[1,0]\n"
      "v_pk_add_f32 v[170:171], v[170:171], v[236:237] op_sel_hi:[1,0]\n"
      "v_pk_add_f32 v[96:97], v[96:97], v[236:237] op_sel_hi:[1,0]\n"
      "v_pk_add_f32 v[98:99], v[98:99], v[236:237] op_sel_hi:[1,0]\n"
      "v_pk_fma_f32 v[238:239], v[168:169], v[168:169], v[238:239]\n"
      "v_pk_fma_f32 v[228:229], v[170:171], v[170:171], v[228:229]\n"
      "v_pk_fma_f32 v[238:239], v[96:97], v[96:97], v[238:239]\n"
      "v_pk_fma_f32 v[228:229], v[98:99], v[98:99], v[228:229]\n"
      "v_cvt_pk_bf16_f32 v168, v168, v96\n"
      "v_cvt_pk_bf16_f32 v169, v169, v97\n"
      "v_cvt_pk_bf16_f32 v170, v170, v98\n"
      "v_cvt_pk_bf16_f32 v171, v171, v99\n"
      "buffer_load_dwordx4 a[12:15], v217, s[44:47], s96 offen\n"
      "s_add_u32 s96, s35, 295424\n"
      "v_pk_add_f32 v[238:239], v[238:239], v[228:229]\n"
      "v_add_f32 v238, v238, v239\n"
      "s_nop 1\n"
      "v_add_f32_dpp v238, v238, v238 quad_perm:[1,0,3,2] row_mask:0xf bank_mask:0xf\n"
      "s_nop 1\n"
      "v_add_f32_dpp v238, v238, v238 quad_perm:[2,3,0,1] row_mask:0xf bank_mask:0xf\n"
      "s_nop 1\n"
      "v_add_f32_dpp v238, v238, v238 row_half_mirror row_mask:0xf bank_mask:0xf\n"
      "v_mov_b32 v239, 0x3727c5ac\n"
      "v_fmac_f32 v239, 0x3ae38e39, v238\n"
      "v_rsq_f32 v238, v239\n"
      "s_nop 0\n"
      "v_xor_b32 v238, s9, v238\n"
      "ds_write_b32 v244, v238 offset:32\n"
      "s_waitcnt vmcnt(30)\n"
      "s_lshl_b32 s3, s76, 8\n"
      "v_readfirstlane_b32 s76, v230\n"
      "v_readfirstlane_b32 s77, v231\n"
      "s_nop 1\n"
      "s_add_u32 s76, s76, 1\n"
      "s_addc_u32 s77, s77, 0\n"
      "s_xor_b32 s77, s77, s3\n"
      "s_waitcnt vmcnt(21) lgkmcnt(0)\n"
      "s_barrier\n"
      "ds_read_b128 a[200:203], v222\n"
      "ds_read_b128 a[208:211], v224\n"
      "ds_read_b128 a[204:207], v222 offset:2048\n"
      "ds_read_b128 a[212:215], v224 offset:4096\n"
      "ds_read_b128 a[216:219], v224 offset:8192\n"
      "ds_read_b128 a[220:223], v224 offset:12288\n"
      "ds_read_b128 a[224:227], v224 offset:16384\n"
      "ds_read_b128 v[32:35], v245\n"
      "ds_read_b128 v[36:39], v245 offset:64\n"
      "s_waitcnt lgkmcnt(5)\n"
      "v_mfma_f32_16x16x32_bf16 v[176:179], a[200:203], a[208:211], 0\n"
      "ds_read_b128 a[228:231], v223\n"
      "buffer_load_dwordx4 a[16:19], v217, s[44:47], s96 offen\n"
      "v_mfma_f32_16x16x32_bf16 v[180:183], a[204:207], a[208:211], 0\n"
      "ds_read_b128 a[236:239], v225\n"
      "s_add_u32 s96, s35, 369152\n"
      "v_mfma_f32_16x16x32_bf16 v[184:187], a[200:203], a[212:215], 0\n"
      "ds_read_b128 a[232:235], v223 offset:2048\n"
      "buffer_load_dwordx4 a[20:23], v217, s[44:47], s96 offen\n"
      "s_waitcnt lgkmcnt(5)\n"
      "v_mfma_f32_16x16x32_bf16 v[188:191], a[204:207], a[212:215], 0\n"
      "ds_read_b128 a[240:243], v225 offset:4096\n"
      "s_add_u32 s96, s35, 442880\n"
      "v_mfma_f32_16x16x32_bf16 v[192:195], a[200:203], a[216:219], 0\n"
      "ds_read_b128 a[244:247], v225 offset:8192\n"
      "buffer_load_dwordx4 a[24:27], v217, s[44:47], s96 offen\n"
      "v_mfma_f32_16x16x32_bf16 v[196:199], a[204:207], a[216:219], 0\n"
      "ds_read_b128 a[248:251], v225 offset:12288\n"
      "s_add_u32 s96, s35, 516608\n"
      "v_mfma_f32_16x16x32_bf16 v[200:203], a[200:203], a[220:223], 0\n"
      "ds_read_b128 a[252:255], v225 offset:16384\n"
      "buffer_load_dwordx4 a[28:31], v217, s[44:47], s96 offen\n"
      "v_mfma_f32_16x16x32_bf16 v[204:207], a[204:207], a[220:223], 0\n"
      "s_add_u32 s96, s35, 590336\n"
      "buffer_load_dwordx4 a[32:35], v217, s[44:47], s96 offen\n"
      "v_mfma_f32_16x16x32_bf16 v[208:211], a[200:203], a[224:227], 0\n"
      "s_add_u32 s96, s35, 768\n"
      "buffer_load_dwordx4 a[36:39], v217, s[44:47], s96 offen\n"
      "v_mfma_f32_16x16x32_bf16 v[212:215], a[204:207], a[224:227], 0\n"
      "s_add_u32 s96, s35, 74496\n"
      "buffer_load_dwordx4 a[40:43], v217, s[44:47], s96 offen\n"
      "s_waitcnt lgkmcnt(3)\n"
      "v_mfma_f32_16x16x32_bf16 v[176:179], a[228:231], a[236:239], v[176:179]\n"
      "ds_read_b128 a[200:203], v222 offset:8192\n"
      "s_add_u32 s96, s35, 148224\n"
      "v_mfma_f32_16x16x32_bf16 v[180:183], a[232:235], a[236:239], v[180:183]\n"
      "ds_read_b128 a[208:211], v224 offset:2048\n"
      "buffer_load_dwordx4 a[44:47], v217, s[44:47], s96 offen\n"
      "v_mfma_f32_16x16x32_bf16 v[184:187], a[228:231], a[240:243], v[184:187]\n"
      "ds_read_b128 a[204:207], v222 offset:10240\n"
      "s_add_u32 s96, s35, 221952\n"
      "s_waitcnt lgkmcnt(3)\n"
      "v_mfma_f32_16x16x32_bf16 v[188:191], a[232:235], a[240:243], v[188:191]\n"
      "ds_read_b128 a[212:215], v224 offset:6144\n"
      "buffer_load_dwordx4 a[48:51], v217, s[44:47], s96 offen\n"
      "v_mfma_f32_16x16x32_bf16 v[192:195], a[228:231], a[244:247], v[192:195]\n"
      "ds_read_b128 a[216:219], v224 offset:10240\n"
      "s_add_u32 s96, s35, 295680\n"
      "v_mfma_f32_16x16x32_bf16 v[196:199], a[232:235], a[244:247], v[196:199]\n"
      "ds_read_b128 a[220:223], v224 offset:14336\n"
      "buffer_load_dwordx4 a[52:55], v217, s[44:47], s96 offen\n"
      "v_mfma_f32_16x16x32_bf16 v[200:203], a[228:231], a[248:251], v[200:203]\n"
      "ds_read_b128 a[224:227], v224 offset:18432\n"
      "s_add_u32 s96, s35, 369408\n"
      "v_mfma_f32_16x16x32_bf16 v[204:207], a[232:235], a[248:251], v[204:207]\n"
      "buffer_load_dwordx4 a[56:59], v217, s[44:47], s96 offen\n"
      "s_add_u32 s96, s35, 443136\n"
      "v_mfma_f32_16x16x32_bf16 v[208:211], a[228:231], a[252:255], v[208:211]\n"
      "buffer_load_dwordx4 a[60:63], v217, s[44:47], s96 offen\n"
      "s_add_u32 s96, s35, 516864\n"
      "v_mfma_f32_16x16x32_bf16 v[212:215], a[232:235], a[252:255], v[212:215]\n"
      "buffer_load_dwordx4 a[64:67], v217, s[44:47], s96 offen\n"
      "s_add_u32 s96, s35, 590592\n"
      "s_waitcnt lgkmcnt(3)\n"
      "v_mfma_f32_16x16x32_bf16 v[176:179], a[200:203], a[208:211], v[176:179]\n"
      "ds_read_b128 a[228:231], v223 offset:8192\n"
      "buffer_load_dwordx4 a[68:71], v217, s[44:47], s96 offen\n"
      "v_mfma_f32_16x16x32_bf16 v[180:183], a[204:207], a[208:211], v[180:183]\n"
      "ds_read_b128 a[236:239], v225 offset:2048\n"
      "buffer_load_dword v249, v246, s[48:51], s4 offen\n"
      "v_mfma_f32_16x16x32_bf16 v[184:187], a[200:203], a[212:215], v[184:187]\n"
      "ds_read_b128 a[232:235], v223 offset:10240\n"
      "buffer_load_dword v250, v254, s[48:51], s4 offen offset:1024\n"
      "s_waitcnt lgkmcnt(3)\n"
      "v_mfma_f32_16x16x32_bf16 v[188:191], a[204:207], a[212:215], v[188:191]\n"
      "ds_read_b128 a[240:243], v225 offset:6144\n"
      "v_mfma_f32_16x16x32_bf16 v[192:195], a[200:203], a[216:219], v[192:195]\n"
      "ds_read_b128 a[244:247], v225 offset:10240\n"
      "v_mfma_f32_16x16x32_bf16 v[196:199], a[204:207], a[216:219], v[196:199]\n"
      "ds_read_b128 a[248:251], v225 offset:14336\n"
      "v_mfma_f32_16x16x32_bf16 v[200:203], a[200:203], a[220:223], v[200:203]\n"
      "ds_read_b128 a[252:255], v225 offset:18432\n"
      "v_mfma_f32_16x16x32_bf16 v[204:207], a[204:207], a[220:223], v[204:207]\n"
      "v_mfma_f32_16x16x32_bf16 v[208:211], a[200:203], a[224:227], v[208:211]\n"
      "v_mfma_f32_16x16x32_bf16 v[212:215], a[204:207], a[224:227], v[212:215]\n"
      "s_waitcnt lgkmcnt(3)\n"
      "v_mfma_f32_16x16x32_bf16 v[176:179], a[228:231], a[236:239], v[176:179]\n"
      "v_mfma_f32_16x16x32_bf16 v[180:183], a[232:235], a[236:239], v[180:183]\n"
      "v_mfma_f32_16x16x32_bf16 v[184:187], a[228:231], a[240:243], v[184:187]\n"
      "s_waitcnt lgkmcnt(0)\n"
      "s_waitcnt vmcnt(28)\n"
      "s_barrier\n"
      "v_mfma_f32_16x16x32_bf16 v[188:191], a[232:235], a[240:243], v[188:191]\n"
      "ds_read_b128 a[200:203], v222 offset:16384\n"
      "v_mfma_f32_16x16x32_bf16 v[192:195], a[228:231], a[244:247], v[192:195]\n"
      "ds_read_b128 a[208:211], v224 offset:36864\n"
      "v_mfma_f32_16x16x32_bf16 v[196:199], a[232:235], a[244:247], v[196:199]\n"
      "ds_read_b128 a[204:207], v222 offset:18432\n"
      "v_mfma_f32_16x16x32_bf16 v[200:203], a[228:231], a[248:251], v[200:203]\n"
      "ds_read_b128 a[212:215], v224 offset:40960\n"
      "v_mfma_f32_16x16x32_bf16 v[204:207], a[232:235], a[248:251], v[204:207]\n"
      "ds_read_b128 a[216:219], v224 offset:45056\n"
      "v_mfma_f32_16x16x32_bf16 v[208:211], a[228:231], a[252:255], v[208:211]\n"
      "ds_read_b128 a[220:223], v224 offset:49152\n"
      "v_mfma_f32_16x16x32_bf16 v[212:215], a[232:235], a[252:255], v[212:215]\n"
      "ds_read_b128 a[224:227], v224 offset:53248\n"
      "s_waitcnt lgkmcnt(3)\n"
      "v_mfma_f32_16x16x32_bf16 v[176:179], a[200:203], a[208:211], v[176:179]\n"
      "ds_read_b128 a[228:231], v223 offset:16384\n"
      "v_mfma_f32_16x16x32_bf16 v[180:183], a[204:207], a[208:211], v[180:183]\n"
      "ds_read_b128 a[236:239], v225 offset:36864\n"
      "v_mfma_f32_16x16x32_bf16 v[184:187], a[200:203], a[212:215], v[184:187]\n"
      "ds_read_b128 a[232:235], v223 offset:18432\n"
      "s_waitcnt lgkmcnt(3)\n"
      "v_mfma_f32_16x16x32_bf16 v[188:191], a[204:207], a[212:215], v[188:191]\n"
      "ds_read_b128 a[240:243], v225 offset:40960\n"
      "v_mfma_f32_16x16x32_bf16 v[192:195], a[200:203], a[216:219], v[192:195]\n"
      "ds_read_b128 a[244:247], v225 offset:45056\n"
      "v_mfma_f32_16x16x32_bf16 v[196:199], a[204:207], a[216:219], v[196:199]\n"
      "ds_read_b128 a[248:251], v225 offset:49152\n"
      "v_mfma_f32_16x16x32_bf16 v[200:203], a[200:203], a[220:223], v[200:203]\n"
      "ds_read_b128 a[252:255], v225 offset:53248\n"
      "v_mfma_f32_16x16x32_bf16 v[204:207], a[204:207], a[220:223], v[204:207]\n"
      "s_waitcnt vmcnt(19)\n"
      "ds_write_b128 v247, a[0:3]\n"
      "v_mfma_f32_16x16x32_bf16 v[208:211], a[200:203], a[224:227], v[208:211]\n"
      "s_waitcnt vmcnt(18)\n"
      "ds_write_b128 v247, a[4:7] offset:4096\n"
      "v_mfma_f32_16x16x32_bf16 v[212:215], a[204:207], a[224:227], v[212:215]\n"
      "s_waitcnt vmcnt(17)\n"
      "ds_write_b128 v247, a[8:11] offset:8192\n"
      "s_waitcnt lgkmcnt(6)\n"
      "v_mfma_f32_16x16x32_bf16 v[176:179], a[228:231], a[236:239], v[176:179]\n"
      "ds_read_b128 a[200:203], v222 offset:24576\n"
      "v_mfma_f32_16x16x32_bf16 v[180:183], a[232:235], a[236:239], v[180:183]\n"
      "ds_read_b128 a[208:211], v224 offset:38912\n"
      "v_mfma_f32_16x16x32_bf16 v[184:187], a[228:231], a[240:243], v[184:187]\n"
      "ds_read_b128 a[204:207], v222 offset:26624\n"
      "s_waitcnt lgkmcnt(6)\n"
      "v_mfma_f32_16x16x32_bf16 v[188:191], a[232:235], a[240:243], v[188:191]\n"
      "ds_read_b128 a[212:215], v224 offset:43008\n"
      "v_mfma_f32_16x16x32_bf16 v[192:195], a[228:231], a[244:247], v[192:195]\n"
      "ds_read_b128 a[216:219], v224 offset:47104\n"
      "v_mfma_f32_16x16x32_bf16 v[196:199], a[232:235], a[244:247], v[196:199]\n"
      "ds_read_b128 a[220:223], v224 offset:51200\n"
      "v_mfma_f32_16x16x32_bf16 v[200:203], a[228:231], a[248:251], v[200:203]\n"
      "ds_read_b128 a[224:227], v224 offset:55296\n"
      "v_mfma_f32_16x16x32_bf16 v[204:207], a[232:235], a[248:251], v[204:207]\n"
      "s_waitcnt vmcnt(16)\n"
      "ds_write_b128 v247, a[12:15] offset:12288\n"
      "v_mfma_f32_16x16x32_bf16 v[208:211], a[228:231], a[252:255], v[208:211]\n"
      "s_waitcnt vmcnt(15)\n"
      "ds_write_b128 v247, a[16:19] offset:16384\n"
      "v_mfma_f32_16x16x32_bf16 v[212:215], a[232:235], a[252:255], v[212:215]\n"
      "s_waitcnt vmcnt(14)\n"
      "ds_write_b128 v247, a[20:23] offset:20480\n"
      "s_waitcnt lgkmcnt(6)\n"
      "v_mfma_f32_16x16x32_bf16 v[176:179], a[200:203], a[208:211], v[176:179]\n"
      "ds_read_b128 a[228:231], v223 offset:24576\n"
      "v_mfma_f32_16x16x32_bf16 v[180:183], a[204:207], a[208:211], v[180:183]\n"
      "ds_read_b128 a[236:239], v225 offset:38912\n"
      "v_mfma_f32_16x16x32_bf16 v[184:187], a[200:203], a[212:215], v[184:187]\n"
      "ds_read_b128 a[232:235], v223 offset:26624\n"
      "s_waitcnt lgkmcnt(6)\n"
      "v_mfma_f32_16x16x32_bf16 v[188:191], a[204:207], a[212:215], v[188:191]\n"
      "ds_read_b128 a[240:243], v225 offset:43008\n"
      "v_mfma_f32_16x16x32_bf16 v[192:195], a[200:203], a[216:219], v[192:195]\n"
      "ds_read_b128 a[244:247], v225 offset:47104\n"
      "v_mfma_f32_16x16x32_bf16 v[196:199], a[204:207], a[216:219], v[196:199]\n"
      "ds_read_b128 a[248:251], v225 offset:51200\n"
      "v_mfma_f32_16x16x32_bf16 v[200:203], a[200:203], a[220:223], v[200:203]\n"
      "ds_read_b128 a[252:255], v225 offset:55296\n"
      "v_mfma_f32_16x16x32_bf16 v[204:207], a[204:207], a[220:223], v[204:207]\n"
      "s_waitcnt vmcnt(13)\n"
      "ds_write_b128 v247, a[24:27] offset:24576\n"
      "v_mfma_f32_16x16x32_bf16 v[208:211], a[200:203], a[224:227], v[208:211]\n"
      "s_waitcnt vmcnt(12)\n"
      "ds_write_b128 v247, a[28:31] offset:28672\n"
      "v_mfma_f32_16x16x32_bf16 v[212:215], a[204:207], a[224:227], v[212:215]\n"
      "s_waitcnt vmcnt(11)\n"
      "ds_write_b128 v247, a[32:35] offset:32768\n"
      "s_waitcnt lgkmcnt(6)\n"
      "v_mfma_f32_16x16x32_bf16 v[176:179], a[228:231], a[236:239], v[176:179]\n"
      "ds_write_b128 v218, v[120:123]\n"
      "v_mfma_f32_16x16x32_bf16 v[180:183], a[232:235], a[236:239], v[180:183]\n"
      "ds_write_b128 v219, v[156:159]\n"
      "v_mfma_f32_16x16x32_bf16 v[184:187], a[228:231], a[240:243], v[184:187]\n"
      "ds_write_b128 v218, v[124:127] offset:8192\n"
      "s_waitcnt lgkmcnt(6)\n"
      "ds_write_b128 v219, v[160:163] offset:8192\n"
      "s_waitcnt lgkmcnt(0)\n"
      "s_barrier\n"
      "v_mfma_f32_16x16x32_bf16 v[188:191], a[232:235], a[240:243], v[188:191]\n"
      "ds_read_b128 a[200:203], v222 offset:32768\n"
      "v_mfma_f32_16x16x32_bf16 v[192:195], a[228:231], a[244:247], v[192:195]\n"
      "ds_read_b128 a[208:211], v226\n"
      "v_mfma_f32_16x16x32_bf16 v[196:199], a[232:235], a[244:247], v[196:199]\n"
      "ds_read_b128 a[204:207], v222 offset:34816\n"
      "v_mfma_f32_16x16x32_bf16 v[200:203], a[228:231], a[248:251], v[200:203]\n"
      "ds_read_b128 a[212:215], v226 offset:2048\n"
      "v_mfma_f32_16x16x32_bf16 v[204:207], a[232:235], a[248:251], v[204:207]\n"
      "ds_read_b128 a[216:219], v226 offset:4096\n"
      "v_mfma_f32_16x16x32_bf16 v[208:211], a[228:231], a[252:255], v[208:211]\n"
      "ds_read_b128 a[220:223], v226 offset:6144\n"
      "v_mfma_f32_16x16x32_bf16 v[212:215], a[232:235], a[252:255], v[212:215]\n"
      "ds_read_b128 a[224:227], v226 offset:8192\n"
      "s_waitcnt lgkmcnt(3)\n"
      "v_mfma_f32_16x16x32_bf16 v[176:179], a[200:203], a[208:211], v[176:179]\n"
      "ds_read_b128 a[228:231], v223 offset:32768\n"
      "v_mfma_f32_16x16x32_bf16 v[180:183], a[204:207], a[208:211], v[180:183]\n"
      "ds_read_b128 a[236:239], v227\n"
      "v_mfma_f32_16x16x32_bf16 v[184:187], a[200:203], a[212:215], v[184:187]\n"
      "ds_read_b128 a[232:235], v223 offset:34816\n"
      "s_waitcnt lgkmcnt(3)\n"
      "v_mfma_f32_16x16x32_bf16 v[188:191], a[204:207], a[212:215], v[188:191]\n"
      "ds_read_b128 a[240:243], v227 offset:2048\n"
      "v_mfma_f32_16x16x32_bf16 v[192:195], a[200:203], a[216:219], v[192:195]\n"
      "ds_read_b128 a[244:247], v227 offset:4096\n"
      "v_mfma_f32_16x16x32_bf16 v[196:199], a[204:207], a[216:219], v[196:199]\n"
      "ds_read_b128 a[248:251], v227 offset:6144\n"
      "v_mfma_f32_16x16x32_bf16 v[200:203], a[200:203], a[220:223], v[200:203]\n"
      "ds_read_b128 a[252:255], v227 offset:8192\n"
      "v_mfma_f32_16x16x32_bf16 v[204:207], a[204:207], a[220:223], v[204:207]\n"
      "s_waitcnt vmcnt(10)\n"
      "ds_write_b128 v248, a[36:39]\n"
      "v_mfma_f32_16x16x32_bf16 v[208:211], a[200:203], a[224:227], v[208:211]\n"
      "s_waitcnt vmcnt(9)\n"
      "ds_write_b128 v248, a[40:43] offset:4096\n"
      "v_mfma_f32_16x16x32_bf16 v[212:215], a[204:207], a[224:227], v[212:215]\n"
      "s_waitcnt vmcnt(8)\n"
      "ds_write_b128 v248, a[44:47] offset:8192\n"
      "s_waitcnt lgkmcnt(6)\n"
      "v_mfma_f32_16x16x32_bf16 v[176:179], a[228:231], a[236:239], v[176:179]\n"
      "ds_read_b128 a[200:203], v222\n"
      "v_mfma_f32_16x16x32_bf16 v[180:183], a[232:235], a[236:239], v[180:183]\n"
      "ds_read_b128 a[208:211], v224\n"
      "v_mfma_f32_16x16x32_bf16 v[184:187], a[228:231], a[240:243], v[184:187]\n"
      "ds_read_b128 a[204:207], v222 offset:2048\n"
      "s_waitcnt lgkmcnt(6)\n"
      "v_mfma_f32_16x16x32_bf16 v[188:191], a[232:235], a[240:243], v[188:191]\n"
      "ds_read_b128 a[212:215], v224 offset:4096\n"
      "v_mfma_f32_16x16x32_bf16 v[192:195], a[228:231], a[244:247], v[192:195]\n"
      "ds_read_b128 a[216:219], v224 offset:8192\n"
      "v_mfma_f32_16x16x32_bf16 v[196:199], a[232:235], a[244:247], v[196:199]\n"
      "ds_read_b128 a[220:223], v224 offset:12288\n"
      "v_mfma_f32_16x16x32_bf16 v[200:203], a[228:231], a[248:251], v[200:203]\n"
      "ds_read_b128 a[224:227], v224 offset:16384\n"
      "v_mfma_f32_16x16x32_bf16 v[204:207], a[232:235], a[248:251], v[204:207]\n"
      "s_waitcnt vmcnt(7)\n"
      "ds_write_b128 v248, a[48:51] offset:12288\n"
      "v_mfma_f32_16x16x32_bf16 v[208:211], a[228:231], a[252:255], v[208:211]\n"
      "s_waitcnt vmcnt(6)\n"
      "ds_write_b128 v248, a[52:55] offset:16384\n"
      "v_mfma_f32_16x16x32_bf16 v[212:215], a[232:235], a[252:255], v[212:215]\n"
      "s_waitcnt vmcnt(5)\n"
      "ds_write_b128 v248, a[56:59] offset:20480\n"
      "s_waitcnt lgkmcnt(6)\n"
      "v_mfma_f32_16x16x32_bf16 v[176:179], a[200:203], a[208:211], v[176:179]\n"
      "ds_read_b128 a[228:231], v223\n"
      "v_mfma_f32_16x16x32_bf16 v[180:183], a[204:207], a[208:211], v[180:183]\n"
      "ds_read_b128 a[236:239], v225\n"
      "v_mfma_f32_16x16x32_bf16 v[184:187], a[200:203], a[212:215], v[184:187]\n"
      "ds_read_b128 a[232:235], v223 offset:2048\n"
      "s_waitcnt lgkmcnt(6)\n"
      "v_mfma_f32_16x16x32_bf16 v[188:191], a[204:207], a[212:215], v[188:191]\n"
      "ds_read_b128 a[240:243], v225 offset:4096\n"
      "v_mfma_f32_16x16x32_bf16 v[192:195], a[200:203], a[216:219], v[192:195]\n"
      "ds_read_b128 a[244:247], v225 offset:8192\n"
      "v_mfma_f32_16x16x32_bf16 v[196:199], a[204:207], a[216:219], v[196:199]\n"
      "ds_read_b128 a[248:251], v225 offset:12288\n"
      "v_mfma_f32_16x16x32_bf16 v[200:203], a[200:203], a[220:223], v[200:203]\n"
      "ds_read_b128 a[252:255], v225 offset:16384\n"
      "v_mfma_f32_16x16x32_bf16 v[204:207], a[204:207], a[220:223], v[204:207]\n"
      "s_waitcnt vmcnt(4)\n"
      "ds_write_b128 v248, a[60:63] offset:24576\n"
      "v_mfma_f32_16x16x32_bf16 v[208:211], a[200:203], a[224:227], v[208:211]\n"
      "s_waitcnt vmcnt(3)\n"
      "ds_write_b128 v248, a[64:67] offset:28672\n"
      "v_mfma_f32_16x16x32_bf16 v[212:215], a[204:207], a[224:227], v[212:215]\n"
      "s_waitcnt vmcnt(2)\n"
      "ds_write_b128 v248, a[68:71] offset:32768\n"
      "s_waitcnt lgkmcnt(6)\n"
      "v_mfma_f32_16x16x32_bf16 v[176:179], a[228:231], a[236:239], v[176:179]\n"
      "ds_read_b128 a[200:203], v222 offset:8192\n"
      "v_mfma_f32_16x16x32_bf16 v[180:183], a[232:235], a[236:239], v[180:183]\n"
      "ds_read_b128 a[208:211], v224 offset:2048\n"
      "v_mfma_f32_16x16x32_bf16 v[184:187], a[228:231], a[240:243], v[184:187]\n"
      "ds_read_b128 a[204:207], v222 offset:10240\n"
      "s_waitcnt lgkmcnt(6)\n"
      "v_mfma_f32_16x16x32_bf16 v[188:191], a[232:235], a[240:243], v[188:191]\n"
      "ds_read_b128 a[212:215], v224 offset:6144\n"
      "v_mfma_f32_16x16x32_bf16 v[192:195], a[228:231], a[244:247], v[192:195]\n"
      "ds_read_b128 a[216:219], v224 offset:10240\n"
      "v_mfma_f32_16x16x32_bf16 v[196:199], a[232:235], a[244:247], v[196:199]\n"
      "ds_read_b128 a[220:223], v224 offset:14336\n"
      "v_mfma_f32_16x16x32_bf16 v[200:203], a[228:231], a[248:251], v[200:203]\n"
      "ds_read_b128 a[224:227], v224 offset:18432\n"
      "v_mfma_f32_16x16x32_bf16 v[204:207], a[232:235], a[248:251], v[204:207]\n"
      "ds_write_b128 v218, v[128:131] offset:16384\n"
      "v_mfma_f32_16x16x32_bf16 v[208:211], a[228:231], a[252:255], v[208:211]\n"
      "ds_write_b128 v219, v[164:167] offset:16384\n"
      "v_mfma_f32_16x16x32_bf16 v[212:215], a[232:235], a[252:255], v[212:215]\n"
      "ds_write_b128 v218, v[132:135] offset:24576\n"
      "s_waitcnt lgkmcnt(6)\n"
      "v_mfma_f32_16x16x32_bf16 v[176:179], a[200:203], a[208:211], v[176:179]\n"
      "ds_read_b128 a[228:231], v223 offset:8192\n"
      "v_mfma_f32_16x16x32_bf16 v[180:183], a[204:207], a[208:211], v[180:183]\n"
      "ds_read_b128 a[236:239], v225 offset:2048\n"
      "v_mfma_f32_16x16x32_bf16 v[184:187], a[200:203], a[212:215], v[184:187]\n"
      "ds_read_b128 a[232:235], v223 offset:10240\n"
      "s_waitcnt lgkmcnt(6)\n"
      "v_mfma_f32_16x16x32_bf16 v[188:191], a[204:207], a[212:215], v[188:191]\n"
      "ds_read_b128 a[240:243], v225 offset:6144\n"
      "v_mfma_f32_16x16x32_bf16 v[192:195], a[200:203], a[216:219], v[192:195]\n"
      "ds_read_b128 a[244:247], v225 offset:10240\n"
      "v_mfma_f32_16x16x32_bf16 v[196:199], a[204:207], a[216:219], v[196:199]\n"
      "ds_read_b128 a[248:251], v225 offset:14336\n"
      "v_mfma_f32_16x16x32_bf16 v[200:203], a[200:203], a[220:223], v[200:203]\n"
      "ds_read_b128 a[252:255], v225 offset:18432\n"
      "v_mfma_f32_16x16x32_bf16 v[204:207], a[204:207], a[220:223], v[204:207]\n"
      "ds_write_b128 v219, v[168:171] offset:24576\n"
      "v_mfma_f32_16x16x32_bf16 v[208:211], a[200:203], a[224:227], v[208:211]\n"
      "v_mfma_f32_16x16x32_bf16 v[212:215], a[204:207], a[224:227], v[212:215]\n"
      "s_waitcnt lgkmcnt(4)\n"
      "v_mfma_f32_16x16x32_bf16 v[176:179], a[228:231], a[236:239], v[176:179]\n"
      "v_mfma_f32_16x16x32_bf16 v[180:183], a[232:235], a[236:239], v[180:183]\n"
      "v_mfma_f32_16x16x32_bf16 v[184:187], a[228:231], a[240:243], v[184:187]\n"
      "Lpad1_%=:\n"
      "s_cbranch_execz Lret_%=\n"
      "s_waitcnt lgkmcnt(1)\n"
      "s_waitcnt lgkmcnt(0)\n"
      "s_barrier\n"
      "v_mfma_f32_16x16x32_bf16 v[188:191], a[232:235], a[240:243], v[188:191]\n"
      "ds_read_b128 a[200:203], v222 offset:16384\n"
      "v_mfma_f32_16x16x32_bf16 v[192:195], a[228:231], a[244:247], v[192:195]\n"
      "ds_read_b128 a[208:211], v224 offset:36864\n"
      "v_mfma_f32_16x16x32_bf16 v[196:199], a[232:235], a[244:247], v[196:199]\n"
      "ds_read_b128 a[204:207], v222 offset:18432\n"
      "v_mfma_f32_16x16x32_bf16 v[200:203], a[228:231], a[248:251], v[200:203]\n"
      "ds_read_b128 a[212:215], v224 offset:40960\n"
      "v_mfma_f32_16x16x32_bf16 v[204:207], a[232:235], a[248:251], v[204:207]\n"
      "ds_read_b128 a[216:219], v224 offset:45056\n"
      "v_mfma_f32_16x16x32_bf16 v[208:211], a[228:231], a[252:255], v[208:211]\n"
      "ds_read_b128 a[220:223], v224 offset:49152\n"
      "v_mfma_f32_16x16x32_bf16 v[212:215], a[232:235], a[252:255], v[212:215]\n"
      "ds_read_b128 a[224:227], v224 offset:53248\n"
      "s_waitcnt lgkmcnt(3)\n"
      "v_mfma_f32_16x16x32_bf16 v[176:179], a[200:203], a[208:211], v[176:179]\n"
      "ds_read_b128 a[228:231], v223 offset:16384\n"
      "v_mfma_f32_16x16x32_bf16 v[180:183], a[204:207], a[208:211], v[180:183]\n"
      "ds_read_b128 a[236:239], v225 offset:36864\n"
      "v_mfma_f32_16x16x32_bf16 v[184:187], a[200:203], a[212:215], v[184:187]\n"
      "ds_read_b128 a[232:235], v223 offset:18432\n"
      "s_waitcnt lgkmcnt(3)\n"
      "v_mfma_f32_16x16x32_bf16 v[188:191], a[204:207], a[212:215], v[188:191]\n"
      "ds_read_b128 a[240:243], v225 offset:40960\n"
      "v_mfma_f32_16x16x32_bf16 v[192:195], a[200:203], a[216:219], v[192:195]\n"
      "ds_read_b128 a[244:247], v225 offset:45056\n"
      "v_mfma_f32_16x16x32_bf16 v[196:199], a[204:207], a[216:219], v[196:199]\n"
      "ds_read_b128 a[248:251], v225 offset:49152\n"
      "v_mfma_f32_16x16x32_bf16 v[200:203], a[200:203], a[220:223], v[200:203]\n"
      "ds_read_b128 a[252:255], v225 offset:53248\n"
      "v_mfma_f32_16x16x32_bf16 v[204:207], a[204:207], a[220:223], v[204:207]\n"
      "v_mfma_f32_16x16x32_bf16 v[208:211], a[200:203], a[224:227], v[208:211]\n"
      "v_mfma_f32_16x16x32_bf16 v[212:215], a[204:207], a[224:227], v[212:215]\n"
      "s_waitcnt lgkmcnt(3)\n"
      "v_mfma_f32_16x16x32_bf16 v[176:179], a[228:231], a[236:239], v[176:179]\n"
      "ds_read_b128 a[200:203], v222 offset:24576\n"
      "v_mfma_f32_16x16x32_bf16 v[180:183], a[232:235], a[236:239], v[180:183]\n"
      "ds_read_b128 a[208:211], v224 offset:38912\n"
      "v_mfma_f32_16x16x32_bf16 v[184:187], a[228:231], a[240:243], v[184:187]\n"
      "ds_read_b128 a[204:207], v222 offset:26624\n"
      "s_waitcnt lgkmcnt(3)\n"
      "v_mfma_f32_16x16x32_bf16 v[188:191], a[232:235], a[240:243], v[188:191]\n"
      "ds_read_b128 a[212:215], v224 offset:43008\n"
      "v_mfma_f32_16x16x32_bf16 v[192:195], a[228:231], a[244:247], v[192:195]\n"
      "ds_read_b128 a[216:219], v224 offset:47104\n"
      "v_mfma_f32_16x16x32_bf16 v[196:199], a[232:235], a[244:247], v[196:199]\n"
      "ds_read_b128 a[220:223], v224 offset:51200\n"
      "v_mfma_f32_16x16x32_bf16 v[200:203], a[228:231], a[248:251], v[200:203]\n"
      "ds_read_b128 a[224:227], v224 offset:55296\n"
      "v_mfma_f32_16x16x32_bf16 v[204:207], a[232:235], a[248:251], v[204:207]\n"
      "v_mfma_f32_16x16x32_bf16 v[208:211], a[228:231], a[252:255], v[208:211]\n"
      "v_mfma_f32_16x16x32_bf16 v[212:215], a[232:235], a[252:255], v[212:215]\n"
      "s_waitcnt lgkmcnt(3)\n"
      "v_mfma_f32_16x16x32_bf16 v[176:179], a[200:203], a[208:211], v[176:179]\n"
      "ds_read_b128 a[228:231], v223 offset:24576\n"
      "v_mfma_f32_16x16x32_bf16 v[180:183], a[204:207], a[208:211], v[180:183]\n"
      "ds_read_b128 a[236:239], v225 offset:38912\n"
      "v_mfma_f32_16x16x32_bf16 v[184:187], a[200:203], a[212:215], v[184:187]\n"
      "ds_read_b128 a[232:235], v223 offset:26624\n"
      "s_waitcnt lgkmcnt(3)\n"
      "v_mfma_f32_16x16x32_bf16 v[188:191], a[204:207], a[212:215], v[188:191]\n"
      "ds_read_b128 a[240:243], v225 offset:43008\n"
      "v_mfma_f32_16x16x32_bf16 v[192:195], a[200:203], a[216:219], v[192:195]\n"
      "ds_read_b128 a[244:247], v225 offset:47104\n"
      "v_mfma_f32_16x16x32_bf16 v[196:199], a[204:207], a[216:219], v[196:199]\n"
      "ds_read_b128 a[248:251], v225 offset:51200\n"
      "v_mfma_f32_16x16x32_bf16 v[200:203], a[200:203], a[220:223], v[200:203]\n"
      "ds_read_b128 a[252:255], v225 offset:55296\n"
      "v_mfma_f32_16x16x32_bf16 v[204:207], a[204:207], a[220:223], v[204:207]\n"
      "v_mfma_f32_16x16x32_bf16 v[208:211], a[200:203], a[224:227], v[208:211]\n"
      "v_mfma_f32_16x16x32_bf16 v[212:215], a[204:207], a[224:227], v[212:215]\n"
      "s_waitcnt lgkmcnt(3)\n"
      "v_mfma_f32_16x16x32_bf16 v[176:179], a[228:231], a[236:239], v[176:179]\n"
      "v_mfma_f32_16x16x32_bf16 v[180:183], a[232:235], a[236:239], v[180:183]\n"
      "v_mfma_f32_16x16x32_bf16 v[184:187], a[228:231], a[240:243], v[184:187]\n"
      "s_waitcnt lgkmcnt(0)\n"
      "v_mfma_f32_16x16x32_bf16 v[188:191], a[232:235], a[240:243], v[188:191]\n"
      "v_mfma_f32_16x16x32_bf16 v[192:195], a[228:231], a[244:247], v[192:195]\n"
      "v_mfma_f32_16x16x32_bf16 v[196:199], a[232:235], a[244:247], v[196:199]\n"
      "v_mfma_f32_16x16x32_bf16 v[200:203], a[228:231], a[248:251], v[200:203]\n"
      "v_mfma_f32_16x16x32_bf16 v[204:207], a[232:235], a[248:251], v[204:207]\n"
      "v_mfma_f32_16x16x32_bf16 v[208:211], a[228:231], a[252:255], v[208:211]\n"
      "v_mfma_f32_16x16x32_bf16 v[212:215], a[232:235], a[252:255], v[212:215]\n"
      "s_barrier\n"
      "s_bitcmp1_b32 s72, 0\n"
      "s_cselect_b32 s90, s58, s57\n"
      "s_cselect_b32 s91, 32, 2\n"
      "v_pk_mul_f32 v[176:177], v[176:177], v[32:33]\n"
      "v_pk_mul_f32 v[178:179], v[178:179], v[34:35]\n"
      "v_pk_mul_f32 v[180:181], v[180:181], v[36:37]\n"
      "v_pk_mul_f32 v[182:183], v[182:183], v[38:39]\n"
      "v_cvt_pk_bf16_f32 v104, v176, v177\n"
      "v_cvt_pk_bf16_f32 v105, v178, v179\n"
      "v_cvt_pk_bf16_f32 v106, v180, v181\n"
      "v_cvt_pk_bf16_f32 v107, v182, v183\n"
      "s_add_u32 s92, s58, 0\n"
      "buffer_store_dwordx4 v[104:107], v1, s[36:39], s92 offen sc1\n"
      "v_pk_mul_f32 v[184:185], v[184:185], v[32:33]\n"
      "v_pk_mul_f32 v[186:187], v[186:187], v[34:35]\n"
      "v_pk_mul_f32 v[188:189], v[188:189], v[36:37]\n"
      "v_pk_mul_f32 v[190:191], v[190:191], v[38:39]\n"
      "v_cvt_pk_bf16_f32 v108, v184, v185\n"
      "v_cvt_pk_bf16_f32 v109, v186, v187\n"
      "v_cvt_pk_bf16_f32 v110, v188, v189\n"
      "v_cvt_pk_bf16_f32 v111, v190, v191\n"
      "s_add_u32 s92, s58, 32768\n"
      "buffer_store_dwordx4 v[108:111], v1, s[36:39], s92 offen sc1\n"
      "v_pk_mul_f32 v[192:193], v[192:193], v[32:33]\n"
      "v_pk_mul_f32 v[194:195], v[194:195], v[34:35]\n"
      "v_pk_mul_f32 v[196:197], v[196:197], v[36:37]\n"
      "v_pk_mul_f32 v[198:199], v[198:199], v[38:39]\n"
      "v_cvt_pk_bf16_f32 v112, v192, v193\n"
      "v_cvt_pk_bf16_f32 v113, v194, v195\n"
      "v_cvt_pk_bf16_f32 v114, v196, v197\n"
      "v_cvt_pk_bf16_f32 v115, v198, v199\n"
      "s_add_u32 s92, s58, 65536\n"
      "buffer_store_dwordx4 v[112:115], v1, s[36:39], s92 offen sc1\n"
      "v_pk_mul_f32 v[200:201], v[200:201], v[32:33]\n"
      "v_pk_mul_f32 v[202:203], v[202:203], v[34:35]\n"
      "v_pk_mul_f32 v[204:205], v[204:205], v[36:37]\n"
      "v_pk_mul_f32 v[206:207], v[206:207], v[38:39]\n"
      "v_cvt_pk_bf16_f32 v116, v200, v201\n"
      "v_cvt_pk_bf16_f32 v117, v202, v203\n"
      "v_cvt_pk_bf16_f32 v118, v204, v205\n"
      "v_cvt_pk_bf16_f32 v119, v206, v207\n"
      "s_add_u32 s92, s58, 98304\n"
      "buffer_store_dwordx4 v[116:119], v1, s[36:39], s92 offen sc1\n"
      "v_pk_mul_f32 v[208:209], v[208:209], v[32:33]\n"
      "v_pk_mul_f32 v[210:211], v[210:211], v[34:35]\n"
      "v_pk_mul_f32 v[212:213], v[212:213], v[36:37]\n"
      "v_pk_mul_f32 v[214:215], v[214:215], v[38:39]\n"
      "v_cvt_pk_bf16_f32 v120, v208, v209\n"
      "v_cvt_pk_bf16_f32 v121, v210, v211\n"
      "v_cvt_pk_bf16_f32 v122, v212, v213\n"
      "v_cvt_pk_bf16_f32 v123, v214, v215\n"
      "s_add_u32 s92, s58, 131072\n"
      "buffer_store_dwordx4 v[120:123], v1, s[36:39], s92 offen sc1\n"
      "buffer_load_dword v251, v255, s[48:51], s4 offen offset:1024\n"
      "s_add_u32 m0, s79, 139264\n"
      "s_add_u32 s96, s56, 0\n"
      "buffer_load_dwordx4 v240, s[48:51], s96 offen lds\n"
      "buffer_load_dwordx4 v241, s[48:51], s96 offen offset:1024 lds\n"
      "s_add_u32 m0, s79, 147456\n"
      "s_add_u32 s96, s56, 16384\n"
      "buffer_load_dwordx4 v240, s[48:51], s96 offen lds\n"
      "buffer_load_dwordx4 v241, s[48:51], s96 offen offset:1024 lds\n"
      "s_add_u32 m0, s79, 155648\n"
      "s_add_u32 s96, s56, 128\n"
      "buffer_load_dwordx4 v240, s[48:51], s96 offen lds\n"
      "buffer_load_dwordx4 v241, s[48:51], s96 offen offset:1024 lds\n"
      "s_add_u32 m0, s79, 114688\n"
      "s_add_u32 s96, s56, 16512\n"
      "buffer_load_dwordx4 v240, s[48:51], s96 offen lds\n"
      "buffer_load_dwordx4 v241, s[48:51], s96 offen offset:1024 lds\n"
      "s_add_u32 m0, s79, 122880\n"
      "s_add_u32 s96, s56, 256\n"
      "buffer_load_dwordx4 v240, s[48:51], s96 offen lds\n"
      "buffer_load_dwordx4 v241, s[48:51], s96 offen offset:1024 lds\n"
      "s_add_u32 m0, s79, 131072\n"
      "s_add_u32 s96, s56, 16640\n"
      "buffer_load_dwordx4 v240, s[48:51], s96 offen lds\n"
      "buffer_load_dwordx4 v241, s[48:51], s96 offen offset:1024 lds\n"
      "v_mov_b32 v74, s76\n"
      "v_mov_b32 v75, s77\n"
      "v_lshrrev_b32 v76, 4, v2\n"
      "v_lshlrev_b32 v76, 28, v76\n"
      "v_and_b32 v30, 15, v2\n"
      "v_lshl_add_u32 v76, v30, 3, v76\n"
      "s_mov_b64 s[44:45], s[36:37]\n"
      "s_cmp_eq_u32 s72, 0\n"
      "s_cselect_b32 s46, s38, 0\n"
      "s_lshl_b32 s7, s73, 7\n"
      "s_add_u32 s7, s7, 0x1000\n"
      "v_add_u32 v28, 65536, v1\n"
      "v_add_u32 v29, 131072, v1\n"
      "s_mul_i32 s90, s30, 1179648\n"
      "s_mul_i32 s97, s29, 294912\n"
      "s_add_u32 s90, s90, s97\n"
      "s_add_u32 s90, s90, 0x100000\n"
      "s_lshl_b32 s9, s74, 2\n"
      "s_bfe_u32 s97, s75, 0x20000\n"
      "s_add_u32 s9, s9, s97\n"
      "s_mul_i32 s91, s30, 1327104\n"
      "s_mul_i32 s97, s29, 331776\n"
      "s_add_u32 s91, s91, s97\n"
      "s_add_u32 s91, s91, 0x1d00000\n"
      "s_add_u32 s82, s90, s78\n"
      "s_add_u32 s86, s91, s78\n"
      "s_add_u32 s83, s90, s79\n"
      "s_add_u32 s87, s91, s79\n"
      "s_add_u32 s84, s90, s80\n"
      "s_add_u32 s88, s91, s80\n"
      "s_add_u32 s85, s90, s81\n"
      "s_add_u32 s89, s91, s81\n"
      "s_bfe_u32 s90, s30, 0x10000\n"
      "s_lshl_b32 s90, s90, 9\n"
      "s_bfe_u32 s97, s30, 0x10001\n"
      "s_lshl_b32 s97, s97, 7\n"
      "s_add_u32 s90, s90, s97\n"
      "s_lshl_b32 s97, s29, 10\n"
      "s_add_u32 s90, s90, s97\n"
      "s_add_u32 s90, s90, 0x1000\n"
      "v_and_b32 v72, 7, v2\n"
      "v_lshlrev_b32 v72, 12, v72\n"
      "v_bfe_u32 v64, v2, 3, 1\n"
      "v_lshl_add_u32 v72, v64, 8, v72\n"
      "v_add_u32 v72, s90, v72\n"
      "s_bfe_u32 s90, s28, 0x30000\n"
      "s_lshl_b32 s90, s90, 12\n"
      "s_bfe_u32 s97, s28, 0x10003\n"
      "s_lshl_b32 s97, s97, 7\n"
      "s_add_u32 s90, s90, s97\n"
      "s_add_u32 s90, s90, 0x1000\n"
      "v_and_b32 v68, 3, v2\n"
      "v_lshlrev_b32 v68, 10, v68\n"
      "v_bfe_u32 v64, v2, 2, 2\n"
      "v_lshl_add_u32 v68, v64, 8, v68\n"
      "v_add_u32 v68, s90, v68\n"
      "v_cmp_gt_u32 vcc, 16, v2\n"
      "s_nop 1\n"
      "v_cndmask_b32 v72, v68, v72, vcc\n"
      "s_getpc_b64 s[14:15]\n"
      "Lwh_4_%=:\n"
      "s_add_u32 s14, s14, Lwb_5_%=-Lwh_4_%=\n"
      "s_addc_u32 s15, s15, 0\n"
      "s_mov_b64 exec, 0\n"
      "s_cmp_eq_u32 s72, 0\n"
      "s_cbranch_scc1 Lpad2_%=\n"
      "s_cmp_eq_u32 s72, 1\n"
      "s_cbranch_scc1 Lpad3_%=\n"
      "s_cmp_eq_u32 s72, 2\n"
      "s_cbranch_scc1 Lpad4_%=\n"
      "s_branch Lpad5_%=\n"
      "Lwb_5_%=:\n"
      "s_mov_b64 exec, -1\n"
      "s_waitcnt vmcnt(13)\n"
      "s_barrier\n"
      "s_add_u32 s95, s7, 0x0\n"
      "buffer_store_dwordx2 v[74:75], v76, s[44:47], s95 offen sc1\n"
      "s_waitcnt vmcnt(11)\n"
      "ds_read_b128 a[0:3], v242 offset:24576\n"
      "ds_read_b128 a[8:11], v243 offset:24576\n"
      "s_waitcnt vmcnt(9)\n"
      "ds_read_b128 a[4:7], v242 offset:32768\n"
      "ds_read_b128 a[12:15], v243 offset:32768\n"
      "s_waitcnt lgkmcnt(2)\n"
      "s_add_u32 m0, s79, 139264\n"
      "s_add_u32 s96, s56, 384\n"
      "buffer_load_dwordx4 v240, s[48:51], s96 offen lds\n"
      "buffer_load_dwordx4 v241, s[48:51], s96 offen offset:1024 lds\n"
      "v_bfe_u32 v73, v2, 2, 2\n"
      "v_mul_u32_u24 v73, 36864, v73\n"
      "v_lshrrev_b32 v67, 5, v2\n"
      "v_lshl_add_u32 v73, v67, 7, v73\n"
      "v_bfe_u32 v67, v2, 4, 1\n"
      "v_lshl_add_u32 v73, v67, 6, v73\n"
      "v_and_b32 v67, 3, v2\n"
      "v_lshl_add_u32 v73, v67, 4, v73\n"
      "s_mul_i32 s33, s28, 589824\n"
      "s_mul_i32 s97, s72, 147456\n"
      "s_add_u32 s33, s33, s97\n"
      "s_mul_i32 s97, s30, 9216\n"
      "s_add_u32 s33, s33, s97\n"
      "s_lshl_b32 s97, s29, 8\n"
      "s_add_u32 s33, s33, s97\n"
      "s_add_u32 s33, s33, 0x600000\n"
      "v_lshrrev_b32 v9, 4, v2\n"
      "v_lshlrev_b32 v9, 10, v9\n"
      "v_bfe_u32 v64, v2, 2, 2\n"
      "v_lshl_add_u32 v9, v64, 7, v9\n"
      "v_and_b32 v64, 3, v2\n"
      "v_lshl_add_u32 v9, v64, 3, v9\n"
      "v_bfe_u32 v64, v2, 3, 1\n"
      "v_lshl_add_u32 v9, v64, 5, v9\n"
      "v_bfe_u32 v64, v2, 4, 1\n"
      "v_lshl_add_u32 v9, v64, 6, v9\n"
      "s_waitcnt vmcnt(9)\n"
      "ds_read_b128 a[16:19], v242 offset:40960\n"
      "ds_read_b128 a[24:27], v243 offset:40960\n"
      "s_waitcnt lgkmcnt(2)\n"
      "s_add_u32 m0, s79, 147456\n"
      "s_add_u32 s96, s56, 16768\n"
      "buffer_load_dwordx4 v240, s[48:51], s96 offen lds\n"
      "buffer_load_dwordx4 v241, s[48:51], s96 offen offset:1024 lds\n"
      "s_waitcnt vmcnt(9)\n"
      "ds_read_b128 a[20:23], v242\n"
      "ds_read_b128 a[28:31], v243\n"
      "s_waitcnt lgkmcnt(2)\n"
      "s_add_u32 m0, s79, 155648\n"
      "s_add_u32 s96, s56, 512\n"
      "buffer_load_dwordx4 v240, s[48:51], s96 offen lds\n"
      "buffer_load_dwordx4 v241, s[48:51], s96 offen offset:1024 lds\n"
      "s_waitcnt vmcnt(9)\n"
      "ds_read_b128 a[32:35], v242 offset:8192\n"
      "ds_read_b128 a[40:43], v243 offset:8192\n"
      "s_waitcnt lgkmcnt(2)\n"
      "s_add_u32 m0, s79, 114688\n"
      "s_add_u32 s96, s56, 16896\n"
      "buffer_load_dwordx4 v240, s[48:51], s96 offen lds\n"
      "buffer_load_dwordx4 v241, s[48:51], s96 offen offset:1024 lds\n"
      "s_getpc_b64 s[94:95]\n"
      "Lpc_6_%=:\n"
      "s_sub_u32 s94, s94, Lpc_6_%=-Lstart_%=\n"
      "s_subb_u32 s95, s95, 0\n"
      "v_and_b32 v199, 7, v0\n"
      "v_lshrrev_b32 v79, 6, v0\n"
      "v_lshl_add_u32 v79, v199, 7, v79\n"
      "s_lshr_b32 s97, s73, 3\n"
      "s_lshl_b32 s97, s97, 2\n"
      "v_add_u32 v79, s97, v79\n"
      "v_lshlrev_b32 v79, 7, v79\n"
      "v_min_u32 v79, Lend_%=-Lstart_%=, v79\n"
      "global_load_dword v199, v79, s[94:95]\n"
      "s_waitcnt vmcnt(10)\n"
      "ds_read_b128 a[36:39], v242 offset:16384\n"
      "ds_read_b128 a[44:47], v243 offset:16384\n"
      "s_waitcnt lgkmcnt(2)\n"
      "s_add_u32 m0, s79, 122880\n"
      "s_add_u32 s96, s56, 640\n"
      "buffer_load_dwordx4 v240, s[48:51], s96 offen lds\n"
      "buffer_load_dwordx4 v241, s[48:51], s96 offen offset:1024 lds\n"
      "s_waitcnt vmcnt(9)\n"
      "ds_read_b128 a[48:51], v242 offset:24576\n"
      "ds_read_b128 a[56:59], v243 offset:24576\n"
      "s_waitcnt lgkmcnt(2)\n"
      "s_add_u32 m0, s79, 131072\n"
      "s_add_u32 s96, s56, 17024\n"
      "buffer_load_dwordx4 v240, s[48:51], s96 offen lds\n"
      "buffer_load_dwordx4 v241, s[48:51], s96 offen offset:1024 lds\n"
      "s_waitcnt vmcnt(9)\n"
      "ds_read_b128 a[52:55], v242 offset:32768\n"
      "ds_read_b128 a[60:63], v243 offset:32768\n"
      "s_waitcnt lgkmcnt(2)\n"
      "s_add_u32 m0, s79, 139264\n"
      "s_add_u32 s96, s56, 768\n"
      "buffer_load_dwordx4 v240, s[48:51], s96 offen lds\n"
      "buffer_load_dwordx4 v241, s[48:51], s96 offen offset:1024 lds\n"
      "s_waitcnt vmcnt(9)\n"
      "ds_read_b128 a[64:67], v242 offset:40960\n"
      "ds_read_b128 a[72:75], v243 offset:40960\n"
      "s_waitcnt lgkmcnt(2)\n"
      "s_add_u32 m0, s79, 147456\n"
      "s_add_u32 s96, s56, 17152\n"
      "buffer_load_dwordx4 v240, s[48:51], s96 offen lds\n"
      "buffer_load_dwordx4 v241, s[48:51], s96 offen offset:1024 lds\n"
      "s_waitcnt vmcnt(9)\n"
      "ds_read_b128 a[68:71], v242\n"
      "ds_read_b128 a[76:79], v243\n"
      "s_waitcnt lgkmcnt(2)\n"
      "s_add_u32 m0, s79, 155648\n"
      "s_add_u32 s96, s56, 896\n"
      "buffer_load_dwordx4 v240, s[48:51], s96 offen lds\n"
      "buffer_load_dwordx4 v241, s[48:51], s96 offen offset:1024 lds\n"
      "s_waitcnt vmcnt(8)\n"
      "ds_read_b128 a[80:83], v242 offset:8192\n"
      "ds_read_b128 a[88:91], v243 offset:8192\n"
      "s_waitcnt lgkmcnt(2)\n"
      "s_add_u32 m0, s79, 114688\n"
      "s_add_u32 s96, s56, 17280\n"
      "buffer_load_dwordx4 v240, s[48:51], s96 offen lds\n"
      "buffer_load_dwordx4 v241, s[48:51], s96 offen offset:1024 lds\n"
      "s_waitcnt vmcnt(8)\n"
      "ds_read_b128 a[84:87], v242 offset:16384\n"
      "ds_read_b128 a[92:95], v243 offset:16384\n"
      "s_waitcnt lgkmcnt(2)\n"
      "s_add_u32 m0, s79, 122880\n"
      "s_add_u32 s96, s56, 1024\n"
      "buffer_load_dwordx4 v240, s[48:51], s96 offen lds\n"
      "buffer_load_dwordx4 v241, s[48:51], s96 offen offset:1024 lds\n"
      "s_waitcnt vmcnt(8)\n"
      "ds_read_b128 a[96:99], v242 offset:24576\n"
      "ds_read_b128 a[104:107], v243 offset:24576\n"
      "s_waitcnt lgkmcnt(2)\n"
      "s_add_u32 m0, s79, 131072\n"
      "s_add_u32 s96, s56, 17408\n"
      "buffer_load_dwordx4 v240, s[48:51], s96 offen lds\n"
      "buffer_load_dwordx4 v241, s[48:51], s96 offen offset:1024 lds\n"
      "s_waitcnt vmcnt(8)\n"
      "ds_read_b128 a[100:103], v242 offset:32768\n"
      "ds_read_b128 a[108:111], v243 offset:32768\n"
      "s_waitcnt lgkmcnt(2)\n"
      "s_add_u32 m0, s79, 139264\n"
      "s_add_u32 s96, s56, 1152\n"
      "buffer_load_dwordx4 v240, s[48:51], s96 offen lds\n"
      "buffer_load_dwordx4 v241, s[48:51], s96 offen offset:1024 lds\n"
      "s_waitcnt vmcnt(8)\n"
      "ds_read_b128 a[112:115], v242 offset:40960\n"
      "ds_read_b128 a[120:123], v243 offset:40960\n"
      "s_waitcnt lgkmcnt(2)\n"
      "s_add_u32 m0, s79, 147456\n"
      "s_add_u32 s96, s56, 17536\n"
      "buffer_load_dwordx4 v240, s[48:51], s96 offen lds\n"
      "buffer_load_dwordx4 v241, s[48:51], s96 offen offset:1024 lds\n"
      "s_waitcnt vmcnt(8)\n"
      "ds_read_b128 a[116:119], v242\n"
      "ds_read_b128 a[124:127], v243\n"
      "s_waitcnt lgkmcnt(2)\n"
      "s_add_u32 m0, s79, 155648\n"
      "s_add_u32 s96, s56, 1280\n"
      "buffer_load_dwordx4 v240, s[48:51], s96 offen lds\n"
      "buffer_load_dwordx4 v241, s[48:51], s96 offen offset:1024 lds\n"
      "s_waitcnt vmcnt(8)\n"
      "ds_read_b128 a[128:131], v242 offset:8192\n"
      "ds_read_b128 a[136:139], v243 offset:8192\n"
      "s_waitcnt lgkmcnt(2)\n"
      "s_add_u32 m0, s79, 114688\n"
      "s_add_u32 s96, s56, 17664\n"
      "buffer_load_dwordx4 v240, s[48:51], s96 offen lds\n"
      "buffer_load_dwordx4 v241, s[48:51], s96 offen offset:1024 lds\n"
      "s_waitcnt vmcnt(8)\n"
      "ds_read_b128 a[132:135], v242 offset:16384\n"
      "ds_read_b128 a[140:143], v243 offset:16384\n"
      "s_waitcnt lgkmcnt(2)\n"
      "s_add_u32 m0, s79, 122880\n"
      "s_add_u32 s96, s56, 1408\n"
      "buffer_load_dwordx4 v240, s[48:51], s96 offen lds\n"
      "buffer_load_dwordx4 v241, s[48:51], s96 offen offset:1024 lds\n"
      "s_waitcnt vmcnt(8)\n"
      "ds_read_b128 a[144:147], v242 offset:24576\n"
      "ds_read_b128 a[152:155], v243 offset:24576\n"
      "s_waitcnt lgkmcnt(2)\n"
      "s_add_u32 m0, s79, 131072\n"
      "s_add_u32 s96, s56, 17792\n"
      "buffer_load_dwordx4 v240, s[48:51], s96 offen lds\n"
      "buffer_load_dwordx4 v241, s[48:51], s96 offen offset:1024 lds\n"
      "s_waitcnt vmcnt(8)\n"
      "ds_read_b128 a[148:151], v242 offset:32768\n"
      "ds_read_b128 a[156:159], v243 offset:32768\n"
      "s_waitcnt lgkmcnt(2)\n"
      "s_add_u32 m0, s79, 139264\n"
      "s_add_u32 s96, s56, 1536\n"
      "buffer_load_dwordx4 v240, s[48:51], s96 offen lds\n"
      "buffer_load_dwordx4 v241, s[48:51], s96 offen offset:1024 lds\n"
      "s_waitcnt vmcnt(8)\n"
      "ds_read_b128 a[160:163], v242 offset:40960\n"
      "ds_read_b128 a[168:171], v243 offset:40960\n"
      "s_waitcnt lgkmcnt(2)\n"
      "s_add_u32 m0, s79, 147456\n"
      "s_add_u32 s96, s56, 17920\n"
      "buffer_load_dwordx4 v240, s[48:51], s96 offen lds\n"
      "buffer_load_dwordx4 v241, s[48:51], s96 offen offset:1024 lds\n"
      "s_waitcnt vmcnt(8)\n"
      "ds_read_b128 a[164:167], v242\n"
      "ds_read_b128 a[172:175], v243\n"
      "s_waitcnt lgkmcnt(2)\n"
      "s_add_u32 m0, s79, 155648\n"
      "s_add_u32 s96, s56, 1664\n"
      "buffer_load_dwordx4 v240, s[48:51], s96 offen lds\n"
      "buffer_load_dwordx4 v241, s[48:51], s96 offen offset:1024 lds\n"
      "s_waitcnt vmcnt(8)\n"
      "ds_read_b128 a[176:179], v242 offset:8192\n"
      "ds_read_b128 a[184:187], v243 offset:8192\n"
      "s_waitcnt lgkmcnt(2)\n"
      "s_add_u32 m0, s79, 114688\n"
      "s_add_u32 s96, s56, 18048\n"
      "buffer_load_dwordx4 v240, s[48:51], s96 offen lds\n"
      "buffer_load_dwordx4 v241, s[48:51], s96 offen offset:1024 lds\n"
      "buffer_load_dwordx2 v[70:71], v72, s[36:39], 0 offen sc1\n"
      "s_waitcnt vmcnt(9)\n"
      "ds_read_b128 a[180:183], v242 offset:16384\n"
      "ds_read_b128 a[188:191], v243 offset:16384\n"
      "s_waitcnt lgkmcnt(2)\n"
      "s_add_u32 m0, s79, 122880\n"
      "s_add_u32 s96, s56, 1792\n"
      "buffer_load_dwordx4 v240, s[48:51], s96 offen lds\n"
      "buffer_load_dwordx4 v241, s[48:51], s96 offen offset:1024 lds\n"
      "s_waitcnt vmcnt(9)\n"
      "ds_read_b128 a[192:195], v242 offset:24576\n"
      "ds_read_b128 a[200:203], v243 offset:24576\n"
      "s_waitcnt lgkmcnt(2)\n"
      "s_add_u32 m0, s79, 131072\n"
      "s_add_u32 s96, s56, 18176\n"
      "buffer_load_dwordx4 v240, s[48:51], s96 offen lds\n"
      "buffer_load_dwordx4 v241, s[48:51], s96 offen offset:1024 lds\n"
      "s_waitcnt vmcnt(9)\n"
      "ds_read_b128 a[196:199], v242 offset:32768\n"
      "ds_read_b128 a[204:207], v243 offset:32768\n"
      "s_waitcnt lgkmcnt(2)\n"
      "s_add_u32 m0, s79, 139264\n"
      "s_add_u32 s96, s56, 1920\n"
      "buffer_load_dwordx4 v240, s[48:51], s96 offen lds\n"
      "buffer_load_dwordx4 v241, s[48:51], s96 offen offset:1024 lds\n"
      "s_waitcnt vmcnt(9)\n"
      "ds_read_b128 a[208:211], v242 offset:40960\n"
      "ds_read_b128 a[216:219], v243 offset:40960\n"
      "s_waitcnt lgkmcnt(2)\n"
      "s_add_u32 m0, s79, 147456\n"
      "s_add_u32 s96, s56, 18304\n"
      "buffer_load_dwordx4 v240, s[48:51], s96 offen lds\n"
      "buffer_load_dwordx4 v241, s[48:51], s96 offen offset:1024 lds\n"
      "s_waitcnt vmcnt(8)\n"
      "v_cmp_ne_u64 vcc, s[76:77], v[70:71]\n"
      "s_cbranch_vccz Lib_7_%=\n"
      "s_mov_b32 s8, 0\n"
      "Lpoll_8_%=:\n"
      "buffer_load_dwordx2 v[70:71], v72, s[36:39], 0 offen sc1\n"
      "s_add_u32 s8, s8, 1\n"
      "s_waitcnt vmcnt(0) lgkmcnt(0)\n"
      "v_cmp_ne_u64 vcc, s[76:77], v[70:71]\n"
      "s_cbranch_vccz Lpolld_9_%=\n"
      "s_cmp_lt_u32 s8, 40000\n"
      "s_cbranch_scc1 Lpoll_8_%=\n"
      "Lpolld_9_%=:\n"
      "Lib_7_%=:\n"
      "s_add_u32 m0, s79, 0\n"
      "s_add_u32 s96, s83, 0\n"
      "buffer_load_dwordx4 v1, s[36:39], s96 offen sc1 lds\n"
      "buffer_load_dwordx4 v1, s[36:39], s96 offen offset:1024 sc1 lds\n"
      "s_add_u32 m0, s79, 8192\n"
      "s_add_u32 s96, s83, 8192\n"
      "buffer_load_dwordx4 v1, s[36:39], s96 offen sc1 lds\n"
      "buffer_load_dwordx4 v1, s[36:39], s96 offen offset:1024 sc1 lds\n"
      "s_add_u32 m0, s79, 16384\n"
      "s_add_u32 s96, s83, 16384\n"
      "buffer_load_dwordx4 v1, s[36:39], s96 offen sc1 lds\n"
      "buffer_load_dwordx4 v1, s[36:39], s96 offen offset:1024 sc1 lds\n"
      "s_add_u32 m0, s79, 24576\n"
      "s_add_u32 s96, s83, 24576\n"
      "buffer_load_dwordx4 v1, s[36:39], s96 offen sc1 lds\n"
      "buffer_load_dwordx4 v1, s[36:39], s96 offen offset:1024 sc1 lds\n"
      "ds_read_b128 a[212:215], v242\n"
      "ds_read_b128 a[220:223], v243\n"
      "s_waitcnt vmcnt(14)\n"
      "ds_read_b128 a[224:227], v242 offset:8192\n"
      "ds_read_b128 a[232:235], v243 offset:8192\n"
      "s_waitcnt vmcnt(12)\n"
      "ds_read_b128 a[228:231], v242 offset:16384\n"
      "ds_read_b128 a[236:239], v243 offset:16384\n"
      "s_waitcnt vmcnt(10)\n"
      "ds_read_b128 a[240:243], v242 offset:24576\n"
      "ds_read_b128 a[248:251], v243 offset:24576\n"
      "s_waitcnt vmcnt(8)\n"
      "ds_read_b128 a[244:247], v242 offset:32768\n"
      "ds_read_b128 a[252:255], v243 offset:32768\n"
      "s_waitcnt lgkmcnt(0)\n"
      "s_waitcnt vmcnt(4)\n"
      "s_barrier\n"
      "ds_read_b128 v[32:35], v1\n"
      "ds_read_b128 v[36:39], v1 offset:1024\n"
      "ds_read_b128 v[40:43], v1 offset:2048\n"
      "ds_read_b128 v[44:47], v1 offset:3072\n"
      "ds_read_b128 v[48:51], v1 offset:4096\n"
      "ds_read_b128 v[52:55], v1 offset:5120\n"
      "s_waitcnt lgkmcnt(4)\n"
      "v_mfma_f32_16x16x32_bf16 v[128:131], v[32:35], a[0:3], 0\n"
      "ds_read_b128 v[56:59], v1 offset:6144\n"
      "v_mfma_f32_16x16x32_bf16 v[128:131], v[36:39], a[8:11], v[128:131]\n"
      "s_add_u32 m0, s81, 32768\n"
      "s_add_u32 s96, s85, 32768\n"
      "v_mfma_f32_16x16x32_bf16 v[132:135], v[32:35], a[4:7], 0\n"
      "ds_read_b128 v[60:63], v1 offset:7168\n"
      "buffer_load_dwordx4 v1, s[36:39], s96 offen sc1 lds\n"
      "v_mfma_f32_16x16x32_bf16 v[132:135], v[36:39], a[12:15], v[132:135]\n"
      "s_waitcnt lgkmcnt(4)\n"
      "v_mfma_f32_16x16x32_bf16 v[128:131], v[40:43], a[16:19], v[128:131]\n"
      "ds_read_b128 v[32:35], v1 offset:8192\n"
      "buffer_load_dwordx4 v1, s[36:39], s96 offen offset:1024 sc1 lds\n"
      "v_mfma_f32_16x16x32_bf16 v[128:131], v[44:47], a[24:27], v[128:131]\n"
      "s_cmp_lt_u32 s73, 216\n"
      "s_cselect_b32 s95, -1, 0\n"
      "v_mfma_f32_16x16x32_bf16 v[132:135], v[40:43], a[20:23], v[132:135]\n"
      "ds_read_b128 v[36:39], v1 offset:9216\n"
      "v_mfma_f32_16x16x32_bf16 v[132:135], v[44:47], a[28:31], v[132:135]\n"
      "s_waitcnt lgkmcnt(4)\n"
      "v_mfma_f32_16x16x32_bf16 v[128:131], v[48:51], a[32:35], v[128:131]\n"
      "ds_read_b128 v[40:43], v1 offset:10240\n"
      "buffer_load_dwordx4 v1, s[36:39], s96 offen offset:2048 sc1 lds\n"
      "v_mfma_f32_16x16x32_bf16 v[128:131], v[52:55], a[40:43], v[128:131]\n"
      "s_bitcmp1_b32 s72, 0\n"
      "s_cselect_b32 s52, s20, s18\n"
      "s_cselect_b32 s53, s21, s19\n"
      "v_mfma_f32_16x16x32_bf16 v[132:135], v[48:51], a[36:39], v[132:135]\n"
      "ds_read_b128 v[44:47], v1 offset:11264\n"
      "s_and_b32 s53, s53, 0xffff\n"
      "v_mfma_f32_16x16x32_bf16 v[132:135], v[52:55], a[44:47], v[132:135]\n"
      "s_waitcnt lgkmcnt(4)\n"
      "s_mov_b32 s54, 0x510000\n"
      "v_mfma_f32_16x16x32_bf16 v[128:131], v[56:59], a[48:51], v[128:131]\n"
      "ds_read_b128 v[48:51], v1 offset:12288\n"
      "buffer_load_dwordx4 v1, s[36:39], s96 offen offset:3072 sc1 lds\n"
      "v_mfma_f32_16x16x32_bf16 v[128:131], v[60:63], a[56:59], v[128:131]\n"
      "s_mov_b32 s55, 0x20000\n"
      "s_mov_b32 s60, s22\n"
      "v_mfma_f32_16x16x32_bf16 v[132:135], v[56:59], a[52:55], v[132:135]\n"
      "ds_read_b128 v[52:55], v1 offset:13312\n"
      "s_and_b32 s61, s23, 0xffff\n"
      "v_mfma_f32_16x16x32_bf16 v[132:135], v[60:63], a[60:63], v[132:135]\n"
      "s_waitcnt lgkmcnt(4)\n"
      "s_mov_b32 s62, 0x510000\n"
      "v_mfma_f32_16x16x32_bf16 v[128:131], v[32:35], a[64:67], v[128:131]\n"
      "ds_read_b128 v[56:59], v1 offset:14336\n"
      "s_mov_b32 s63, 0x20000\n"
      "v_mfma_f32_16x16x32_bf16 v[128:131], v[36:39], a[72:75], v[128:131]\n"
      "s_add_u32 m0, s81, 49152\n"
      "s_add_u32 s96, s85, 49152\n"
      "v_mfma_f32_16x16x32_bf16 v[132:135], v[32:35], a[68:71], v[132:135]\n"
      "ds_read_b128 v[60:63], v1 offset:15360\n"
      "buffer_load_dwordx4 v1, s[36:39], s96 offen sc1 lds\n"
      "Lpad2_%=:\n"
      "s_cbranch_execz Lret_%=\n"
      "v_mfma_f32_16x16x32_bf16 v[132:135], v[36:39], a[76:79], v[132:135]\n"
      "s_waitcnt lgkmcnt(4)\n"
      "s_mov_b32 s68, s26\n"
      "v_mfma_f32_16x16x32_bf16 v[128:131], v[40:43], a[80:83], v[128:131]\n"
      "s_waitcnt vmcnt(5)\n"
      "s_barrier\n"
      "ds_read_b128 v[32:35], v1 offset:16384\n"
      "v_mfma_f32_16x16x32_bf16 v[128:131], v[44:47], a[88:91], v[128:131]\n"
      "buffer_load_dwordx4 v1, s[36:39], s96 offen offset:1024 sc1 lds\n"
      "s_and_b32 s69, s27, 0xffff\n"
      "v_mfma_f32_16x16x32_bf16 v[132:135], v[40:43], a[84:87], v[132:135]\n"
      "ds_read_b128 v[36:39], v1 offset:17408\n"
      "s_mov_b32 s70, 0x4000000\n"
      "v_mfma_f32_16x16x32_bf16 v[132:135], v[44:47], a[92:95], v[132:135]\n"
      "s_waitcnt lgkmcnt(4)\n"
      "s_mov_b32 s71, 0x20000\n"
      "v_mfma_f32_16x16x32_bf16 v[128:131], v[48:51], a[96:99], v[128:131]\n"
      "ds_read_b128 v[40:43], v1 offset:18432\n"
      "buffer_load_dwordx4 v1, s[36:39], s96 offen offset:2048 sc1 lds\n"
      "v_mfma_f32_16x16x32_bf16 v[128:131], v[52:55], a[104:107], v[128:131]\n"
      "s_and_b32 s62, s62, s95\n"
      "s_and_b32 s70, s70, s95\n"
      "v_mfma_f32_16x16x32_bf16 v[132:135], v[48:51], a[100:103], v[132:135]\n"
      "ds_read_b128 v[44:47], v1 offset:19456\n"
      "s_bfe_u32 s10, s9, 0x30002\n"
      "v_mfma_f32_16x16x32_bf16 v[132:135], v[52:55], a[108:111], v[132:135]\n"
      "s_waitcnt lgkmcnt(4)\n"
      "s_mul_i32 s10, s10, 1152\n"
      "v_mfma_f32_16x16x32_bf16 v[128:131], v[56:59], a[112:115], v[128:131]\n"
      "ds_read_b128 v[48:51], v1 offset:20480\n"
      "buffer_load_dwordx4 v1, s[36:39], s96 offen offset:3072 sc1 lds\n"
      "v_mfma_f32_16x16x32_bf16 v[128:131], v[60:63], a[120:123], v[128:131]\n"
      "s_bfe_u32 s97, s9, 0x20000\n"
      "s_lshl_b32 s97, s97, 7\n"
      "v_mfma_f32_16x16x32_bf16 v[132:135], v[56:59], a[116:119], v[132:135]\n"
      "ds_read_b128 v[52:55], v1 offset:21504\n"
      "s_add_u32 s10, s10, s97\n"
      "v_mfma_f32_16x16x32_bf16 v[132:135], v[60:63], a[124:127], v[132:135]\n"
      "s_waitcnt lgkmcnt(4)\n"
      "s_bfe_u32 s97, s72, 0x10001\n"
      "v_mfma_f32_16x16x32_bf16 v[128:131], v[32:35], a[128:131], v[128:131]\n"
      "ds_read_b128 v[56:59], v1 offset:22528\n"
      "s_mul_i32 s97, s97, 294912\n"
      "v_mfma_f32_16x16x32_bf16 v[128:131], v[36:39], a[136:139], v[128:131]\n"
      "s_add_u32 m0, s81, 65536\n"
      "s_add_u32 s96, s85, 65536\n"
      "v_mfma_f32_16x16x32_bf16 v[132:135], v[32:35], a[132:135], v[132:135]\n"
      "ds_read_b128 v[60:63], v1 offset:23552\n"
      "buffer_load_dwordx4 v1, s[36:39], s96 offen sc1 lds\n"
      "v_mfma_f32_16x16x32_bf16 v[132:135], v[36:39], a[140:143], v[132:135]\n"
      "s_waitcnt lgkmcnt(4)\n"
      "s_add_u32 s10, s10, s97\n"
      "v_mfma_f32_16x16x32_bf16 v[128:131], v[40:43], a[144:147], v[128:131]\n"
      "ds_read_b128 v[32:35], v1 offset:24576\n"
      "buffer_load_dwordx4 v1, s[36:39], s96 offen offset:1024 sc1 lds\n"
      "v_mfma_f32_16x16x32_bf16 v[128:131], v[44:47], a[152:155], v[128:131]\n"
      "s_mul_i32 s11, s74, 1152\n"
      "s_mul_i32 s97, s75, 294912\n"
      "v_mfma_f32_16x16x32_bf16 v[132:135], v[40:43], a[148:151], v[132:135]\n"
      "ds_read_b128 v[36:39], v1 offset:25600\n"
      "s_add_u32 s11, s11, s97\n"
      "v_mfma_f32_16x16x32_bf16 v[132:135], v[44:47], a[156:159], v[132:135]\n"
      "s_waitcnt lgkmcnt(4)\n"
      "s_bfe_u32 s97, s72, 0x10001\n"
      "v_mfma_f32_16x16x32_bf16 v[128:131], v[48:51], a[160:163], v[128:131]\n"
      "ds_read_b128 v[40:43], v1 offset:26624\n"
      "buffer_load_dwordx4 v1, s[36:39], s96 offen offset:2048 sc1 lds\n"
      "v_mfma_f32_16x16x32_bf16 v[128:131], v[52:55], a[168:171], v[128:131]\n"
      "s_mul_i32 s97, s97, 147456\n"
      "s_add_u32 s11, s11, s97\n"
      "v_mfma_f32_16x16x32_bf16 v[132:135], v[48:51], a[164:167], v[132:135]\n"
      "ds_read_b128 v[44:47], v1 offset:27648\n"
      "s_add_u32 s11, s11, 0x400\n"
      "v_mfma_f32_16x16x32_bf16 v[132:135], v[52:55], a[172:175], v[132:135]\n"
      "s_waitcnt lgkmcnt(4)\n"
      "s_mov_b64 s[64:65], s[52:53]\n"
      "v_mfma_f32_16x16x32_bf16 v[128:131], v[56:59], a[176:179], v[128:131]\n"
      "ds_read_b128 v[48:51], v1 offset:28672\n"
      "buffer_load_dwordx4 v1, s[36:39], s96 offen offset:3072 sc1 lds\n"
      "v_mfma_f32_16x16x32_bf16 v[128:131], v[60:63], a[184:187], v[128:131]\n"
      "s_cmp_lt_u32 s75, 18\n"
      "s_cselect_b32 s66, s54, 0\n"
      "v_mfma_f32_16x16x32_bf16 v[132:135], v[56:59], a[180:183], v[132:135]\n"
      "ds_read_b128 v[52:55], v1 offset:29696\n"
      "s_mov_b32 s67, 0x20000\n"
      "v_mfma_f32_16x16x32_bf16 v[132:135], v[60:63], a[188:191], v[132:135]\n"
      "s_waitcnt lgkmcnt(4)\n"
      "v_lshrrev_b32 v198, 3, v2\n"
      "v_mfma_f32_16x16x32_bf16 v[128:131], v[32:35], a[192:195], v[128:131]\n"
      "ds_read_b128 v[56:59], v1 offset:30720\n"
      "v_mul_u32_u24 v198, 9216, v198\n"
      "v_mfma_f32_16x16x32_bf16 v[128:131], v[36:39], a[200:203], v[128:131]\n"
      "s_add_u32 m0, s81, 81920\n"
      "s_add_u32 s96, s85, 81920\n"
      "v_mfma_f32_16x16x32_bf16 v[132:135], v[32:35], a[196:199], v[132:135]\n"
      "ds_read_b128 v[60:63], v1 offset:31744\n"
      "buffer_load_dwordx4 v1, s[36:39], s96 offen sc1 lds\n"
      "v_mfma_f32_16x16x32_bf16 v[132:135], v[36:39], a[204:207], v[132:135]\n"
      "s_waitcnt lgkmcnt(4)\n"
      "v_and_b32 v112, 7, v2\n"
      "v_mfma_f32_16x16x32_bf16 v[128:131], v[40:43], a[208:211], v[128:131]\n"
      "s_waitcnt vmcnt(9)\n"
      "s_barrier\n"
      "ds_read_b128 v[32:35], v1 offset:32768\n"
      "v_mfma_f32_16x16x32_bf16 v[128:131], v[44:47], a[216:219], v[128:131]\n"
      "buffer_load_dwordx4 v1, s[36:39], s96 offen offset:1024 sc1 lds\n"
      "v_lshl_add_u32 v198, v112, 4, v198\n"
      "v_mfma_f32_16x16x32_bf16 v[132:135], v[40:43], a[212:215], v[132:135]\n"
      "ds_read_b128 v[36:39], v1 offset:33792\n"
      "s_mul_i32 s4, s74, 1152\n"
      "v_mfma_f32_16x16x32_bf16 v[132:135], v[44:47], a[220:223], v[132:135]\n"
      "s_waitcnt lgkmcnt(4)\n"
      "s_mul_i32 s97, s75, 165888\n"
      "v_mfma_f32_16x16x32_bf16 v[128:131], v[48:51], a[224:227], v[128:131]\n"
      "ds_read_b128 v[40:43], v1 offset:34816\n"
      "buffer_load_dwordx4 v1, s[36:39], s96 offen offset:2048 sc1 lds\n"
      "v_mfma_f32_16x16x32_bf16 v[128:131], v[52:55], a[232:235], v[128:131]\n"
      "s_add_u32 s4, s4, s97\n"
      "s_bfe_u32 s97, s72, 0x10001\n"
      "v_mfma_f32_16x16x32_bf16 v[132:135], v[48:51], a[228:231], v[132:135]\n"
      "ds_read_b128 v[44:47], v1 offset:35840\n"
      "s_mul_i32 s97, s97, 82944\n"
      "v_mfma_f32_16x16x32_bf16 v[132:135], v[52:55], a[236:239], v[132:135]\n"
      "s_waitcnt lgkmcnt(4)\n"
      "s_add_u32 s4, s4, s97\n"
      "v_mfma_f32_16x16x32_bf16 v[128:131], v[56:59], a[240:243], v[128:131]\n"
      "ds_read_b128 v[48:51], v1 offset:36864\n"
      "buffer_load_dwordx4 v1, s[36:39], s96 offen offset:3072 sc1 lds\n"
      "v_mfma_f32_16x16x32_bf16 v[128:131], v[60:63], a[248:251], v[128:131]\n"
      "s_add_u32 s18, s33, 4096\n"
      "s_add_u32 s19, s33, 8192\n"
      "v_mfma_f32_16x16x32_bf16 v[132:135], v[56:59], a[244:247], v[132:135]\n"
      "ds_read_b128 v[52:55], v1 offset:37888\n"
      "s_mul_i32 s22, s28, 589824\n"
      "v_mfma_f32_16x16x32_bf16 v[132:135], v[60:63], a[252:255], v[132:135]\n"
      "s_waitcnt lgkmcnt(4)\n"
      "s_mul_i32 s97, s30, 147456\n"
      "v_mfma_f32_16x16x32_bf16 v[136:139], v[32:35], a[0:3], 0\n"
      "ds_read_b128 v[56:59], v1 offset:38912\n"
      "s_add_u32 s22, s22, s97\n"
      "v_mfma_f32_16x16x32_bf16 v[136:139], v[36:39], a[8:11], v[136:139]\n"
      "s_add_u32 m0, s81, 98304\n"
      "s_add_u32 s96, s85, 98304\n"
      "v_mfma_f32_16x16x32_bf16 v[140:143], v[32:35], a[4:7], 0\n"
      "ds_read_b128 v[60:63], v1 offset:39936\n"
      "buffer_load_dwordx4 v1, s[36:39], s96 offen sc1 lds\n"
      "v_mfma_f32_16x16x32_bf16 v[140:143], v[36:39], a[12:15], v[140:143]\n"
      "s_waitcnt lgkmcnt(4)\n"
      "s_mul_i32 s97, s29, 36864\n"
      "v_mfma_f32_16x16x32_bf16 v[136:139], v[40:43], a[16:19], v[136:139]\n"
      "ds_read_b128 v[32:35], v1 offset:40960\n"
      "buffer_load_dwordx4 v1, s[36:39], s96 offen offset:1024 sc1 lds\n"
      "v_mfma_f32_16x16x32_bf16 v[136:139], v[44:47], a[24:27], v[136:139]\n"
      "s_add_u32 s22, s22, s97\n"
      "s_add_u32 s22, s22, 0x600000\n"
      "v_mfma_f32_16x16x32_bf16 v[140:143], v[40:43], a[20:23], v[140:143]\n"
      "ds_read_b128 v[36:39], v1 offset:41984\n"
      "s_add_u32 s12, s22, s78\n"
      "v_mfma_f32_16x16x32_bf16 v[140:143], v[44:47], a[28:31], v[140:143]\n"
      "s_waitcnt lgkmcnt(4)\n"
      "s_mul_i32 s97, s72, 9216\n"
      "v_mfma_f32_16x16x32_bf16 v[136:139], v[48:51], a[32:35], v[136:139]\n"
      "ds_read_b128 v[40:43], v1 offset:43008\n"
      "buffer_load_dwordx4 v1, s[36:39], s96 offen offset:2048 sc1 lds\n"
      "v_mfma_f32_16x16x32_bf16 v[136:139], v[52:55], a[40:43], v[136:139]\n"
      "v_cvt_pk_bf16_f32 v96, v128, v129\n"
      "v_cvt_pk_bf16_f32 v98, v132, v133\n"
      "v_mfma_f32_16x16x32_bf16 v[140:143], v[48:51], a[36:39], v[140:143]\n"
      "ds_read_b128 v[44:47], v1 offset:44032\n"
      "v_cvt_pk_bf16_f32 v97, v130, v131\n"
      "v_mfma_f32_16x16x32_bf16 v[140:143], v[52:55], a[44:47], v[140:143]\n"
      "s_waitcnt lgkmcnt(4)\n"
      "v_cvt_pk_bf16_f32 v99, v134, v135\n"
      "v_mfma_f32_16x16x32_bf16 v[136:139], v[56:59], a[48:51], v[136:139]\n"
      "ds_read_b128 v[48:51], v1 offset:45056\n"
      "buffer_load_dwordx4 v1, s[36:39], s96 offen offset:3072 sc1 lds\n"
      "v_mfma_f32_16x16x32_bf16 v[136:139], v[60:63], a[56:59], v[136:139]\n"
      "v_permlane16_swap_b32 v96, v98\n"
      "v_permlane16_swap_b32 v97, v99\n"
      "v_mfma_f32_16x16x32_bf16 v[140:143], v[56:59], a[52:55], v[140:143]\n"
      "ds_read_b128 v[52:55], v1 offset:46080\n"
      "buffer_store_dwordx4 v[96:99], v73, s[36:39], s33 offen sc1\n"
      "v_mfma_f32_16x16x32_bf16 v[140:143], v[60:63], a[60:63], v[140:143]\n"
      "s_waitcnt lgkmcnt(4)\n"
      "s_add_u32 s22, s22, s97\n"
      "v_mfma_f32_16x16x32_bf16 v[136:139], v[32:35], a[64:67], v[136:139]\n"
      "ds_read_b128 v[56:59], v1 offset:47104\n"
      "v_lshrrev_b32 v78, 3, v2\n"
      "v_mfma_f32_16x16x32_bf16 v[136:139], v[36:39], a[72:75], v[136:139]\n"
      "s_add_u32 m0, s81, 114688\n"
      "s_add_u32 s96, s85, 114688\n"
      "v_mfma_f32_16x16x32_bf16 v[140:143], v[32:35], a[68:71], v[140:143]\n"
      "ds_read_b128 v[60:63], v1 offset:48128\n"
      "buffer_load_dwordx4 v1, s[36:39], s96 offen sc1 lds\n"
      "v_mfma_f32_16x16x32_bf16 v[140:143], v[36:39], a[76:79], v[140:143]\n"
      "s_waitcnt lgkmcnt(4)\n"
      "v_lshlrev_b32 v78, 8, v78\n"
      "v_mfma_f32_16x16x32_bf16 v[136:139], v[40:43], a[80:83], v[136:139]\n"
      "s_waitcnt vmcnt(14)\n"
      "s_barrier\n"
      "ds_read_b128 v[32:35], v1 offset:49152\n"
      "v_mfma_f32_16x16x32_bf16 v[136:139], v[44:47], a[88:91], v[136:139]\n"
      "buffer_load_dwordx4 v1, s[36:39], s96 offen offset:1024 sc1 lds\n"
      "v_and_b32 v113, 7, v2\n"
      "v_mfma_f32_16x16x32_bf16 v[140:143], v[40:43], a[84:87], v[140:143]\n"
      "ds_read_b128 v[36:39], v1 offset:50176\n"
      "v_lshl_add_u32 v78, v113, 4, v78\n"
      "v_mfma_f32_16x16x32_bf16 v[140:143], v[44:47], a[92:95], v[140:143]\n"
      "s_waitcnt lgkmcnt(4)\n"
      "s_mul_i32 s13, s28, 147456\n"
      "v_mfma_f32_16x16x32_bf16 v[136:139], v[48:51], a[96:99], v[136:139]\n"
      "ds_read_b128 v[40:43], v1 offset:51200\n"
      "buffer_load_dwordx4 v1, s[36:39], s96 offen offset:2048 sc1 lds\n"
      "v_mfma_f32_16x16x32_bf16 v[136:139], v[52:55], a[104:107], v[136:139]\n"
      "s_bfe_u32 s97, s30, 0x10001\n"
      "s_mul_i32 s97, s97, 73728\n"
      "v_mfma_f32_16x16x32_bf16 v[140:143], v[48:51], a[100:103], v[140:143]\n"
      "ds_read_b128 v[44:47], v1 offset:52224\n"
      "s_add_u32 s13, s13, s97\n"
      "v_mfma_f32_16x16x32_bf16 v[140:143], v[52:55], a[108:111], v[140:143]\n"
      "s_waitcnt lgkmcnt(4)\n"
      "s_bfe_u32 s97, s30, 0x10000\n"
      "v_mfma_f32_16x16x32_bf16 v[136:139], v[56:59], a[112:115], v[136:139]\n"
      "ds_read_b128 v[48:51], v1 offset:53248\n"
      "buffer_load_dwordx4 v1, s[36:39], s96 offen offset:3072 sc1 lds\n"
      "v_mfma_f32_16x16x32_bf16 v[136:139], v[60:63], a[120:123], v[136:139]\n"
      "s_lshl_b32 s97, s97, 12\n"
      "s_add_u32 s13, s13, s97\n"
      "v_mfma_f32_16x16x32_bf16 v[140:143], v[56:59], a[116:119], v[140:143]\n"
      "ds_read_b128 v[52:55], v1 offset:54272\n"
      "s_bfe_u32 s97, s29, 0x10001\n"
      "v_mfma_f32_16x16x32_bf16 v[140:143], v[60:63], a[124:127], v[140:143]\n"
      "s_waitcnt lgkmcnt(4)\n"
      "s_lshl_b32 s97, s97, 11\n"
      "v_mfma_f32_16x16x32_bf16 v[136:139], v[32:35], a[128:131], v[136:139]\n"
      "ds_read_b128 v[56:59], v1 offset:55296\n"
      "s_add_u32 s13, s13, s97\n"
      "v_mfma_f32_16x16x32_bf16 v[136:139], v[36:39], a[136:139], v[136:139]\n"
      "s_add_u32 m0, s79, 131072\n"
      "s_add_u32 s96, s83, 131072\n"
      "v_mfma_f32_16x16x32_bf16 v[140:143], v[32:35], a[132:135], v[140:143]\n"
      "ds_read_b128 v[60:63], v1 offset:56320\n"
      "buffer_load_dwordx4 v1, s[36:39], s96 offen sc1 lds\n"
      "v_mfma_f32_16x16x32_bf16 v[140:143], v[36:39], a[140:143], v[140:143]\n"
      "s_waitcnt lgkmcnt(4)\n"
      "s_bfe_u32 s97, s29, 0x10000\n"
      "v_mfma_f32_16x16x32_bf16 v[136:139], v[40:43], a[144:147], v[136:139]\n"
      "ds_read_b128 v[32:35], v1 offset:57344\n"
      "buffer_load_dwordx4 v1, s[36:39], s96 offen offset:1024 sc1 lds\n"
      "v_mfma_f32_16x16x32_bf16 v[136:139], v[44:47], a[152:155], v[136:139]\n"
      "s_lshl_b32 s97, s97, 7\n"
      "s_add_u32 s13, s13, s97\n"
      "v_mfma_f32_16x16x32_bf16 v[140:143], v[40:43], a[148:151], v[140:143]\n"
      "ds_read_b128 v[36:39], v1 offset:58368\n"
      "s_lshl_b32 s97, s72, 13\n"
      "v_mfma_f32_16x16x32_bf16 v[140:143], v[44:47], a[156:159], v[140:143]\n"
      "s_waitcnt lgkmcnt(4)\n"
      "s_add_u32 s13, s13, s97\n"
      "v_mfma_f32_16x16x32_bf16 v[136:139], v[48:51], a[160:163], v[136:139]\n"
      "ds_read_b128 v[40:43], v1 offset:59392\n"
      "s_add_u32 s13, s13, 0xf00000\n"
      "v_mfma_f32_16x16x32_bf16 v[136:139], v[52:55], a[168:171], v[136:139]\n"
      "s_add_u32 m0, s79, 139264\n"
      "s_add_u32 s96, s83, 139264\n"
      "v_mfma_f32_16x16x32_bf16 v[140:143], v[48:51], a[164:167], v[140:143]\n"
      "ds_read_b128 v[44:47], v1 offset:60416\n"
      "buffer_load_dwordx4 v1, s[36:39], s96 offen sc1 lds\n"
      "v_mfma_f32_16x16x32_bf16 v[140:143], v[52:55], a[172:175], v[140:143]\n"
      "s_waitcnt lgkmcnt(4)\n"
      "s_lshl_b32 s16, s72, 15\n"
      "v_mfma_f32_16x16x32_bf16 v[136:139], v[56:59], a[176:179], v[136:139]\n"
      "ds_read_b128 v[48:51], v1 offset:61440\n"
      "buffer_load_dwordx4 v1, s[36:39], s96 offen offset:1024 sc1 lds\n"
      "v_mfma_f32_16x16x32_bf16 v[136:139], v[60:63], a[184:187], v[136:139]\n"
      "s_add_u32 s16, s16, s7\n"
      "s_add_u32 s16, s16, 0x18000\n"
      "v_mfma_f32_16x16x32_bf16 v[140:143], v[56:59], a[180:183], v[140:143]\n"
      "ds_read_b128 v[52:55], v1 offset:62464\n"
      "v_min_u32 v112, 23, v2\n"
      "v_mfma_f32_16x16x32_bf16 v[140:143], v[60:63], a[188:191], v[140:143]\n"
      "s_waitcnt lgkmcnt(4)\n"
      "v_mul_u32_u24 v113, 8192, v112\n"
      "v_mfma_f32_16x16x32_bf16 v[136:139], v[32:35], a[192:195], v[136:139]\n"
      "ds_read_b128 v[56:59], v1 offset:63488\n"
      "v_lshrrev_b32 v113, 16, v113\n"
      "v_mfma_f32_16x16x32_bf16 v[136:139], v[36:39], a[200:203], v[136:139]\n"
      "s_add_u32 m0, s79, 0\n"
      "s_add_u32 s96, s83, 147456\n"
      "v_mfma_f32_16x16x32_bf16 v[140:143], v[32:35], a[196:199], v[140:143]\n"
      "ds_read_b128 v[60:63], v1 offset:64512\n"
      "buffer_load_dwordx4 v1, s[36:39], s96 offen sc1 lds\n"
      "v_mfma_f32_16x16x32_bf16 v[140:143], v[36:39], a[204:207], v[140:143]\n"
      "s_waitcnt lgkmcnt(4)\n"
      "v_mul_u32_u24 v113, 8192, v113\n"
      "v_mfma_f32_16x16x32_bf16 v[136:139], v[40:43], a[208:211], v[136:139]\n"
      "s_waitcnt vmcnt(14)\n"
      "s_barrier\n"
      "ds_read_b128 v[32:35], v28\n"
      "v_mfma_f32_16x16x32_bf16 v[136:139], v[44:47], a[216:219], v[136:139]\n"
      "buffer_load_dwordx4 v1, s[36:39], s96 offen offset:1024 sc1 lds\n"
      "v_lshl_add_u32 v15, v112, 7, v113\n"
      "v_mfma_f32_16x16x32_bf16 v[140:143], v[40:43], a[212:215], v[140:143]\n"
      "ds_read_b128 v[36:39], v28 offset:1024\n"
      "v_bfe_u32 v112, v2, 4, 1\n"
      "v_mfma_f32_16x16x32_bf16 v[140:143], v[44:47], a[220:223], v[140:143]\n"
      "s_waitcnt lgkmcnt(4)\n"
      "v_lshlrev_b32 v112, 5, v112\n"
      "v_mfma_f32_16x16x32_bf16 v[136:139], v[48:51], a[224:227], v[136:139]\n"
      "ds_read_b128 v[40:43], v28 offset:2048\n"
      "v_and_b32 v113, 0x70, v1\n"
      "v_mfma_f32_16x16x32_bf16 v[136:139], v[52:55], a[232:235], v[136:139]\n"
      "s_add_u32 m0, s81, 8192\n"
      "s_add_u32 s96, s85, 155648\n"
      "v_mfma_f32_16x16x32_bf16 v[140:143], v[48:51], a[228:231], v[140:143]\n"
      "ds_read_b128 v[44:47], v28 offset:3072\n"
      "buffer_load_dwordx4 v1, s[36:39], s96 offen sc1 lds\n"
      "v_mfma_f32_16x16x32_bf16 v[140:143], v[52:55], a[236:239], v[140:143]\n"
      "s_waitcnt lgkmcnt(4)\n"
      "v_xor_b32 v112, v112, v113\n"
      "v_mfma_f32_16x16x32_bf16 v[136:139], v[56:59], a[240:243], v[136:139]\n"
      "ds_read_b128 v[48:51], v28 offset:4096\n"
      "buffer_load_dwordx4 v1, s[36:39], s96 offen offset:1024 sc1 lds\n"
      "v_mfma_f32_16x16x32_bf16 v[136:139], v[60:63], a[248:251], v[136:139]\n"
      "v_lshrrev_b32 v113, 3, v2\n"
      "v_mul_u32_u24 v113, 9216, v113\n"
      "v_mfma_f32_16x16x32_bf16 v[140:143], v[56:59], a[244:247], v[140:143]\n"
      "ds_read_b128 v[52:55], v28 offset:5120\n"
      "v_xor_b32 v114, 0, v112\n"
      "v_mfma_f32_16x16x32_bf16 v[140:143], v[60:63], a[252:255], v[140:143]\n"
      "s_waitcnt lgkmcnt(4)\n"
      "v_add_u32 v114, v114, v113\n"
      "v_mfma_f32_16x16x32_bf16 v[144:147], v[32:35], a[0:3], 0\n"
      "ds_read_b128 v[56:59], v28 offset:6144\n"
      "buffer_load_dwordx4 v1, s[36:39], s96 offen offset:2048 sc1 lds\n"
      "v_mfma_f32_16x16x32_bf16 v[144:147], v[36:39], a[8:11], v[144:147]\n"
      "v_add_u32 v4, 0, v114\n"
      "v_xor_b32 v114, 64, v112\n"
      "v_mfma_f32_16x16x32_bf16 v[148:151], v[32:35], a[4:7], 0\n"
      "ds_read_b128 v[60:63], v28 offset:7168\n"
      "v_add_u32 v114, v114, v113\n"
      "v_mfma_f32_16x16x32_bf16 v[148:151], v[36:39], a[12:15], v[148:151]\n"
      "s_waitcnt lgkmcnt(4)\n"
      "v_add_u32 v5, 72704, v114\n"
      "v_mfma_f32_16x16x32_bf16 v[144:147], v[40:43], a[16:19], v[144:147]\n"
      "ds_read_b128 v[32:35], v28 offset:8192\n"
      "buffer_load_dwordx4 v1, s[36:39], s96 offen offset:3072 sc1 lds\n"
      "v_mfma_f32_16x16x32_bf16 v[144:147], v[44:47], a[24:27], v[144:147]\n"
      "v_xor_b32 v114, 0, v112\n"
      "v_add_u32 v114, v114, v113\n"
      "v_mfma_f32_16x16x32_bf16 v[148:151], v[40:43], a[20:23], v[148:151]\n"
      "ds_read_b128 v[36:39], v28 offset:9216\n"
      "v_add_u32 v6, 145408, v114\n"
      "v_mfma_f32_16x16x32_bf16 v[148:151], v[44:47], a[28:31], v[148:151]\n"
      "s_waitcnt lgkmcnt(4)\n"
      "v_xor_b32 v114, 64, v112\n"
      "v_mfma_f32_16x16x32_bf16 v[144:147], v[48:51], a[32:35], v[144:147]\n"
      "ds_read_b128 v[40:43], v28 offset:10240\n"
      "v_cvt_pk_bf16_f32 v100, v136, v137\n"
      "v_mfma_f32_16x16x32_bf16 v[144:147], v[52:55], a[40:43], v[144:147]\n"
      "s_add_u32 m0, s81, 24576\n"
      "s_add_u32 s96, s85, 172032\n"
      "v_mfma_f32_16x16x32_bf16 v[148:151], v[48:51], a[36:39], v[148:151]\n"
      "ds_read_b128 v[44:47], v28 offset:11264\n"
      "buffer_load_dwordx4 v1, s[36:39], s96 offen sc1 lds\n"
      "v_mfma_f32_16x16x32_bf16 v[148:151], v[52:55], a[44:47], v[148:151]\n"
      "s_waitcnt lgkmcnt(4)\n"
      "v_cvt_pk_bf16_f32 v102, v140, v141\n"
      "v_mfma_f32_16x16x32_bf16 v[144:147], v[56:59], a[48:51], v[144:147]\n"
      "ds_read_b128 v[48:51], v28 offset:12288\n"
      "buffer_load_dwordx4 v1, s[36:39], s96 offen offset:1024 sc1 lds\n"
      "v_mfma_f32_16x16x32_bf16 v[144:147], v[60:63], a[56:59], v[144:147]\n"
      "v_cvt_pk_bf16_f32 v101, v138, v139\n"
      "v_cvt_pk_bf16_f32 v103, v142, v143\n"
      "v_mfma_f32_16x16x32_bf16 v[148:151], v[56:59], a[52:55], v[148:151]\n"
      "ds_read_b128 v[52:55], v28 offset:13312\n"
      "v_permlane16_swap_b32 v100, v102\n"
      "v_mfma_f32_16x16x32_bf16 v[148:151], v[60:63], a[60:63], v[148:151]\n"
      "s_waitcnt lgkmcnt(4)\n"
      "v_permlane16_swap_b32 v101, v103\n"
      "v_mfma_f32_16x16x32_bf16 v[144:147], v[32:35], a[64:67], v[144:147]\n"
      "ds_read_b128 v[56:59], v28 offset:14336\n"
      "buffer_load_dwordx4 v1, s[36:39], s96 offen offset:2048 sc1 lds\n"
      "v_mfma_f32_16x16x32_bf16 v[144:147], v[36:39], a[72:75], v[144:147]\n"
      "buffer_store_dwordx4 v[100:103], v73, s[36:39], s33 offen offset:1024 sc1\n"
      "v_add_u32 v114, v114, v113\n"
      "v_mfma_f32_16x16x32_bf16 v[148:151], v[32:35], a[68:71], v[148:151]\n"
      "ds_read_b128 v[60:63], v28 offset:15360\n"
      "v_add_u32 v7, 218112, v114\n"
      "v_mfma_f32_16x16x32_bf16 v[148:151], v[36:39], a[76:79], v[148:151]\n"
      "s_waitcnt lgkmcnt(4)\n"
      "s_lshl_b32 s92, s73, 2\n"
      "v_mfma_f32_16x16x32_bf16 v[144:147], v[40:43], a[80:83], v[144:147]\n"
      "ds_read_b128 v[32:35], v28 offset:16384\n"
      "buffer_load_dwordx4 v1, s[36:39], s96 offen offset:3072 sc1 lds\n"
      "v_mfma_f32_16x16x32_bf16 v[144:147], v[44:47], a[88:91], v[144:147]\n"
      "s_add_u32 s92, s92, s72\n"
      "s_mul_i32 s90, s92, 21846\n"
      "v_mfma_f32_16x16x32_bf16 v[148:151], v[40:43], a[84:87], v[148:151]\n"
      "ds_read_b128 v[36:39], v28 offset:17408\n"
      "s_lshr_b32 s90, s90, 16\n"
      "v_mfma_f32_16x16x32_bf16 v[148:151], v[44:47], a[92:95], v[148:151]\n"
      "s_waitcnt lgkmcnt(4)\n"
      "s_mul_i32 s97, s90, 3\n"
      "v_mfma_f32_16x16x32_bf16 v[144:147], v[48:51], a[96:99], v[144:147]\n"
      "ds_read_b128 v[40:43], v28 offset:18432\n"
      "s_sub_u32 s91, s92, s97\n"
      "v_mfma_f32_16x16x32_bf16 v[144:147], v[52:55], a[104:107], v[144:147]\n"
      "s_add_u32 m0, s81, 40960\n"
      "s_add_u32 s96, s85, 188416\n"
      "v_mfma_f32_16x16x32_bf16 v[148:151], v[48:51], a[100:103], v[148:151]\n"
      "ds_read_b128 v[44:47], v28 offset:19456\n"
      "buffer_load_dwordx4 v1, s[36:39], s96 offen sc1 lds\n"
      "v_mfma_f32_16x16x32_bf16 v[148:151], v[52:55], a[108:111], v[148:151]\n"
      "s_waitcnt lgkmcnt(4)\n"
      "s_mul_i32 s57, s90, 18432\n"
      "v_mfma_f32_16x16x32_bf16 v[144:147], v[56:59], a[112:115], v[144:147]\n"
      "ds_read_b128 v[48:51], v28 offset:20480\n"
      "buffer_load_dwordx4 v1, s[36:39], s96 offen offset:1024 sc1 lds\n"
      "v_mfma_f32_16x16x32_bf16 v[144:147], v[60:63], a[120:123], v[144:147]\n"
      "s_mul_i32 s97, s91, 384\n"
      "s_add_u32 s57, s57, s97\n"
      "v_mfma_f32_16x16x32_bf16 v[148:151], v[56:59], a[116:119], v[148:151]\n"
      "ds_read_b128 v[52:55], v28 offset:21504\n"
      "v_lshrrev_b32 v77, 3, v2\n"
      "v_mfma_f32_16x16x32_bf16 v[148:151], v[60:63], a[124:127], v[148:151]\n"
      "s_waitcnt lgkmcnt(4)\n"
      "v_mul_u32_u24 v77, 1152, v77\n"
      "v_mfma_f32_16x16x32_bf16 v[144:147], v[32:35], a[128:131], v[144:147]\n"
      "ds_read_b128 v[56:59], v28 offset:22528\n"
      "buffer_load_dwordx4 v1, s[36:39], s96 offen offset:2048 sc1 lds\n"
      "v_mfma_f32_16x16x32_bf16 v[144:147], v[36:39], a[136:139], v[144:147]\n"
      "v_and_b32 v112, 7, v2\n"
      "v_lshl_add_u32 v77, v112, 4, v77\n"
      "v_mfma_f32_16x16x32_bf16 v[148:151], v[32:35], a[132:135], v[148:151]\n"
      "ds_read_b128 v[60:63], v28 offset:23552\n"
      "v_and_b32 v31, 7, v2\n"
      "v_mfma_f32_16x16x32_bf16 v[148:151], v[36:39], a[140:143], v[148:151]\n"
      "s_waitcnt lgkmcnt(4)\n"
      "v_mul_u32_u24 v31, 1152, v31\n"
      "v_mfma_f32_16x16x32_bf16 v[144:147], v[40:43], a[144:147], v[144:147]\n"
      "ds_read_b128 v[32:35], v28 offset:24576\n"
      "buffer_load_dwordx4 v1, s[36:39], s96 offen offset:3072 sc1 lds\n"
      "v_mfma_f32_16x16x32_bf16 v[144:147], v[44:47], a[152:155], v[144:147]\n"
      "v_lshrrev_b32 v112, 3, v2\n"
      "v_lshl_add_u32 v31, v112, 4, v31\n"
      "v_mfma_f32_16x16x32_bf16 v[148:151], v[40:43], a[148:151], v[148:151]\n"
      "ds_read_b128 v[36:39], v28 offset:25600\n"
      "s_min_u32 s91, s72, 2\n"
      "v_mfma_f32_16x16x32_bf16 v[148:151], v[44:47], a[156:159], v[148:151]\n"
      "s_waitcnt lgkmcnt(4)\n"
      "s_mul_i32 s34, s28, 147456\n"
      "v_mfma_f32_16x16x32_bf16 v[144:147], v[48:51], a[160:163], v[144:147]\n"
      "ds_read_b128 v[40:43], v28 offset:26624\n"
      "s_mul_i32 s97, s30, 36864\n"
      "v_mfma_f32_16x16x32_bf16 v[144:147], v[52:55], a[168:171], v[144:147]\n"
      "s_add_u32 s34, s34, s97\n"
      "s_mul_i32 s97, s29, 9216\n"
      "v_mfma_f32_16x16x32_bf16 v[148:151], v[48:51], a[164:167], v[148:151]\n"
      "ds_read_b128 v[44:47], v28 offset:27648\n"
      "s_add_u32 s34, s34, s97\n"
      "v_mfma_f32_16x16x32_bf16 v[148:151], v[52:55], a[172:175], v[148:151]\n"
      "s_waitcnt lgkmcnt(4)\n"
      "s_lshl_b32 s97, s91, 7\n"
      "v_mfma_f32_16x16x32_bf16 v[144:147], v[56:59], a[176:179], v[144:147]\n"
      "ds_read_b128 v[48:51], v28 offset:28672\n"
      "s_add_u32 s34, s34, s97\n"
      "v_mfma_f32_16x16x32_bf16 v[144:147], v[60:63], a[184:187], v[144:147]\n"
      "s_lshl_b32 s92, s73, 2\n"
      "s_add_u32 s92, s92, s72\n"
      "v_mfma_f32_16x16x32_bf16 v[148:151], v[56:59], a[180:183], v[148:151]\n"
      "ds_read_b128 v[52:55], v28 offset:29696\n"
      "s_mul_i32 s90, s92, 21846\n"
      "v_mfma_f32_16x16x32_bf16 v[148:151], v[60:63], a[188:191], v[148:151]\n"
      "s_waitcnt lgkmcnt(4)\n"
      "s_lshr_b32 s90, s90, 16\n"
      "v_mfma_f32_16x16x32_bf16 v[144:147], v[32:35], a[192:195], v[144:147]\n"
      "ds_read_b128 v[56:59], v28 offset:30720\n"
      "s_mul_i32 s97, s90, 3\n"
      "v_mfma_f32_16x16x32_bf16 v[144:147], v[36:39], a[200:203], v[144:147]\n"
      "s_sub_u32 s91, s92, s97\n"
      "s_mul_i32 s92, s90, 1821\n"
      "v_mfma_f32_16x16x32_bf16 v[148:151], v[32:35], a[196:199], v[148:151]\n"
      "ds_read_b128 v[60:63], v28 offset:31744\n"
      "s_lshr_b32 s92, s92, 16\n"
      "v_mfma_f32_16x16x32_bf16 v[148:151], v[36:39], a[204:207], v[148:151]\n"
      "s_waitcnt lgkmcnt(4)\n"
      "s_mul_i32 s97, s92, 36\n"
      "v_mfma_f32_16x16x32_bf16 v[144:147], v[40:43], a[208:211], v[144:147]\n"
      "s_waitcnt vmcnt(19)\n"
      "s_barrier\n"
      "ds_read_b128 v[32:35], v28 offset:32768\n"
      "v_mfma_f32_16x16x32_bf16 v[144:147], v[44:47], a[216:219], v[144:147]\n"
      "s_add_u32 m0, s81, 57344\n"
      "s_add_u32 s96, s85, 204800\n"
      "v_mfma_f32_16x16x32_bf16 v[148:151], v[40:43], a[212:215], v[148:151]\n"
      "ds_read_b128 v[36:39], v28 offset:33792\n"
      "buffer_load_dwordx4 v1, s[36:39], s96 offen sc1 lds\n"
      "v_mfma_f32_16x16x32_bf16 v[148:151], v[44:47], a[220:223], v[148:151]\n"
      "s_waitcnt lgkmcnt(4)\n"
      "s_sub_u32 s6, s90, s97\n"
      "v_mfma_f32_16x16x32_bf16 v[144:147], v[48:51], a[224:227], v[144:147]\n"
      "ds_read_b128 v[40:43], v28 offset:34816\n"
      "buffer_load_dwordx4 v1, s[36:39], s96 offen offset:1024 sc1 lds\n"
      "v_mfma_f32_16x16x32_bf16 v[144:147], v[52:55], a[232:235], v[144:147]\n"
      "s_bfe_u32 s94, s6, 0x20002\n"
      "s_bfe_u32 s95, s6, 0x20000\n"
      "v_mfma_f32_16x16x32_bf16 v[148:151], v[48:51], a[228:231], v[148:151]\n"
      "ds_read_b128 v[44:47], v28 offset:35840\n"
      "s_bfe_u32 s97, s6, 0x10004\n"
      "v_mfma_f32_16x16x32_bf16 v[148:151], v[52:55], a[236:239], v[148:151]\n"
      "s_waitcnt lgkmcnt(4)\n"
      "s_mul_i32 s97, s97, 5\n"
      "v_mfma_f32_16x16x32_bf16 v[144:147], v[56:59], a[240:243], v[144:147]\n"
      "ds_read_b128 v[48:51], v28 offset:36864\n"
      "buffer_load_dwordx4 v1, s[36:39], s96 offen offset:2048 sc1 lds\n"
      "v_mfma_f32_16x16x32_bf16 v[144:147], v[60:63], a[248:251], v[144:147]\n"
      "s_add_u32 s95, s95, s97\n"
      "s_and_b32 s17, s6, 3\n"
      "v_mfma_f32_16x16x32_bf16 v[148:151], v[56:59], a[244:247], v[148:151]\n"
      "ds_read_b128 v[52:55], v28 offset:37888\n"
      "v_mfma_f32_16x16x32_bf16 v[148:151], v[60:63], a[252:255], v[148:151]\n"
      "s_waitcnt lgkmcnt(4)\n"
      "v_mfma_f32_16x16x32_bf16 v[152:155], v[32:35], a[0:3], 0\n"
      "ds_read_b128 v[56:59], v28 offset:38912\n"
      "buffer_load_dwordx4 v1, s[36:39], s96 offen offset:3072 sc1 lds\n"
      "v_mfma_f32_16x16x32_bf16 v[152:155], v[36:39], a[8:11], v[152:155]\n"
      "s_cmp_ge_u32 s6, 32\n"
      "s_cselect_b32 s94, s17, s94\n"
      "s_cselect_b32 s6, 4, s95\n"
      "v_mfma_f32_16x16x32_bf16 v[156:159], v[32:35], a[4:7], 0\n"
      "ds_read_b128 v[60:63], v28 offset:39936\n"
      "s_lshl_b32 s92, s92, 2\n"
      "v_mfma_f32_16x16x32_bf16 v[156:159], v[36:39], a[12:15], v[156:159]\n"
      "s_waitcnt lgkmcnt(4)\n"
      "s_add_u32 s92, s92, s94\n"
      "v_mfma_f32_16x16x32_bf16 v[152:155], v[40:43], a[16:19], v[152:155]\n"
      "ds_read_b128 v[32:35], v28 offset:40960\n"
      "s_lshl_b32 s6, s6, 2\n"
      "v_mfma_f32_16x16x32_bf16 v[152:155], v[44:47], a[24:27], v[152:155]\n"
      "s_add_u32 m0, s81, 73728\n"
      "s_add_u32 s96, s85, 221184\n"
      "v_mfma_f32_16x16x32_bf16 v[156:159], v[40:43], a[20:23], v[156:159]\n"
      "ds_read_b128 v[36:39], v28 offset:41984\n"
      "buffer_load_dwordx4 v1, s[36:39], s96 offen sc1 lds\n"
      "v_mfma_f32_16x16x32_bf16 v[156:159], v[44:47], a[28:31], v[156:159]\n"
      "s_waitcnt lgkmcnt(4)\n"
      "s_bfe_u32 s97, s92, 0x20001\n"
      "v_mfma_f32_16x16x32_bf16 v[152:155], v[48:51], a[32:35], v[152:155]\n"
      "ds_read_b128 v[40:43], v28 offset:43008\n"
      "buffer_load_dwordx4 v1, s[36:39], s96 offen offset:1024 sc1 lds\n"
      "v_mfma_f32_16x16x32_bf16 v[152:155], v[52:55], a[40:43], v[152:155]\n"
      "v_cvt_pk_bf16_f32 v104, v144, v145\n"
      "v_cvt_pk_bf16_f32 v106, v148, v149\n"
      "v_mfma_f32_16x16x32_bf16 v[156:159], v[48:51], a[36:39], v[156:159]\n"
      "ds_read_b128 v[44:47], v28 offset:44032\n"
      "v_cvt_pk_bf16_f32 v105, v146, v147\n"
      "v_mfma_f32_16x16x32_bf16 v[156:159], v[52:55], a[44:47], v[156:159]\n"
      "s_waitcnt lgkmcnt(4)\n"
      "v_cvt_pk_bf16_f32 v107, v150, v151\n"
      "v_mfma_f32_16x16x32_bf16 v[152:155], v[56:59], a[48:51], v[152:155]\n"
      "ds_read_b128 v[48:51], v28 offset:45056\n"
      "buffer_load_dwordx4 v1, s[36:39], s96 offen offset:2048 sc1 lds\n"
      "v_mfma_f32_16x16x32_bf16 v[152:155], v[60:63], a[56:59], v[152:155]\n"
      "v_permlane16_swap_b32 v104, v106\n"
      "v_permlane16_swap_b32 v105, v107\n"
      "v_mfma_f32_16x16x32_bf16 v[156:159], v[56:59], a[52:55], v[156:159]\n"
      "ds_read_b128 v[52:55], v28 offset:46080\n"
      "buffer_store_dwordx4 v[104:107], v73, s[36:39], s33 offen offset:2048 sc1\n"
      "v_mfma_f32_16x16x32_bf16 v[156:159], v[60:63], a[60:63], v[156:159]\n"
      "s_waitcnt lgkmcnt(4)\n"
      "s_add_u32 s6, s6, s97\n"
      "v_mfma_f32_16x16x32_bf16 v[152:155], v[32:35], a[64:67], v[152:155]\n"
      "ds_read_b128 v[56:59], v28 offset:47104\n"
      "buffer_load_dwordx4 v1, s[36:39], s96 offen offset:3072 sc1 lds\n"
      "v_mfma_f32_16x16x32_bf16 v[152:155], v[36:39], a[72:75], v[152:155]\n"
      "s_bfe_u32 s5, s92, 0x20003\n"
      "s_mul_i32 s5, s5, 1327104\n"
      "v_mfma_f32_16x16x32_bf16 v[156:159], v[32:35], a[68:71], v[156:159]\n"
      "ds_read_b128 v[60:63], v28 offset:48128\n"
      "s_bfe_u32 s97, s92, 0x10000\n"
      "v_mfma_f32_16x16x32_bf16 v[156:159], v[36:39], a[76:79], v[156:159]\n"
      "s_waitcnt lgkmcnt(4)\n"
      "s_lshl_b32 s97, s97, 9\n"
      "v_mfma_f32_16x16x32_bf16 v[152:155], v[40:43], a[80:83], v[152:155]\n"
      "ds_read_b128 v[32:35], v28 offset:49152\n"
      "Lpad3_%=:\n"
      "s_cbranch_execz Lret_%=\n"
      "s_add_u32 s5, s5, s97\n"
      "v_mfma_f32_16x16x32_bf16 v[152:155], v[44:47], a[88:91], v[152:155]\n"
      "s_add_u32 s5, s5, 0x1d00000\n"
      "s_bfe_u32 s94, s6, 0x50001\n"
      "v_mfma_f32_16x16x32_bf16 v[156:159], v[40:43], a[84:87], v[156:159]\n"
      "ds_read_b128 v[36:39], v28 offset:50176\n"
      "s_mul_i32 s94, s94, 12\n"
      "v_mfma_f32_16x16x32_bf16 v[156:159], v[44:47], a[92:95], v[156:159]\n"
      "s_waitcnt lgkmcnt(4)\n"
      "s_bfe_u32 s97, s6, 0x10000\n"
      "v_mfma_f32_16x16x32_bf16 v[152:155], v[48:51], a[96:99], v[152:155]\n"
      "ds_read_b128 v[40:43], v28 offset:51200\n"
      "s_add_u32 s93, s4, 0\n"
      "v_mfma_f32_16x16x32_bf16 v[152:155], v[52:55], a[104:107], v[152:155]\n"
      "buffer_load_dword v192, v15, s[52:55], s93 offen\n"
      "s_add_u32 s94, s94, s97\n"
      "v_mfma_f32_16x16x32_bf16 v[156:159], v[48:51], a[100:103], v[156:159]\n"
      "ds_read_b128 v[44:47], v28 offset:52224\n"
      "s_bfe_u32 s95, s6, 0x50001\n"
      "v_mfma_f32_16x16x32_bf16 v[156:159], v[52:55], a[108:111], v[156:159]\n"
      "s_waitcnt lgkmcnt(4)\n"
      "s_mul_i32 s95, s95, 18\n"
      "v_mfma_f32_16x16x32_bf16 v[152:155], v[56:59], a[112:115], v[152:155]\n"
      "ds_read_b128 v[48:51], v28 offset:53248\n"
      "s_bfe_u32 s97, s6, 0x10000\n"
      "v_mfma_f32_16x16x32_bf16 v[152:155], v[60:63], a[120:123], v[152:155]\n"
      "s_add_u32 s95, s95, s97\n"
      "v_mfma_f32_16x16x32_bf16 v[156:159], v[56:59], a[116:119], v[156:159]\n"
      "ds_read_b128 v[52:55], v28 offset:54272\n"
      "v_mfma_f32_16x16x32_bf16 v[156:159], v[60:63], a[124:127], v[156:159]\n"
      "s_waitcnt lgkmcnt(4)\n"
      "v_mfma_f32_16x16x32_bf16 v[152:155], v[32:35], a[128:131], v[152:155]\n"
      "ds_read_b128 v[56:59], v28 offset:55296\n"
      "v_mfma_f32_16x16x32_bf16 v[152:155], v[36:39], a[136:139], v[152:155]\n"
      "s_cmp_ge_u32 s6, 20\n"
      "s_cselect_b32 s94, s95, s94\n"
      "s_cselect_b32 s17, 9, 6\n"
      "v_mfma_f32_16x16x32_bf16 v[156:159], v[32:35], a[132:135], v[156:159]\n"
      "ds_read_b128 v[60:63], v28 offset:56320\n"
      "s_add_u32 s95, s6, 224\n"
      "v_mfma_f32_16x16x32_bf16 v[156:159], v[36:39], a[140:143], v[156:159]\n"
      "s_waitcnt lgkmcnt(4)\n"
      "v_mfma_f32_16x16x32_bf16 v[152:155], v[40:43], a[144:147], v[152:155]\n"
      "ds_read_b128 v[32:35], v28 offset:57344\n"
      "v_mfma_f32_16x16x32_bf16 v[152:155], v[44:47], a[152:155], v[152:155]\n"
      "s_cmp_ge_u32 s6, 28\n"
      "s_cselect_b32 s6, s95, s6\n"
      "s_cselect_b32 s95, 8, 20\n"
      "s_cselect_b32 s17, 0, s17\n"
      "v_mfma_f32_16x16x32_bf16 v[156:159], v[40:43], a[148:151], v[156:159]\n"
      "ds_read_b128 v[36:39], v28 offset:58368\n"
      "s_mul_i32 s92, s91, 12\n"
      "v_mfma_f32_16x16x32_bf16 v[156:159], v[44:47], a[156:159], v[156:159]\n"
      "s_waitcnt lgkmcnt(4)\n"
      "v_lshrrev_b32 v116, 5, v2\n"
      "v_mfma_f32_16x16x32_bf16 v[152:155], v[48:51], a[160:163], v[152:155]\n"
      "ds_read_b128 v[40:43], v28 offset:59392\n"
      "v_add_u32 v116, s92, v116\n"
      "v_mfma_f32_16x16x32_bf16 v[152:155], v[52:55], a[168:171], v[152:155]\n"
      "v_and_b32 v117, 0x1f0, v1\n"
      "v_add_u32 v112, 0, v116\n"
      "v_mfma_f32_16x16x32_bf16 v[156:159], v[48:51], a[164:167], v[156:159]\n"
      "ds_read_b128 v[44:47], v28 offset:60416\n"
      "v_and_b32 v113, 3, v112\n"
      "v_mfma_f32_16x16x32_bf16 v[156:159], v[52:55], a[172:175], v[156:159]\n"
      "s_waitcnt lgkmcnt(4)\n"
      "v_lshrrev_b32 v112, 2, v112\n"
      "v_mfma_f32_16x16x32_bf16 v[152:155], v[56:59], a[176:179], v[152:155]\n"
      "ds_read_b128 v[48:51], v28 offset:61440\n"
      "v_cmp_gt_u32 vcc, s17, v112\n"
      "v_mfma_f32_16x16x32_bf16 v[152:155], v[60:63], a[184:187], v[152:155]\n"
      "v_lshl_add_u32 v114, v112, 1, s94\n"
      "v_mul_u32_u24 v115, s95, v112\n"
      "v_mfma_f32_16x16x32_bf16 v[156:159], v[56:59], a[180:183], v[156:159]\n"
      "ds_read_b128 v[52:55], v28 offset:62464\n"
      "v_add_u32 v115, s6, v115\n"
      "v_mfma_f32_16x16x32_bf16 v[156:159], v[60:63], a[188:191], v[156:159]\n"
      "s_waitcnt lgkmcnt(4)\n"
      "v_cndmask_b32 v114, v115, v114, vcc\n"
      "v_mfma_f32_16x16x32_bf16 v[152:155], v[32:35], a[192:195], v[152:155]\n"
      "ds_read_b128 v[56:59], v28 offset:63488\n"
      "v_mul_u32_u24 v113, 324, v113\n"
      "v_mfma_f32_16x16x32_bf16 v[152:155], v[36:39], a[200:203], v[152:155]\n"
      "v_add_u32 v114, v114, v113\n"
      "v_lshl_add_u32 v248, v114, 10, v117\n"
      "v_mfma_f32_16x16x32_bf16 v[156:159], v[32:35], a[196:199], v[156:159]\n"
      "ds_read_b128 v[60:63], v28 offset:64512\n"
      "v_add_u32 v112, 2, v116\n"
      "v_mfma_f32_16x16x32_bf16 v[156:159], v[36:39], a[204:207], v[156:159]\n"
      "s_waitcnt lgkmcnt(4)\n"
      "v_and_b32 v113, 3, v112\n"
      "v_mfma_f32_16x16x32_bf16 v[152:155], v[40:43], a[208:211], v[152:155]\n"
      "s_waitcnt vmcnt(19)\n"
      "s_barrier\n"
      "ds_read_b128 v[32:35], v29\n"
      "v_mfma_f32_16x16x32_bf16 v[152:155], v[44:47], a[216:219], v[152:155]\n"
      "v_lshrrev_b32 v112, 2, v112\n"
      "v_cmp_gt_u32 vcc, s17, v112\n"
      "v_mfma_f32_16x16x32_bf16 v[156:159], v[40:43], a[212:215], v[156:159]\n"
      "ds_read_b128 v[36:39], v29 offset:1024\n"
      "v_lshl_add_u32 v114, v112, 1, s94\n"
      "v_mfma_f32_16x16x32_bf16 v[156:159], v[44:47], a[220:223], v[156:159]\n"
      "s_waitcnt lgkmcnt(4)\n"
      "v_mul_u32_u24 v115, s95, v112\n"
      "v_mfma_f32_16x16x32_bf16 v[152:155], v[48:51], a[224:227], v[152:155]\n"
      "ds_read_b128 v[40:43], v29 offset:2048\n"
      "v_add_u32 v115, s6, v115\n"
      "v_mfma_f32_16x16x32_bf16 v[152:155], v[52:55], a[232:235], v[152:155]\n"
      "v_cndmask_b32 v114, v115, v114, vcc\n"
      "v_mul_u32_u24 v113, 324, v113\n"
      "v_mfma_f32_16x16x32_bf16 v[156:159], v[48:51], a[228:231], v[156:159]\n"
      "ds_read_b128 v[44:47], v29 offset:3072\n"
      "v_add_u32 v114, v114, v113\n"
      "v_mfma_f32_16x16x32_bf16 v[156:159], v[52:55], a[236:239], v[156:159]\n"
      "s_waitcnt lgkmcnt(4)\n"
      "v_lshl_add_u32 v249, v114, 10, v117\n"
      "v_mfma_f32_16x16x32_bf16 v[152:155], v[56:59], a[240:243], v[152:155]\n"
      "ds_read_b128 v[48:51], v29 offset:4096\n"
      "v_add_u32 v112, 4, v116\n"
      "v_mfma_f32_16x16x32_bf16 v[152:155], v[60:63], a[248:251], v[152:155]\n"
      "v_and_b32 v113, 3, v112\n"
      "v_lshrrev_b32 v112, 2, v112\n"
      "v_mfma_f32_16x16x32_bf16 v[156:159], v[56:59], a[244:247], v[156:159]\n"
      "ds_read_b128 v[52:55], v29 offset:5120\n"
      "v_cmp_gt_u32 vcc, s17, v112\n"
      "v_mfma_f32_16x16x32_bf16 v[156:159], v[60:63], a[252:255], v[156:159]\n"
      "s_waitcnt lgkmcnt(4)\n"
      "buffer_load_dwordx4 v[16:19], v31, s[40:43], s34 offen\n"
      "v_mfma_f32_16x16x32_bf16 v[160:163], v[32:35], a[0:3], 0\n"
      "ds_read_b128 v[56:59], v29 offset:6144\n"
      "v_lshl_add_u32 v114, v112, 1, s94\n"
      "v_mfma_f32_16x16x32_bf16 v[160:163], v[36:39], a[8:11], v[160:163]\n"
      "s_add_u32 m0, s81, 90112\n"
      "s_add_u32 s96, s85, 237568\n"
      "v_mfma_f32_16x16x32_bf16 v[164:167], v[32:35], a[4:7], 0\n"
      "ds_read_b128 v[60:63], v29 offset:7168\n"
      "v_mul_u32_u24 v115, s95, v112\n"
      "v_mfma_f32_16x16x32_bf16 v[164:167], v[36:39], a[12:15], v[164:167]\n"
      "s_waitcnt lgkmcnt(4)\n"
      "buffer_load_dwordx4 v1, s[36:39], s96 offen sc1 lds\n"
      "v_mfma_f32_16x16x32_bf16 v[160:163], v[40:43], a[16:19], v[160:163]\n"
      "ds_read_b128 v[32:35], v29 offset:8192\n"
      "v_add_u32 v115, s6, v115\n"
      "v_mfma_f32_16x16x32_bf16 v[160:163], v[44:47], a[24:27], v[160:163]\n"
      "v_cndmask_b32 v114, v115, v114, vcc\n"
      "v_mul_u32_u24 v113, 324, v113\n"
      "v_mfma_f32_16x16x32_bf16 v[164:167], v[40:43], a[20:23], v[164:167]\n"
      "ds_read_b128 v[36:39], v29 offset:9216\n"
      "v_add_u32 v114, v114, v113\n"
      "v_mfma_f32_16x16x32_bf16 v[164:167], v[44:47], a[28:31], v[164:167]\n"
      "s_waitcnt lgkmcnt(4)\n"
      "buffer_load_dwordx4 v1, s[36:39], s96 offen offset:1024 sc1 lds\n"
      "v_mfma_f32_16x16x32_bf16 v[160:163], v[48:51], a[32:35], v[160:163]\n"
      "ds_read_b128 v[40:43], v29 offset:10240\n"
      "v_cvt_pk_bf16_f32 v108, v152, v153\n"
      "v_mfma_f32_16x16x32_bf16 v[160:163], v[52:55], a[40:43], v[160:163]\n"
      "v_cvt_pk_bf16_f32 v110, v156, v157\n"
      "v_cvt_pk_bf16_f32 v109, v154, v155\n"
      "v_mfma_f32_16x16x32_bf16 v[164:167], v[48:51], a[36:39], v[164:167]\n"
      "ds_read_b128 v[44:47], v29 offset:11264\n"
      "v_cvt_pk_bf16_f32 v111, v158, v159\n"
      "v_mfma_f32_16x16x32_bf16 v[164:167], v[52:55], a[44:47], v[164:167]\n"
      "s_waitcnt lgkmcnt(4)\n"
      "buffer_load_dwordx4 v1, s[36:39], s96 offen offset:2048 sc1 lds\n"
      "v_mfma_f32_16x16x32_bf16 v[160:163], v[56:59], a[48:51], v[160:163]\n"
      "ds_read_b128 v[48:51], v29 offset:12288\n"
      "v_permlane16_swap_b32 v108, v110\n"
      "v_mfma_f32_16x16x32_bf16 v[160:163], v[60:63], a[56:59], v[160:163]\n"
      "v_permlane16_swap_b32 v109, v111\n"
      "buffer_store_dwordx4 v[108:111], v73, s[36:39], s33 offen offset:3072 sc1\n"
      "v_mfma_f32_16x16x32_bf16 v[164:167], v[56:59], a[52:55], v[164:167]\n"
      "ds_read_b128 v[52:55], v29 offset:13312\n"
      "v_lshl_add_u32 v250, v114, 10, v117\n"
      "v_mfma_f32_16x16x32_bf16 v[164:167], v[60:63], a[60:63], v[164:167]\n"
      "s_waitcnt lgkmcnt(4)\n"
      "buffer_load_dwordx4 v1, s[36:39], s96 offen offset:3072 sc1 lds\n"
      "v_mfma_f32_16x16x32_bf16 v[160:163], v[32:35], a[64:67], v[160:163]\n"
      "ds_read_b128 v[56:59], v29 offset:14336\n"
      "v_add_u32 v112, 6, v116\n"
      "v_mfma_f32_16x16x32_bf16 v[160:163], v[36:39], a[72:75], v[160:163]\n"
      "v_and_b32 v113, 3, v112\n"
      "v_lshrrev_b32 v112, 2, v112\n"
      "v_mfma_f32_16x16x32_bf16 v[164:167], v[32:35], a[68:71], v[164:167]\n"
      "ds_read_b128 v[60:63], v29 offset:15360\n"
      "v_cmp_gt_u32 vcc, s17, v112\n"
      "v_mfma_f32_16x16x32_bf16 v[164:167], v[36:39], a[76:79], v[164:167]\n"
      "s_waitcnt lgkmcnt(4)\n"
      "buffer_load_dwordx4 v[20:23], v31, s[40:43], s34 offen offset:384\n"
      "v_mfma_f32_16x16x32_bf16 v[160:163], v[40:43], a[80:83], v[160:163]\n"
      "ds_read_b128 v[32:35], v1\n"
      "v_lshl_add_u32 v114, v112, 1, s94\n"
      "v_mfma_f32_16x16x32_bf16 v[160:163], v[44:47], a[88:91], v[160:163]\n"
      "s_add_u32 m0, s81, 106496\n"
      "s_add_u32 s96, s85, 253952\n"
      "v_mfma_f32_16x16x32_bf16 v[164:167], v[40:43], a[84:87], v[164:167]\n"
      "ds_read_b128 v[36:39], v1 offset:1024\n"
      "v_mul_u32_u24 v115, s95, v112\n"
      "v_mfma_f32_16x16x32_bf16 v[164:167], v[44:47], a[92:95], v[164:167]\n"
      "s_waitcnt lgkmcnt(4)\n"
      "buffer_load_dwordx4 v1, s[36:39], s96 offen sc1 lds\n"
      "v_mfma_f32_16x16x32_bf16 v[160:163], v[48:51], a[96:99], v[160:163]\n"
      "ds_read_b128 v[40:43], v1 offset:2048\n"
      "v_add_u32 v115, s6, v115\n"
      "v_mfma_f32_16x16x32_bf16 v[160:163], v[52:55], a[104:107], v[160:163]\n"
      "v_cndmask_b32 v114, v115, v114, vcc\n"
      "v_mul_u32_u24 v113, 324, v113\n"
      "v_mfma_f32_16x16x32_bf16 v[164:167], v[48:51], a[100:103], v[164:167]\n"
      "ds_read_b128 v[44:47], v1 offset:3072\n"
      "v_add_u32 v114, v114, v113\n"
      "v_mfma_f32_16x16x32_bf16 v[164:167], v[52:55], a[108:111], v[164:167]\n"
      "s_waitcnt lgkmcnt(4)\n"
      "buffer_load_dwordx4 v1, s[36:39], s96 offen offset:1024 sc1 lds\n"
      "v_mfma_f32_16x16x32_bf16 v[160:163], v[56:59], a[112:115], v[160:163]\n"
      "ds_read_b128 v[48:51], v1 offset:4096\n"
      "v_lshl_add_u32 v251, v114, 10, v117\n"
      "v_mfma_f32_16x16x32_bf16 v[160:163], v[60:63], a[120:123], v[160:163]\n"
      "v_add_u32 v112, 8, v116\n"
      "v_and_b32 v113, 3, v112\n"
      "v_mfma_f32_16x16x32_bf16 v[164:167], v[56:59], a[116:119], v[164:167]\n"
      "ds_read_b128 v[52:55], v1 offset:5120\n"
      "v_lshrrev_b32 v112, 2, v112\n"
      "v_mfma_f32_16x16x32_bf16 v[164:167], v[60:63], a[124:127], v[164:167]\n"
      "s_waitcnt lgkmcnt(4)\n"
      "buffer_load_dwordx4 v1, s[36:39], s96 offen offset:2048 sc1 lds\n"
      "v_mfma_f32_16x16x32_bf16 v[160:163], v[32:35], a[128:131], v[160:163]\n"
      "ds_read_b128 v[56:59], v1 offset:6144\n"
      "v_cmp_gt_u32 vcc, s17, v112\n"
      "v_mfma_f32_16x16x32_bf16 v[160:163], v[36:39], a[136:139], v[160:163]\n"
      "v_lshl_add_u32 v114, v112, 1, s94\n"
      "v_mul_u32_u24 v115, s95, v112\n"
      "v_mfma_f32_16x16x32_bf16 v[164:167], v[32:35], a[132:135], v[164:167]\n"
      "ds_read_b128 v[60:63], v1 offset:7168\n"
      "v_add_u32 v115, s6, v115\n"
      "v_mfma_f32_16x16x32_bf16 v[164:167], v[36:39], a[140:143], v[164:167]\n"
      "s_waitcnt lgkmcnt(4)\n"
      "buffer_load_dwordx4 v1, s[36:39], s96 offen offset:3072 sc1 lds\n"
      "v_mfma_f32_16x16x32_bf16 v[160:163], v[40:43], a[144:147], v[160:163]\n"
      "ds_read_b128 v[32:35], v1 offset:8192\n"
      "v_cndmask_b32 v114, v115, v114, vcc\n"
      "v_mfma_f32_16x16x32_bf16 v[160:163], v[44:47], a[152:155], v[160:163]\n"
      "s_add_u32 s93, s4, 27648\n"
      "v_mul_u32_u24 v113, 324, v113\n"
      "v_mfma_f32_16x16x32_bf16 v[164:167], v[40:43], a[148:151], v[164:167]\n"
      "ds_read_b128 v[36:39], v1 offset:9216\n"
      "v_add_u32 v114, v114, v113\n"
      "v_mfma_f32_16x16x32_bf16 v[164:167], v[44:47], a[156:159], v[164:167]\n"
      "s_waitcnt lgkmcnt(4)\n"
      "buffer_load_dword v193, v15, s[52:55], s93 offen\n"
      "v_mfma_f32_16x16x32_bf16 v[160:163], v[48:51], a[160:163], v[160:163]\n"
      "ds_read_b128 v[40:43], v1 offset:10240\n"
      "v_lshl_add_u32 v252, v114, 10, v117\n"
      "v_mfma_f32_16x16x32_bf16 v[160:163], v[52:55], a[168:171], v[160:163]\n"
      "v_add_u32 v112, 10, v116\n"
      "v_and_b32 v113, 3, v112\n"
      "v_mfma_f32_16x16x32_bf16 v[164:167], v[48:51], a[164:167], v[164:167]\n"
      "ds_read_b128 v[44:47], v1 offset:11264\n"
      "v_lshrrev_b32 v112, 2, v112\n"
      "v_mfma_f32_16x16x32_bf16 v[164:167], v[52:55], a[172:175], v[164:167]\n"
      "s_waitcnt lgkmcnt(4)\n"
      "v_cmp_gt_u32 vcc, s17, v112\n"
      "v_mfma_f32_16x16x32_bf16 v[160:163], v[56:59], a[176:179], v[160:163]\n"
      "ds_read_b128 v[48:51], v1 offset:12288\n"
      "v_lshl_add_u32 v114, v112, 1, s94\n"
      "v_mfma_f32_16x16x32_bf16 v[160:163], v[60:63], a[184:187], v[160:163]\n"
      "v_mul_u32_u24 v115, s95, v112\n"
      "v_add_u32 v115, s6, v115\n"
      "v_mfma_f32_16x16x32_bf16 v[164:167], v[56:59], a[180:183], v[164:167]\n"
      "ds_read_b128 v[52:55], v1 offset:13312\n"
      "v_cndmask_b32 v114, v115, v114, vcc\n"
      "v_mfma_f32_16x16x32_bf16 v[164:167], v[60:63], a[188:191], v[164:167]\n"
      "s_waitcnt lgkmcnt(4)\n"
      "v_mul_u32_u24 v113, 324, v113\n"
      "v_mfma_f32_16x16x32_bf16 v[160:163], v[32:35], a[192:195], v[160:163]\n"
      "ds_read_b128 v[56:59], v1 offset:14336\n"
      "v_add_u32 v114, v114, v113\n"
      "v_mfma_f32_16x16x32_bf16 v[160:163], v[36:39], a[200:203], v[160:163]\n"
      "v_lshl_add_u32 v253, v114, 10, v117\n"
      "s_lshl_b32 s90, s72, 11\n"
      "v_mfma_f32_16x16x32_bf16 v[164:167], v[32:35], a[196:199], v[164:167]\n"
      "ds_read_b128 v[60:63], v1 offset:15360\n"
      "s_add_u32 s90, s90, 0x24400\n"
      "v_mfma_f32_16x16x32_bf16 v[164:167], v[36:39], a[204:207], v[164:167]\n"
      "s_waitcnt lgkmcnt(4)\n"
      "v_add_u32 v254, s90, v1\n"
      "v_mfma_f32_16x16x32_bf16 v[160:163], v[40:43], a[208:211], v[160:163]\n"
      "s_waitcnt vmcnt(22)\n"
      "s_barrier\n"
      "ds_read_b128 v[32:35], v1 offset:16384\n"
      "v_mfma_f32_16x16x32_bf16 v[160:163], v[44:47], a[216:219], v[160:163]\n"
      "v_bfe_u32 v255, v2, 4, 1\n"
      "v_lshlrev_b32 v255, 10, v255\n"
      "v_mfma_f32_16x16x32_bf16 v[164:167], v[40:43], a[212:215], v[164:167]\n"
      "ds_read_b128 v[36:39], v1 offset:17408\n"
      "v_lshrrev_b32 v112, 5, v2\n"
      "v_mfma_f32_16x16x32_bf16 v[164:167], v[44:47], a[220:223], v[164:167]\n"
      "s_waitcnt lgkmcnt(4)\n"
      "v_lshl_add_u32 v255, v112, 5, v255\n"
      "v_mfma_f32_16x16x32_bf16 v[160:163], v[48:51], a[224:227], v[160:163]\n"
      "ds_read_b128 v[40:43], v1 offset:18432\n"
      "v_bfe_u32 v112, v2, 2, 2\n"
      "v_mfma_f32_16x16x32_bf16 v[160:163], v[52:55], a[232:235], v[160:163]\n"
      "v_lshl_add_u32 v255, v112, 7, v255\n"
      "v_and_b32 v112, 3, v2\n"
      "v_mfma_f32_16x16x32_bf16 v[164:167], v[48:51], a[228:231], v[164:167]\n"
      "ds_read_b128 v[44:47], v1 offset:19456\n"
      "v_lshl_add_u32 v255, v112, 3, v255\n"
      "v_mfma_f32_16x16x32_bf16 v[164:167], v[52:55], a[236:239], v[164:167]\n"
      "s_waitcnt lgkmcnt(4)\n"
      "v_add_u32 v255, s90, v255\n"
      "v_mfma_f32_16x16x32_bf16 v[160:163], v[56:59], a[240:243], v[160:163]\n"
      "ds_read_b128 v[48:51], v1 offset:20480\n"
      "v_mfma_f32_16x16x32_bf16 v[160:163], v[60:63], a[248:251], v[160:163]\n"
      "v_mfma_f32_16x16x32_bf16 v[164:167], v[56:59], a[244:247], v[164:167]\n"
      "ds_read_b128 v[52:55], v1 offset:21504\n"
      "v_mfma_f32_16x16x32_bf16 v[164:167], v[60:63], a[252:255], v[164:167]\n"
      "s_waitcnt lgkmcnt(4)\n"
      "v_mfma_f32_16x16x32_bf16 v[168:171], v[32:35], a[0:3], 0\n"
      "ds_read_b128 v[56:59], v1 offset:22528\n"
      "v_mfma_f32_16x16x32_bf16 v[168:171], v[36:39], a[8:11], v[168:171]\n"
      "v_mfma_f32_16x16x32_bf16 v[172:175], v[32:35], a[4:7], 0\n"
      "ds_read_b128 v[60:63], v1 offset:23552\n"
      "v_mfma_f32_16x16x32_bf16 v[172:175], v[36:39], a[12:15], v[172:175]\n"
      "s_waitcnt lgkmcnt(4)\n"
      "v_mfma_f32_16x16x32_bf16 v[168:171], v[40:43], a[16:19], v[168:171]\n"
      "ds_read_b128 v[32:35], v1 offset:24576\n"
      "v_mfma_f32_16x16x32_bf16 v[168:171], v[44:47], a[24:27], v[168:171]\n"
      "v_mfma_f32_16x16x32_bf16 v[172:175], v[40:43], a[20:23], v[172:175]\n"
      "ds_read_b128 v[36:39], v1 offset:25600\n"
      "v_mfma_f32_16x16x32_bf16 v[172:175], v[44:47], a[28:31], v[172:175]\n"
      "s_waitcnt lgkmcnt(4)\n"
      "v_mfma_f32_16x16x32_bf16 v[168:171], v[48:51], a[32:35], v[168:171]\n"
      "ds_read_b128 v[40:43], v1 offset:26624\n"
      "v_cvt_pk_bf16_f32 v96, v160, v161\n"
      "v_mfma_f32_16x16x32_bf16 v[168:171], v[52:55], a[40:43], v[168:171]\n"
      "v_cvt_pk_bf16_f32 v98, v164, v165\n"
      "v_cvt_pk_bf16_f32 v97, v162, v163\n"
      "v_mfma_f32_16x16x32_bf16 v[172:175], v[48:51], a[36:39], v[172:175]\n"
      "ds_read_b128 v[44:47], v1 offset:27648\n"
      "v_cvt_pk_bf16_f32 v99, v166, v167\n"
      "v_mfma_f32_16x16x32_bf16 v[172:175], v[52:55], a[44:47], v[172:175]\n"
      "s_waitcnt lgkmcnt(4)\n"
      "v_permlane16_swap_b32 v96, v98\n"
      "v_mfma_f32_16x16x32_bf16 v[168:171], v[56:59], a[48:51], v[168:171]\n"
      "ds_read_b128 v[48:51], v1 offset:28672\n"
      "v_permlane16_swap_b32 v97, v99\n"
      "v_mfma_f32_16x16x32_bf16 v[168:171], v[60:63], a[56:59], v[168:171]\n"
      "buffer_store_dwordx4 v[96:99], v73, s[36:39], s18 offen sc1\n"
      "v_mfma_f32_16x16x32_bf16 v[172:175], v[56:59], a[52:55], v[172:175]\n"
      "ds_read_b128 v[52:55], v1 offset:29696\n"
      "v_mfma_f32_16x16x32_bf16 v[172:175], v[60:63], a[60:63], v[172:175]\n"
      "s_waitcnt lgkmcnt(4)\n"
      "buffer_load_dwordx4 v[24:27], v31, s[40:43], s34 offen offset:768\n"
      "v_mfma_f32_16x16x32_bf16 v[168:171], v[32:35], a[64:67], v[168:171]\n"
      "ds_read_b128 v[56:59], v1 offset:30720\n"
      "v_mfma_f32_16x16x32_bf16 v[168:171], v[36:39], a[72:75], v[168:171]\n"
      "s_add_u32 m0, s81, 122880\n"
      "s_add_u32 s96, s85, 270336\n"
      "v_mfma_f32_16x16x32_bf16 v[172:175], v[32:35], a[68:71], v[172:175]\n"
      "ds_read_b128 v[60:63], v1 offset:31744\n"
      "v_mfma_f32_16x16x32_bf16 v[172:175], v[36:39], a[76:79], v[172:175]\n"
      "s_waitcnt lgkmcnt(4)\n"
      "buffer_load_dwordx4 v1, s[36:39], s96 offen sc1 lds\n"
      "v_mfma_f32_16x16x32_bf16 v[168:171], v[40:43], a[80:83], v[168:171]\n"
      "ds_read_b128 v[32:35], v1 offset:32768\n"
      "v_mfma_f32_16x16x32_bf16 v[168:171], v[44:47], a[88:91], v[168:171]\n"
      "v_mfma_f32_16x16x32_bf16 v[172:175], v[40:43], a[84:87], v[172:175]\n"
      "ds_read_b128 v[36:39], v1 offset:33792\n"
      "v_mfma_f32_16x16x32_bf16 v[172:175], v[44:47], a[92:95], v[172:175]\n"
      "s_waitcnt lgkmcnt(4)\n"
      "buffer_load_dwordx4 v1, s[36:39], s96 offen offset:1024 sc1 lds\n"
      "v_mfma_f32_16x16x32_bf16 v[168:171], v[48:51], a[96:99], v[168:171]\n"
      "ds_read_b128 v[40:43], v1 offset:34816\n"
      "v_mfma_f32_16x16x32_bf16 v[168:171], v[52:55], a[104:107], v[168:171]\n"
      "v_mfma_f32_16x16x32_bf16 v[172:175], v[48:51], a[100:103], v[172:175]\n"
      "ds_read_b128 v[44:47], v1 offset:35840\n"
      "v_mfma_f32_16x16x32_bf16 v[172:175], v[52:55], a[108:111], v[172:175]\n"
      "s_waitcnt lgkmcnt(4)\n"
      "buffer_load_dwordx4 v1, s[36:39], s96 offen offset:2048 sc1 lds\n"
      "v_mfma_f32_16x16x32_bf16 v[168:171], v[56:59], a[112:115], v[168:171]\n"
      "ds_read_b128 v[48:51], v1 offset:36864\n"
      "v_mfma_f32_16x16x32_bf16 v[168:171], v[60:63], a[120:123], v[168:171]\n"
      "v_mfma_f32_16x16x32_bf16 v[172:175], v[56:59], a[116:119], v[172:175]\n"
      "ds_read_b128 v[52:55], v1 offset:37888\n"
      "v_mfma_f32_16x16x32_bf16 v[172:175], v[60:63], a[124:127], v[172:175]\n"
      "s_waitcnt lgkmcnt(4)\n"
      "buffer_load_dwordx4 v1, s[36:39], s96 offen offset:3072 sc1 lds\n"
      "v_mfma_f32_16x16x32_bf16 v[168:171], v[32:35], a[128:131], v[168:171]\n"
      "ds_read_b128 v[56:59], v1 offset:38912\n"
      "v_mfma_f32_16x16x32_bf16 v[168:171], v[36:39], a[136:139], v[168:171]\n"
      "s_add_u32 s93, s4, 55296\n"
      "v_mfma_f32_16x16x32_bf16 v[172:175], v[32:35], a[132:135], v[172:175]\n"
      "ds_read_b128 v[60:63], v1 offset:39936\n"
      "v_mfma_f32_16x16x32_bf16 v[172:175], v[36:39], a[140:143], v[172:175]\n"
      "s_waitcnt lgkmcnt(4)\n"
      "buffer_load_dword v194, v15, s[52:55], s93 offen\n"
      "v_mfma_f32_16x16x32_bf16 v[168:171], v[40:43], a[144:147], v[168:171]\n"
      "ds_read_b128 v[32:35], v1 offset:40960\n"
      "v_mfma_f32_16x16x32_bf16 v[168:171], v[44:47], a[152:155], v[168:171]\n"
      "v_mfma_f32_16x16x32_bf16 v[172:175], v[40:43], a[148:151], v[172:175]\n"
      "ds_read_b128 v[36:39], v1 offset:41984\n"
      "v_mfma_f32_16x16x32_bf16 v[172:175], v[44:47], a[156:159], v[172:175]\n"
      "s_waitcnt lgkmcnt(4)\n"
      "v_mfma_f32_16x16x32_bf16 v[168:171], v[48:51], a[160:163], v[168:171]\n"
      "ds_read_b128 v[40:43], v1 offset:43008\n"
      "v_mfma_f32_16x16x32_bf16 v[168:171], v[52:55], a[168:171], v[168:171]\n"
      "v_mfma_f32_16x16x32_bf16 v[172:175], v[48:51], a[164:167], v[172:175]\n"
      "ds_read_b128 v[44:47], v1 offset:44032\n"
      "v_mfma_f32_16x16x32_bf16 v[172:175], v[52:55], a[172:175], v[172:175]\n"
      "s_waitcnt lgkmcnt(4)\n"
      "v_mfma_f32_16x16x32_bf16 v[168:171], v[56:59], a[176:179], v[168:171]\n"
      "ds_read_b128 v[48:51], v1 offset:45056\n"
      "v_mfma_f32_16x16x32_bf16 v[168:171], v[60:63], a[184:187], v[168:171]\n"
      "v_mfma_f32_16x16x32_bf16 v[172:175], v[56:59], a[180:183], v[172:175]\n"
      "ds_read_b128 v[52:55], v1 offset:46080\n"
      "v_mfma_f32_16x16x32_bf16 v[172:175], v[60:63], a[188:191], v[172:175]\n"
      "s_waitcnt lgkmcnt(4)\n"
      "v_mfma_f32_16x16x32_bf16 v[168:171], v[32:35], a[192:195], v[168:171]\n"
      "ds_read_b128 v[56:59], v1 offset:47104\n"
      "v_mfma_f32_16x16x32_bf16 v[168:171], v[36:39], a[200:203], v[168:171]\n"
      "v_mfma_f32_16x16x32_bf16 v[172:175], v[32:35], a[196:199], v[172:175]\n"
      "ds_read_b128 v[60:63], v1 offset:48128\n"
      "v_mfma_f32_16x16x32_bf16 v[172:175], v[36:39], a[204:207], v[172:175]\n"
      "s_waitcnt lgkmcnt(4)\n"
      "v_mfma_f32_16x16x32_bf16 v[168:171], v[40:43], a[208:211], v[168:171]\n"
      "s_waitcnt vmcnt(20)\n"
      "s_barrier\n"
      "ds_read_b128 v[32:35], v1 offset:49152\n"
      "v_mfma_f32_16x16x32_bf16 v[168:171], v[44:47], a[216:219], v[168:171]\n"
      "v_mfma_f32_16x16x32_bf16 v[172:175], v[40:43], a[212:215], v[172:175]\n"
      "ds_read_b128 v[36:39], v1 offset:50176\n"
      "v_mfma_f32_16x16x32_bf16 v[172:175], v[44:47], a[220:223], v[172:175]\n"
      "s_waitcnt lgkmcnt(4)\n"
      "v_mfma_f32_16x16x32_bf16 v[168:171], v[48:51], a[224:227], v[168:171]\n"
      "ds_read_b128 v[40:43], v1 offset:51200\n"
      "v_mfma_f32_16x16x32_bf16 v[168:171], v[52:55], a[232:235], v[168:171]\n"
      "v_mfma_f32_16x16x32_bf16 v[172:175], v[48:51], a[228:231], v[172:175]\n"
      "ds_read_b128 v[44:47], v1 offset:52224\n"
      "v_mfma_f32_16x16x32_bf16 v[172:175], v[52:55], a[236:239], v[172:175]\n"
      "s_waitcnt lgkmcnt(4)\n"
      "v_mfma_f32_16x16x32_bf16 v[168:171], v[56:59], a[240:243], v[168:171]\n"
      "ds_read_b128 v[48:51], v1 offset:53248\n"
      "v_mfma_f32_16x16x32_bf16 v[168:171], v[60:63], a[248:251], v[168:171]\n"
      "v_mfma_f32_16x16x32_bf16 v[172:175], v[56:59], a[244:247], v[172:175]\n"
      "ds_read_b128 v[52:55], v1 offset:54272\n"
      "v_mfma_f32_16x16x32_bf16 v[172:175], v[60:63], a[252:255], v[172:175]\n"
      "s_waitcnt lgkmcnt(4)\n"
      "v_mfma_f32_16x16x32_bf16 v[176:179], v[32:35], a[0:3], 0\n"
      "ds_read_b128 v[56:59], v1 offset:55296\n"
      "v_mfma_f32_16x16x32_bf16 v[176:179], v[36:39], a[8:11], v[176:179]\n"
      "v_mfma_f32_16x16x32_bf16 v[180:183], v[32:35], a[4:7], 0\n"
      "ds_read_b128 v[60:63], v1 offset:56320\n"
      "v_mfma_f32_16x16x32_bf16 v[180:183], v[36:39], a[12:15], v[180:183]\n"
      "s_waitcnt lgkmcnt(4)\n"
      "v_mfma_f32_16x16x32_bf16 v[176:179], v[40:43], a[16:19], v[176:179]\n"
      "ds_read_b128 v[32:35], v1 offset:57344\n"
      "v_mfma_f32_16x16x32_bf16 v[176:179], v[44:47], a[24:27], v[176:179]\n"
      "v_mfma_f32_16x16x32_bf16 v[180:183], v[40:43], a[20:23], v[180:183]\n"
      "ds_read_b128 v[36:39], v1 offset:58368\n"
      "v_mfma_f32_16x16x32_bf16 v[180:183], v[44:47], a[28:31], v[180:183]\n"
      "s_waitcnt lgkmcnt(4)\n"
      "v_mfma_f32_16x16x32_bf16 v[176:179], v[48:51], a[32:35], v[176:179]\n"
      "ds_read_b128 v[40:43], v1 offset:59392\n"
      "v_cvt_pk_bf16_f32 v100, v168, v169\n"
      "v_mfma_f32_16x16x32_bf16 v[176:179], v[52:55], a[40:43], v[176:179]\n"
      "s_add_u32 m0, s79, 139264\n"
      "s_add_u32 s96, s83, 286720\n"
      "v_mfma_f32_16x16x32_bf16 v[180:183], v[48:51], a[36:39], v[180:183]\n"
      "ds_read_b128 v[44:47], v1 offset:60416\n"
      "v_cvt_pk_bf16_f32 v102, v172, v173\n"
      "v_mfma_f32_16x16x32_bf16 v[180:183], v[52:55], a[44:47], v[180:183]\n"
      "s_waitcnt lgkmcnt(4)\n"
      "buffer_load_dwordx4 v1, s[36:39], s96 offen sc1 lds\n"
      "v_mfma_f32_16x16x32_bf16 v[176:179], v[56:59], a[48:51], v[176:179]\n"
      "ds_read_b128 v[48:51], v1 offset:61440\n"
      "v_cvt_pk_bf16_f32 v101, v170, v171\n"
      "v_mfma_f32_16x16x32_bf16 v[176:179], v[60:63], a[56:59], v[176:179]\n"
      "v_cvt_pk_bf16_f32 v103, v174, v175\n"
      "v_permlane16_swap_b32 v100, v102\n"
      "v_mfma_f32_16x16x32_bf16 v[180:183], v[56:59], a[52:55], v[180:183]\n"
      "ds_read_b128 v[52:55], v1 offset:62464\n"
      "v_permlane16_swap_b32 v101, v103\n"
      "v_mfma_f32_16x16x32_bf16 v[180:183], v[60:63], a[60:63], v[180:183]\n"
      "s_waitcnt lgkmcnt(4)\n"
      "buffer_load_dwordx4 v1, s[36:39], s96 offen offset:1024 sc1 lds\n"
      "v_mfma_f32_16x16x32_bf16 v[176:179], v[32:35], a[64:67], v[176:179]\n"
      "ds_read_b128 v[56:59], v1 offset:63488\n"
      "buffer_store_dwordx4 v[100:103], v73, s[36:39], s18 offen offset:1024 sc1\n"
      "v_mfma_f32_16x16x32_bf16 v[176:179], v[36:39], a[72:75], v[176:179]\n"
      "s_getpc_b64 s[90:91]\n"
      "Lpc_10_%=:\n"
      "s_sub_u32 s90, s90, Lpc_10_%=-Lstart_%=\n"
      "s_subb_u32 s91, s91, 0\n"
      "global_load_dword v199, v79, s[90:91]\n"
      "v_mfma_f32_16x16x32_bf16 v[180:183], v[32:35], a[68:71], v[180:183]\n"
      "ds_read_b128 v[60:63], v1 offset:64512\n"
      "v_mfma_f32_16x16x32_bf16 v[180:183], v[36:39], a[76:79], v[180:183]\n"
      "s_waitcnt lgkmcnt(4)\n"
      "v_mfma_f32_16x16x32_bf16 v[176:179], v[40:43], a[80:83], v[176:179]\n"
      "ds_read_b128 v[32:35], v28\n"
      "v_mfma_f32_16x16x32_bf16 v[176:179], v[44:47], a[88:91], v[176:179]\n"
      "v_mfma_f32_16x16x32_bf16 v[180:183], v[40:43], a[84:87], v[180:183]\n"
      "ds_read_b128 v[36:39], v28 offset:1024\n"
      "v_mfma_f32_16x16x32_bf16 v[180:183], v[44:47], a[92:95], v[180:183]\n"
      "s_waitcnt lgkmcnt(4)\n"
      "v_mfma_f32_16x16x32_bf16 v[176:179], v[48:51], a[96:99], v[176:179]\n"
      "ds_read_b128 v[40:43], v28 offset:2048\n"
      "s_add_u32 s93, s57, 0\n"
      "v_mfma_f32_16x16x32_bf16 v[176:179], v[52:55], a[104:107], v[176:179]\n"
      "buffer_load_dwordx4 v[224:227], v77, s[60:63], s93 offen nt\n"
      "v_mfma_f32_16x16x32_bf16 v[180:183], v[48:51], a[100:103], v[180:183]\n"
      "ds_read_b128 v[44:47], v28 offset:3072\n"
      "v_mfma_f32_16x16x32_bf16 v[180:183], v[52:55], a[108:111], v[180:183]\n"
      "s_waitcnt lgkmcnt(4)\n"
      "v_mfma_f32_16x16x32_bf16 v[176:179], v[56:59], a[112:115], v[176:179]\n"
      "ds_read_b128 v[48:51], v28 offset:4096\n"
      "v_mfma_f32_16x16x32_bf16 v[176:179], v[60:63], a[120:123], v[176:179]\n"
      "v_mfma_f32_16x16x32_bf16 v[180:183], v[56:59], a[116:119], v[180:183]\n"
      "ds_read_b128 v[52:55], v28 offset:5120\n"
      "v_mfma_f32_16x16x32_bf16 v[180:183], v[60:63], a[124:127], v[180:183]\n"
      "s_waitcnt lgkmcnt(4)\n"
      "v_mfma_f32_16x16x32_bf16 v[176:179], v[32:35], a[128:131], v[176:179]\n"
      "ds_read_b128 v[56:59], v28 offset:6144\n"
      "v_mfma_f32_16x16x32_bf16 v[176:179], v[36:39], a[136:139], v[176:179]\n"
      "v_mfma_f32_16x16x32_bf16 v[180:183], v[32:35], a[132:135], v[180:183]\n"
      "ds_read_b128 v[60:63], v28 offset:7168\n"
      "v_mfma_f32_16x16x32_bf16 v[180:183], v[36:39], a[140:143], v[180:183]\n"
      "s_waitcnt lgkmcnt(4)\n"
      "buffer_load_dwordx4 v[232:235], v77, s[60:63], s93 offen offset:128 nt\n"
      "v_mfma_f32_16x16x32_bf16 v[176:179], v[40:43], a[144:147], v[176:179]\n"
      "ds_read_b128 v[32:35], v28 offset:8192\n"
      "v_mfma_f32_16x16x32_bf16 v[176:179], v[44:47], a[152:155], v[176:179]\n"
      "v_mfma_f32_16x16x32_bf16 v[180:183], v[40:43], a[148:151], v[180:183]\n"
      "ds_read_b128 v[36:39], v28 offset:9216\n"
      "v_mfma_f32_16x16x32_bf16 v[180:183], v[44:47], a[156:159], v[180:183]\n"
      "s_waitcnt lgkmcnt(4)\n"
      "v_mfma_f32_16x16x32_bf16 v[176:179], v[48:51], a[160:163], v[176:179]\n"
      "ds_read_b128 v[40:43], v28 offset:10240\n"
      "v_mfma_f32_16x16x32_bf16 v[176:179], v[52:55], a[168:171], v[176:179]\n"
      "v_mfma_f32_16x16x32_bf16 v[180:183], v[48:51], a[164:167], v[180:183]\n"
      "ds_read_b128 v[44:47], v28 offset:11264\n"
      "v_mfma_f32_16x16x32_bf16 v[180:183], v[52:55], a[172:175], v[180:183]\n"
      "s_waitcnt lgkmcnt(4)\n"
      "v_mfma_f32_16x16x32_bf16 v[176:179], v[56:59], a[176:179], v[176:179]\n"
      "ds_read_b128 v[48:51], v28 offset:12288\n"
      "v_mfma_f32_16x16x32_bf16 v[176:179], v[60:63], a[184:187], v[176:179]\n"
      "v_mfma_f32_16x16x32_bf16 v[180:183], v[56:59], a[180:183], v[180:183]\n"
      "ds_read_b128 v[52:55], v28 offset:13312\n"
      "v_mfma_f32_16x16x32_bf16 v[180:183], v[60:63], a[188:191], v[180:183]\n"
      "s_waitcnt lgkmcnt(4)\n"
      "buffer_load_dwordx4 v[240:243], v77, s[60:63], s93 offen offset:256 nt\n"
      "v_mfma_f32_16x16x32_bf16 v[176:179], v[32:35], a[192:195], v[176:179]\n"
      "ds_read_b128 v[56:59], v28 offset:14336\n"
      "v_mfma_f32_16x16x32_bf16 v[176:179], v[36:39], a[200:203], v[176:179]\n"
      "v_mfma_f32_16x16x32_bf16 v[180:183], v[32:35], a[196:199], v[180:183]\n"
      "ds_read_b128 v[60:63], v28 offset:15360\n"
      "v_mfma_f32_16x16x32_bf16 v[180:183], v[36:39], a[204:207], v[180:183]\n"
      "s_waitcnt lgkmcnt(4)\n"
      "v_mfma_f32_16x16x32_bf16 v[176:179], v[40:43], a[208:211], v[176:179]\n"
      "s_waitcnt vmcnt(15)\n"
      "s_barrier\n"
      "ds_read_b128 v[32:35], v28 offset:16384\n"
      "v_mfma_f32_16x16x32_bf16 v[176:179], v[44:47], a[216:219], v[176:179]\n"
      "v_mfma_f32_16x16x32_bf16 v[180:183], v[40:43], a[212:215], v[180:183]\n"
      "ds_read_b128 v[36:39], v28 offset:17408\n"
      "v_mfma_f32_16x16x32_bf16 v[180:183], v[44:47], a[220:223], v[180:183]\n"
      "s_waitcnt lgkmcnt(4)\n"
      "v_mfma_f32_16x16x32_bf16 v[176:179], v[48:51], a[224:227], v[176:179]\n"
      "ds_read_b128 v[40:43], v28 offset:18432\n"
      "v_mfma_f32_16x16x32_bf16 v[176:179], v[52:55], a[232:235], v[176:179]\n"
      "s_add_u32 s93, s57, 9216\n"
      "v_mfma_f32_16x16x32_bf16 v[180:183], v[48:51], a[228:231], v[180:183]\n"
      "ds_read_b128 v[44:47], v28 offset:19456\n"
      "v_mfma_f32_16x16x32_bf16 v[180:183], v[52:55], a[236:239], v[180:183]\n"
      "s_waitcnt lgkmcnt(4)\n"
      "buffer_load_dwordx4 v[228:231], v77, s[60:63], s93 offen nt\n"
      "v_mfma_f32_16x16x32_bf16 v[176:179], v[56:59], a[240:243], v[176:179]\n"
      "ds_read_b128 v[48:51], v28 offset:20480\n"
      "v_mfma_f32_16x16x32_bf16 v[176:179], v[60:63], a[248:251], v[176:179]\n"
      "v_mfma_f32_16x16x32_bf16 v[180:183], v[56:59], a[244:247], v[180:183]\n"
      "ds_read_b128 v[52:55], v28 offset:21504\n"
      "v_mfma_f32_16x16x32_bf16 v[180:183], v[60:63], a[252:255], v[180:183]\n"
      "s_waitcnt lgkmcnt(4)\n"
      "v_mfma_f32_16x16x32_bf16 v[184:187], v[32:35], a[0:3], 0\n"
      "ds_read_b128 v[56:59], v28 offset:22528\n"
      "v_mfma_f32_16x16x32_bf16 v[184:187], v[36:39], a[8:11], v[184:187]\n"
      "Lpad4_%=:\n"
      "s_cbranch_execz Lret_%=\n"
      "s_lshl_b32 s95, s72, 15\n"
      "s_add_u32 s95, s95, 0x60000\n"
      "v_mfma_f32_16x16x32_bf16 v[188:191], v[32:35], a[4:7], 0\n"
      "ds_read_b128 v[60:63], v28 offset:23552\n"
      "s_add_u32 s95, s95, s7\n"
      "v_mfma_f32_16x16x32_bf16 v[188:191], v[36:39], a[12:15], v[188:191]\n"
      "s_waitcnt lgkmcnt(4)\n"
      "v_mfma_f32_16x16x32_bf16 v[184:187], v[40:43], a[16:19], v[184:187]\n"
      "ds_read_b128 v[32:35], v28 offset:24576\n"
      "v_mfma_f32_16x16x32_bf16 v[184:187], v[44:47], a[24:27], v[184:187]\n"
      "s_waitcnt vmcnt(5)\n"
      "buffer_store_dwordx2 v[74:75], v76, s[36:39], s95 offen sc1\n"
      "v_mfma_f32_16x16x32_bf16 v[188:191], v[40:43], a[20:23], v[188:191]\n"
      "ds_read_b128 v[36:39], v28 offset:25600\n"
      "v_mfma_f32_16x16x32_bf16 v[188:191], v[44:47], a[28:31], v[188:191]\n"
      "s_waitcnt lgkmcnt(4)\n"
      "buffer_load_dwordx4 v[236:239], v77, s[60:63], s93 offen offset:128 nt\n"
      "v_mfma_f32_16x16x32_bf16 v[184:187], v[48:51], a[32:35], v[184:187]\n"
      "ds_read_b128 v[40:43], v28 offset:26624\n"
      "v_cvt_pk_bf16_f32 v104, v176, v177\n"
      "v_mfma_f32_16x16x32_bf16 v[184:187], v[52:55], a[40:43], v[184:187]\n"
      "v_cvt_pk_bf16_f32 v106, v180, v181\n"
      "v_cvt_pk_bf16_f32 v105, v178, v179\n"
      "v_mfma_f32_16x16x32_bf16 v[188:191], v[48:51], a[36:39], v[188:191]\n"
      "ds_read_b128 v[44:47], v28 offset:27648\n"
      "v_cvt_pk_bf16_f32 v107, v182, v183\n"
      "v_mfma_f32_16x16x32_bf16 v[188:191], v[52:55], a[44:47], v[188:191]\n"
      "s_waitcnt lgkmcnt(4)\n"
      "v_permlane16_swap_b32 v104, v106\n"
      "v_mfma_f32_16x16x32_bf16 v[184:187], v[56:59], a[48:51], v[184:187]\n"
      "ds_read_b128 v[48:51], v28 offset:28672\n"
      "v_permlane16_swap_b32 v105, v107\n"
      "v_mfma_f32_16x16x32_bf16 v[184:187], v[60:63], a[56:59], v[184:187]\n"
      "v_min_u32 v104, -2, v104\n"
      "v_min_u32 v106, -2, v106\n"
      "v_mfma_f32_16x16x32_bf16 v[188:191], v[56:59], a[52:55], v[188:191]\n"
      "ds_read_b128 v[52:55], v28 offset:29696\n"
      "v_min_u32 v105, -2, v105\n"
      "v_mfma_f32_16x16x32_bf16 v[188:191], v[60:63], a[60:63], v[188:191]\n"
      "s_waitcnt lgkmcnt(4)\n"
      "v_min_u32 v107, -2, v107\n"
      "v_mfma_f32_16x16x32_bf16 v[184:187], v[32:35], a[64:67], v[184:187]\n"
      "ds_read_b128 v[56:59], v28 offset:30720\n"
      "buffer_store_dwordx4 v[104:107], v73, s[36:39], s18 offen offset:2048 sc1\n"
      "v_mfma_f32_16x16x32_bf16 v[184:187], v[36:39], a[72:75], v[184:187]\n"
      "v_mfma_f32_16x16x32_bf16 v[188:191], v[32:35], a[68:71], v[188:191]\n"
      "ds_read_b128 v[60:63], v28 offset:31744\n"
      "v_mfma_f32_16x16x32_bf16 v[188:191], v[36:39], a[76:79], v[188:191]\n"
      "s_waitcnt lgkmcnt(4)\n"
      "buffer_load_dwordx4 v[244:247], v77, s[60:63], s93 offen offset:256 nt\n"
      "v_mfma_f32_16x16x32_bf16 v[184:187], v[40:43], a[80:83], v[184:187]\n"
      "ds_read_b128 v[32:35], v28 offset:32768\n"
      "v_mfma_f32_16x16x32_bf16 v[184:187], v[44:47], a[88:91], v[184:187]\n"
      "v_mfma_f32_16x16x32_bf16 v[188:191], v[40:43], a[84:87], v[188:191]\n"
      "ds_read_b128 v[36:39], v28 offset:33792\n"
      "v_mfma_f32_16x16x32_bf16 v[188:191], v[44:47], a[92:95], v[188:191]\n"
      "s_waitcnt lgkmcnt(4)\n"
      "v_mfma_f32_16x16x32_bf16 v[184:187], v[48:51], a[96:99], v[184:187]\n"
      "ds_read_b128 v[40:43], v28 offset:34816\n"
      "v_mfma_f32_16x16x32_bf16 v[184:187], v[52:55], a[104:107], v[184:187]\n"
      "v_mfma_f32_16x16x32_bf16 v[188:191], v[48:51], a[100:103], v[188:191]\n"
      "ds_read_b128 v[44:47], v28 offset:35840\n"
      "v_mfma_f32_16x16x32_bf16 v[188:191], v[52:55], a[108:111], v[188:191]\n"
      "s_waitcnt lgkmcnt(4)\n"
      "v_mfma_f32_16x16x32_bf16 v[184:187], v[56:59], a[112:115], v[184:187]\n"
      "ds_read_b128 v[48:51], v28 offset:36864\n"
      "v_mfma_f32_16x16x32_bf16 v[184:187], v[60:63], a[120:123], v[184:187]\n"
      "s_add_u32 s93, s11, 0\n"
      "v_mfma_f32_16x16x32_bf16 v[188:191], v[56:59], a[116:119], v[188:191]\n"
      "ds_read_b128 v[52:55], v28 offset:37888\n"
      "v_mfma_f32_16x16x32_bf16 v[188:191], v[60:63], a[124:127], v[188:191]\n"
      "s_waitcnt lgkmcnt(4)\n"
      "buffer_load_dwordx4 v[88:91], v198, s[64:67], s93 offen\n"
      "v_mfma_f32_16x16x32_bf16 v[184:187], v[32:35], a[128:131], v[184:187]\n"
      "ds_read_b128 v[56:59], v28 offset:38912\n"
      "v_mfma_f32_16x16x32_bf16 v[184:187], v[36:39], a[136:139], v[184:187]\n"
      "s_add_u32 s93, s11, 73728\n"
      "v_mfma_f32_16x16x32_bf16 v[188:191], v[32:35], a[132:135], v[188:191]\n"
      "ds_read_b128 v[60:63], v28 offset:39936\n"
      "v_mfma_f32_16x16x32_bf16 v[188:191], v[36:39], a[140:143], v[188:191]\n"
      "s_waitcnt lgkmcnt(4)\n"
      "buffer_load_dwordx4 v[92:95], v198, s[64:67], s93 offen\n"
      "v_mfma_f32_16x16x32_bf16 v[184:187], v[40:43], a[144:147], v[184:187]\n"
      "ds_read_b128 v[32:35], v28 offset:40960\n"
      "v_mfma_f32_16x16x32_bf16 v[184:187], v[44:47], a[152:155], v[184:187]\n"
      "v_mfma_f32_16x16x32_bf16 v[188:191], v[40:43], a[148:151], v[188:191]\n"
      "ds_read_b128 v[36:39], v28 offset:41984\n"
      "v_mfma_f32_16x16x32_bf16 v[188:191], v[44:47], a[156:159], v[188:191]\n"
      "s_waitcnt lgkmcnt(4)\n"
      "v_mfma_f32_16x16x32_bf16 v[184:187], v[48:51], a[160:163], v[184:187]\n"
      "ds_read_b128 v[40:43], v28 offset:43008\n"
      "v_mfma_f32_16x16x32_bf16 v[184:187], v[52:55], a[168:171], v[184:187]\n"
      "v_mfma_f32_16x16x32_bf16 v[188:191], v[48:51], a[164:167], v[188:191]\n"
      "ds_read_b128 v[44:47], v28 offset:44032\n"
      "v_mfma_f32_16x16x32_bf16 v[188:191], v[52:55], a[172:175], v[188:191]\n"
      "s_waitcnt lgkmcnt(4)\n"
      "v_mfma_f32_16x16x32_bf16 v[184:187], v[56:59], a[176:179], v[184:187]\n"
      "ds_read_b128 v[48:51], v28 offset:45056\n"
      "v_mfma_f32_16x16x32_bf16 v[184:187], v[60:63], a[184:187], v[184:187]\n"
      "v_mfma_f32_16x16x32_bf16 v[188:191], v[56:59], a[180:183], v[188:191]\n"
      "ds_read_b128 v[52:55], v28 offset:46080\n"
      "v_mfma_f32_16x16x32_bf16 v[188:191], v[60:63], a[188:191], v[188:191]\n"
      "s_waitcnt lgkmcnt(4)\n"
      "v_mfma_f32_16x16x32_bf16 v[184:187], v[32:35], a[192:195], v[184:187]\n"
      "ds_read_b128 v[56:59], v28 offset:47104\n"
      "v_mfma_f32_16x16x32_bf16 v[184:187], v[36:39], a[200:203], v[184:187]\n"
      "v_mfma_f32_16x16x32_bf16 v[188:191], v[32:35], a[196:199], v[188:191]\n"
      "ds_read_b128 v[60:63], v28 offset:48128\n"
      "v_mfma_f32_16x16x32_bf16 v[188:191], v[36:39], a[204:207], v[188:191]\n"
      "s_waitcnt lgkmcnt(4)\n"
      "v_mfma_f32_16x16x32_bf16 v[184:187], v[40:43], a[208:211], v[184:187]\n"
      "s_barrier\n"
      "ds_read_b128 v[32:35], v28 offset:49152\n"
      "v_mfma_f32_16x16x32_bf16 v[184:187], v[44:47], a[216:219], v[184:187]\n"
      "v_mfma_f32_16x16x32_bf16 v[188:191], v[40:43], a[212:215], v[188:191]\n"
      "ds_read_b128 v[36:39], v28 offset:50176\n"
      "v_mfma_f32_16x16x32_bf16 v[188:191], v[44:47], a[220:223], v[188:191]\n"
      "s_waitcnt lgkmcnt(4)\n"
      "v_mfma_f32_16x16x32_bf16 v[184:187], v[48:51], a[224:227], v[184:187]\n"
      "ds_read_b128 v[40:43], v28 offset:51200\n"
      "v_mfma_f32_16x16x32_bf16 v[184:187], v[52:55], a[232:235], v[184:187]\n"
      "v_mfma_f32_16x16x32_bf16 v[188:191], v[48:51], a[228:231], v[188:191]\n"
      "ds_read_b128 v[44:47], v28 offset:52224\n"
      "v_mfma_f32_16x16x32_bf16 v[188:191], v[52:55], a[236:239], v[188:191]\n"
      "s_waitcnt lgkmcnt(4)\n"
      "v_mfma_f32_16x16x32_bf16 v[184:187], v[56:59], a[240:243], v[184:187]\n"
      "ds_read_b128 v[48:51], v28 offset:53248\n"
      "v_mfma_f32_16x16x32_bf16 v[184:187], v[60:63], a[248:251], v[184:187]\n"
      "v_mfma_f32_16x16x32_bf16 v[188:191], v[56:59], a[244:247], v[188:191]\n"
      "ds_read_b128 v[52:55], v28 offset:54272\n"
      "v_mfma_f32_16x16x32_bf16 v[188:191], v[60:63], a[252:255], v[188:191]\n"
      "s_waitcnt lgkmcnt(4)\n"
      "v_mfma_f32_16x16x32_bf16 v[80:83], v[32:35], a[0:3], 0\n"
      "ds_read_b128 v[56:59], v28 offset:55296\n"
      "v_mfma_f32_16x16x32_bf16 v[80:83], v[36:39], a[8:11], v[80:83]\n"
      "v_mfma_f32_16x16x32_bf16 v[84:87], v[32:35], a[4:7], 0\n"
      "ds_read_b128 v[60:63], v28 offset:56320\n"
      "v_mfma_f32_16x16x32_bf16 v[84:87], v[36:39], a[12:15], v[84:87]\n"
      "s_waitcnt lgkmcnt(4)\n"
      "v_mfma_f32_16x16x32_bf16 v[80:83], v[40:43], a[16:19], v[80:83]\n"
      "ds_read_b128 v[32:35], v28 offset:57344\n"
      "v_mfma_f32_16x16x32_bf16 v[80:83], v[44:47], a[24:27], v[80:83]\n"
      "v_mfma_f32_16x16x32_bf16 v[84:87], v[40:43], a[20:23], v[84:87]\n"
      "ds_read_b128 v[36:39], v28 offset:58368\n"
      "v_mfma_f32_16x16x32_bf16 v[84:87], v[44:47], a[28:31], v[84:87]\n"
      "s_waitcnt lgkmcnt(4)\n"
      "v_mfma_f32_16x16x32_bf16 v[80:83], v[48:51], a[32:35], v[80:83]\n"
      "ds_read_b128 v[40:43], v28 offset:59392\n"
      "v_cvt_pk_bf16_f32 v108, v184, v185\n"
      "v_mfma_f32_16x16x32_bf16 v[80:83], v[52:55], a[40:43], v[80:83]\n"
      "v_cvt_pk_bf16_f32 v110, v188, v189\n"
      "v_cvt_pk_bf16_f32 v109, v186, v187\n"
      "v_mfma_f32_16x16x32_bf16 v[84:87], v[48:51], a[36:39], v[84:87]\n"
      "ds_read_b128 v[44:47], v28 offset:60416\n"
      "v_cvt_pk_bf16_f32 v111, v190, v191\n"
      "v_mfma_f32_16x16x32_bf16 v[84:87], v[52:55], a[44:47], v[84:87]\n"
      "s_waitcnt lgkmcnt(4)\n"
      "v_permlane16_swap_b32 v108, v110\n"
      "v_mfma_f32_16x16x32_bf16 v[80:83], v[56:59], a[48:51], v[80:83]\n"
      "ds_read_b128 v[48:51], v28 offset:61440\n"
      "v_permlane16_swap_b32 v109, v111\n"
      "v_mfma_f32_16x16x32_bf16 v[80:83], v[60:63], a[56:59], v[80:83]\n"
      "v_min_u32 v108, -2, v108\n"
      "v_min_u32 v110, -2, v110\n"
      "v_mfma_f32_16x16x32_bf16 v[84:87], v[56:59], a[52:55], v[84:87]\n"
      "ds_read_b128 v[52:55], v28 offset:62464\n"
      "v_min_u32 v109, -2, v109\n"
      "v_mfma_f32_16x16x32_bf16 v[84:87], v[60:63], a[60:63], v[84:87]\n"
      "s_waitcnt lgkmcnt(4)\n"
      "v_min_u32 v111, -2, v111\n"
      "v_mfma_f32_16x16x32_bf16 v[80:83], v[32:35], a[64:67], v[80:83]\n"
      "ds_read_b128 v[56:59], v28 offset:63488\n"
      "buffer_store_dwordx4 v[108:111], v73, s[36:39], s18 offen offset:3072 sc1\n"
      "v_mfma_f32_16x16x32_bf16 v[80:83], v[36:39], a[72:75], v[80:83]\n"
      "v_mfma_f32_16x16x32_bf16 v[84:87], v[32:35], a[68:71], v[84:87]\n"
      "ds_read_b128 v[60:63], v28 offset:64512\n"
      "v_mfma_f32_16x16x32_bf16 v[84:87], v[36:39], a[76:79], v[84:87]\n"
      "s_waitcnt lgkmcnt(4)\n"
      "v_mfma_f32_16x16x32_bf16 v[80:83], v[40:43], a[80:83], v[80:83]\n"
      "ds_read_b128 v[32:35], v29\n"
      "v_mfma_f32_16x16x32_bf16 v[80:83], v[44:47], a[88:91], v[80:83]\n"
      "v_mfma_f32_16x16x32_bf16 v[84:87], v[40:43], a[84:87], v[84:87]\n"
      "ds_read_b128 v[36:39], v29 offset:1024\n"
      "v_mfma_f32_16x16x32_bf16 v[84:87], v[44:47], a[92:95], v[84:87]\n"
      "s_waitcnt lgkmcnt(4)\n"
      "v_mfma_f32_16x16x32_bf16 v[80:83], v[48:51], a[96:99], v[80:83]\n"
      "ds_read_b128 v[40:43], v29 offset:2048\n"
      "v_mfma_f32_16x16x32_bf16 v[80:83], v[52:55], a[104:107], v[80:83]\n"
      "v_mfma_f32_16x16x32_bf16 v[84:87], v[48:51], a[100:103], v[84:87]\n"
      "ds_read_b128 v[44:47], v29 offset:3072\n"
      "v_mfma_f32_16x16x32_bf16 v[84:87], v[52:55], a[108:111], v[84:87]\n"
      "s_waitcnt lgkmcnt(4)\n"
      "v_mfma_f32_16x16x32_bf16 v[80:83], v[56:59], a[112:115], v[80:83]\n"
      "ds_read_b128 v[48:51], v29 offset:4096\n"
      "v_mfma_f32_16x16x32_bf16 v[80:83], v[60:63], a[120:123], v[80:83]\n"
      "v_mfma_f32_16x16x32_bf16 v[84:87], v[56:59], a[116:119], v[84:87]\n"
      "ds_read_b128 v[52:55], v29 offset:5120\n"
      "v_mfma_f32_16x16x32_bf16 v[84:87], v[60:63], a[124:127], v[84:87]\n"
      "s_waitcnt lgkmcnt(4)\n"
      "v_mfma_f32_16x16x32_bf16 v[80:83], v[32:35], a[128:131], v[80:83]\n"
      "ds_read_b128 v[56:59], v29 offset:6144\n"
      "v_mfma_f32_16x16x32_bf16 v[80:83], v[36:39], a[136:139], v[80:83]\n"
      "v_mfma_f32_16x16x32_bf16 v[84:87], v[32:35], a[132:135], v[84:87]\n"
      "ds_read_b128 v[60:63], v29 offset:7168\n"
      "v_mfma_f32_16x16x32_bf16 v[84:87], v[36:39], a[140:143], v[84:87]\n"
      "s_waitcnt lgkmcnt(4)\n"
      "v_mfma_f32_16x16x32_bf16 v[80:83], v[40:43], a[144:147], v[80:83]\n"
      "ds_read_b128 v[32:35], v29 offset:8192\n"
      "v_mfma_f32_16x16x32_bf16 v[80:83], v[44:47], a[152:155], v[80:83]\n"
      "v_mfma_f32_16x16x32_bf16 v[84:87], v[40:43], a[148:151], v[84:87]\n"
      "ds_read_b128 v[36:39], v29 offset:9216\n"
      "v_mfma_f32_16x16x32_bf16 v[84:87], v[44:47], a[156:159], v[84:87]\n"
      "s_waitcnt lgkmcnt(4)\n"
      "v_mfma_f32_16x16x32_bf16 v[80:83], v[48:51], a[160:163], v[80:83]\n"
      "ds_read_b128 v[40:43], v29 offset:10240\n"
      "v_mfma_f32_16x16x32_bf16 v[80:83], v[52:55], a[168:171], v[80:83]\n"
      "s_lshl_b32 s20, s30, 15\n"
      "s_add_u32 s20, s20, 0x60000\n"
      "v_mfma_f32_16x16x32_bf16 v[84:87], v[48:51], a[164:167], v[84:87]\n"
      "ds_read_b128 v[44:47], v29 offset:11264\n"
      "buffer_load_dwordx2 v[70:71], v68, s[36:39], s20 offen sc1\n"
      "v_mfma_f32_16x16x32_bf16 v[84:87], v[52:55], a[172:175], v[84:87]\n"
      "s_waitcnt lgkmcnt(4)\n"
      "v_mfma_f32_16x16x32_bf16 v[80:83], v[56:59], a[176:179], v[80:83]\n"
      "ds_read_b128 v[48:51], v29 offset:12288\n"
      "v_mfma_f32_16x16x32_bf16 v[80:83], v[60:63], a[184:187], v[80:83]\n"
      "v_mfma_f32_16x16x32_bf16 v[84:87], v[56:59], a[180:183], v[84:87]\n"
      "ds_read_b128 v[52:55], v29 offset:13312\n"
      "v_mfma_f32_16x16x32_bf16 v[84:87], v[60:63], a[188:191], v[84:87]\n"
      "s_waitcnt lgkmcnt(4)\n"
      "v_mfma_f32_16x16x32_bf16 v[80:83], v[32:35], a[192:195], v[80:83]\n"
      "ds_read_b128 v[56:59], v29 offset:14336\n"
      "v_mfma_f32_16x16x32_bf16 v[80:83], v[36:39], a[200:203], v[80:83]\n"
      "v_mfma_f32_16x16x32_bf16 v[84:87], v[32:35], a[196:199], v[84:87]\n"
      "ds_read_b128 v[60:63], v29 offset:15360\n"
      "v_mfma_f32_16x16x32_bf16 v[84:87], v[36:39], a[204:207], v[84:87]\n"
      "s_waitcnt lgkmcnt(4)\n"
      "v_mfma_f32_16x16x32_bf16 v[80:83], v[40:43], a[208:211], v[80:83]\n"
      "v_mfma_f32_16x16x32_bf16 v[80:83], v[44:47], a[216:219], v[80:83]\n"
      "v_mfma_f32_16x16x32_bf16 v[84:87], v[40:43], a[212:215], v[84:87]\n"
      "v_mfma_f32_16x16x32_bf16 v[84:87], v[44:47], a[220:223], v[84:87]\n"
      "s_waitcnt lgkmcnt(2)\n"
      "v_mfma_f32_16x16x32_bf16 v[80:83], v[48:51], a[224:227], v[80:83]\n"
      "v_mfma_f32_16x16x32_bf16 v[80:83], v[52:55], a[232:235], v[80:83]\n"
      "v_mfma_f32_16x16x32_bf16 v[84:87], v[48:51], a[228:231], v[84:87]\n"
      "v_mfma_f32_16x16x32_bf16 v[84:87], v[52:55], a[236:239], v[84:87]\n"
      "s_waitcnt lgkmcnt(0)\n"
      "v_mfma_f32_16x16x32_bf16 v[80:83], v[56:59], a[240:243], v[80:83]\n"
      "v_mfma_f32_16x16x32_bf16 v[80:83], v[60:63], a[248:251], v[80:83]\n"
      "v_mfma_f32_16x16x32_bf16 v[84:87], v[56:59], a[244:247], v[84:87]\n"
      "v_mfma_f32_16x16x32_bf16 v[84:87], v[60:63], a[252:255], v[84:87]\n"
      "s_nop 5\n"
      "v_cvt_pk_bf16_f32 v96, v80, v81\n"
      "s_nop 0\n"
      "v_cvt_pk_bf16_f32 v98, v84, v85\n"
      "v_cvt_pk_bf16_f32 v97, v82, v83\n"
      "v_cvt_pk_bf16_f32 v99, v86, v87\n"
      "v_permlane16_swap_b32 v96, v98\n"
      "s_nop 0\n"
      "v_permlane16_swap_b32 v97, v99\n"
      "v_min_u32 v96, -2, v96\n"
      "v_min_u32 v98, -2, v98\n"
      "v_min_u32 v97, -2, v97\n"
      "v_min_u32 v99, -2, v99\n"
      "buffer_store_dwordx4 v[96:99], v73, s[36:39], s19 offen sc1\n"
      "s_barrier\n"
      "v_and_b32 v66, 7, v2\n"
      "v_lshlrev_b32 v66, 3, v66\n"
      "v_add_u32 v66, 0x24000, v66\n"
      "s_lshl_b32 s91, s72, 6\n"
      "v_add_u32 v67, s91, v66\n"
      "s_cmp_eq_u32 s72, 3\n"
      "s_cbranch_scc1 Lf3_12_%=\n"
      "s_waitcnt vmcnt(1)\n"
      "v_cmp_ne_u64 vcc, s[76:77], v[70:71]\n"
      "s_cbranch_vccz Lib_13_%=\n"
      "s_mov_b32 s8, 0\n"
      "Lpoll_14_%=:\n"
      "buffer_load_dwordx2 v[70:71], v68, s[36:39], s20 offen sc1\n"
      "s_add_u32 s8, s8, 1\n"
      "s_waitcnt vmcnt(0) lgkmcnt(0)\n"
      "v_cmp_ne_u64 vcc, s[76:77], v[70:71]\n"
      "s_cbranch_vccz Lpolld_15_%=\n"
      "s_cmp_lt_u32 s8, 40000\n"
      "s_cbranch_scc1 Lpoll_14_%=\n"
      "Lpolld_15_%=:\n"
      "Lib_13_%=:\n"
      "s_add_u32 s95, s12, 0\n"
      "buffer_load_dwordx4 v[96:99], v1, s[36:39], s95 offen sc1\n"
      "s_add_u32 s95, s12, 9216\n"
      "buffer_load_dwordx4 v[100:103], v1, s[36:39], s95 offen sc1\n"
      "s_add_u32 s95, s12, 18432\n"
      "buffer_load_dwordx4 v[104:107], v1, s[36:39], s95 offen sc1\n"
      "s_add_u32 s95, s12, 27648\n"
      "buffer_load_dwordx4 v[108:111], v1, s[36:39], s95 offen sc1\n"
      "s_add_u32 s95, s12, 3072\n"
      "buffer_load_dwordx4 v[112:115], v1, s[36:39], s95 offen sc1\n"
      "s_add_u32 s95, s12, 12288\n"
      "buffer_load_dwordx4 v[116:119], v1, s[36:39], s95 offen sc1\n"
      "s_add_u32 s95, s12, 21504\n"
      "buffer_load_dwordx4 v[120:123], v1, s[36:39], s95 offen sc1\n"
      "s_add_u32 s95, s12, 30720\n"
      "buffer_load_dwordx4 v[124:127], v1, s[36:39], s95 offen sc1\n"
      "ds_write_b128 v254, v[224:227]\n"
      "ds_write_b128 v254, v[228:231] offset:1024\n"
      "ds_read_b64_tr_b16 v[224:225], v255\n"
      "ds_read_b64_tr_b16 v[226:227], v255 offset:512\n"
      "ds_read_b64_tr_b16 v[228:229], v255 offset:64\n"
      "ds_read_b64_tr_b16 v[230:231], v255 offset:576\n"
      "ds_write_b128 v254, v[232:235]\n"
      "ds_write_b128 v254, v[236:239] offset:1024\n"
      "ds_read_b64_tr_b16 v[232:233], v255\n"
      "ds_read_b64_tr_b16 v[234:235], v255 offset:512\n"
      "ds_read_b64_tr_b16 v[236:237], v255 offset:64\n"
      "ds_read_b64_tr_b16 v[238:239], v255 offset:576\n"
      "ds_write_b128 v254, v[240:243]\n"
      "ds_write_b128 v254, v[244:247] offset:1024\n"
      "ds_read_b64_tr_b16 v[240:241], v255\n"
      "ds_read_b64_tr_b16 v[242:243], v255 offset:512\n"
      "ds_read_b64_tr_b16 v[244:245], v255 offset:64\n"
      "ds_read_b64_tr_b16 v[246:247], v255 offset:576\n"
      "ds_write_b128 v254, v[88:91]\n"
      "ds_write_b128 v254, v[92:95] offset:1024\n"
      "ds_read_b64_tr_b16 v[88:89], v255\n"
      "ds_read_b64_tr_b16 v[90:91], v255 offset:512\n"
      "ds_read_b64_tr_b16 v[92:93], v255 offset:64\n"
      "ds_read_b64_tr_b16 v[94:95], v255 offset:576\n"
      "v_lshlrev_b32 v144, 16, v16\n"
      "v_and_b32 v156, 0xffff0000, v16\n"
      "v_lshlrev_b32 v145, 16, v17\n"
      "v_and_b32 v157, 0xffff0000, v17\n"
      "v_lshlrev_b32 v146, 16, v18\n"
      "v_and_b32 v158, 0xffff0000, v18\n"
      "v_lshlrev_b32 v147, 16, v19\n"
      "v_and_b32 v159, 0xffff0000, v19\n"
      "v_lshlrev_b32 v148, 16, v20\n"
      "v_and_b32 v160, 0xffff0000, v20\n"
      "v_lshlrev_b32 v149, 16, v21\n"
      "v_and_b32 v161, 0xffff0000, v21\n"
      "v_lshlrev_b32 v150, 16, v22\n"
      "v_and_b32 v162, 0xffff0000, v22\n"
      "v_lshlrev_b32 v151, 16, v23\n"
      "v_and_b32 v163, 0xffff0000, v23\n"
      "v_lshlrev_b32 v152, 16, v24\n"
      "v_and_b32 v164, 0xffff0000, v24\n"
      "v_lshlrev_b32 v153, 16, v25\n"
      "v_and_b32 v165, 0xffff0000, v25\n"
      "v_lshlrev_b32 v154, 16, v26\n"
      "v_and_b32 v166, 0xffff0000, v26\n"
      "v_lshlrev_b32 v155, 16, v27\n"
      "v_and_b32 v167, 0xffff0000, v27\n"
      "s_waitcnt vmcnt(8)\n"
      "s_add_u32 s95, s12, 6144\n"
      "buffer_load_dwordx4 v[128:131], v1, s[36:39], s95 offen sc1\n"
      "s_add_u32 s95, s12, 15360\n"
      "buffer_load_dwordx4 v[132:135], v1, s[36:39], s95 offen sc1\n"
      "s_add_u32 s95, s12, 24576\n"
      "buffer_load_dwordx4 v[136:139], v1, s[36:39], s95 offen sc1\n"
      "s_add_u32 s95, s12, 33792\n"
      "buffer_load_dwordx4 v[140:143], v1, s[36:39], s95 offen sc1\n"
      "s_waitcnt vmcnt(11)\n"
      "v_lshlrev_b32 v168, 16, v96\n"
      "v_lshlrev_b32 v169, 16, v97\n"
      "v_lshlrev_b32 v170, 16, v98\n"
      "v_lshlrev_b32 v171, 16, v99\n"
      "v_and_b32 v96, 0xffff0000, v96\n"
      "v_and_b32 v97, 0xffff0000, v97\n"
      "v_and_b32 v98, 0xffff0000, v98\n"
      "v_and_b32 v99, 0xffff0000, v99\n"
      "v_pk_add_f32 v[144:145], v[144:145], v[168:169]\n"
      "v_pk_add_f32 v[156:157], v[156:157], v[96:97]\n"
      "v_pk_add_f32 v[146:147], v[146:147], v[170:171]\n"
      "v_pk_add_f32 v[158:159], v[158:159], v[98:99]\n"
      "s_add_u32 m0, s81, 0\n"
      "s_add_u32 s96, s10, 0\n"
      "buffer_load_dwordx4 v4, s[52:55], s96 offen lds\n"
      "s_waitcnt vmcnt(11)\n"
      "v_lshlrev_b32 v168, 16, v100\n"
      "v_lshlrev_b32 v169, 16, v101\n"
      "v_lshlrev_b32 v170, 16, v102\n"
      "v_lshlrev_b32 v171, 16, v103\n"
      "v_and_b32 v100, 0xffff0000, v100\n"
      "v_and_b32 v101, 0xffff0000, v101\n"
      "v_and_b32 v102, 0xffff0000, v102\n"
      "v_and_b32 v103, 0xffff0000, v103\n"
      "v_pk_add_f32 v[144:145], v[144:145], v[168:169]\n"
      "v_pk_add_f32 v[156:157], v[156:157], v[100:101]\n"
      "v_pk_add_f32 v[146:147], v[146:147], v[170:171]\n"
      "v_pk_add_f32 v[158:159], v[158:159], v[102:103]\n"
      "buffer_load_dwordx4 v5, s[52:55], s96 offen offset:1024 lds\n"
      "s_waitcnt vmcnt(11)\n"
      "v_lshlrev_b32 v168, 16, v104\n"
      "v_lshlrev_b32 v169, 16, v105\n"
      "v_lshlrev_b32 v170, 16, v106\n"
      "v_lshlrev_b32 v171, 16, v107\n"
      "v_and_b32 v104, 0xffff0000, v104\n"
      "v_and_b32 v105, 0xffff0000, v105\n"
      "v_and_b32 v106, 0xffff0000, v106\n"
      "v_and_b32 v107, 0xffff0000, v107\n"
      "v_pk_add_f32 v[144:145], v[144:145], v[168:169]\n"
      "v_pk_add_f32 v[156:157], v[156:157], v[104:105]\n"
      "v_pk_add_f32 v[146:147], v[146:147], v[170:171]\n"
      "v_pk_add_f32 v[158:159], v[158:159], v[106:107]\n"
      "buffer_load_dwordx4 v6, s[52:55], s96 offen offset:2048 lds\n"
      "s_waitcnt vmcnt(11)\n"
      "v_lshlrev_b32 v168, 16, v108\n"
      "v_lshlrev_b32 v169, 16, v109\n"
      "v_lshlrev_b32 v170, 16, v110\n"
      "v_lshlrev_b32 v171, 16, v111\n"
      "v_and_b32 v108, 0xffff0000, v108\n"
      "v_and_b32 v109, 0xffff0000, v109\n"
      "v_and_b32 v110, 0xffff0000, v110\n"
      "v_and_b32 v111, 0xffff0000, v111\n"
      "v_pk_add_f32 v[144:145], v[144:145], v[168:169]\n"
      "v_pk_add_f32 v[156:157], v[156:157], v[108:109]\n"
      "v_pk_add_f32 v[146:147], v[146:147], v[170:171]\n"
      "v_pk_add_f32 v[158:159], v[158:159], v[110:111]\n"
      "buffer_load_dwordx4 v7, s[52:55], s96 offen offset:3072 lds\n"
      "s_waitcnt vmcnt(11)\n"
      "v_lshlrev_b32 v168, 16, v112\n"
      "v_lshlrev_b32 v169, 16, v113\n"
      "v_lshlrev_b32 v170, 16, v114\n"
      "v_lshlrev_b32 v171, 16, v115\n"
      "v_and_b32 v112, 0xffff0000, v112\n"
      "v_and_b32 v113, 0xffff0000, v113\n"
      "v_and_b32 v114, 0xffff0000, v114\n"
      "v_and_b32 v115, 0xffff0000, v115\n"
      "v_pk_add_f32 v[148:149], v[148:149], v[168:169]\n"
      "v_pk_add_f32 v[160:161], v[160:161], v[112:113]\n"
      "v_pk_add_f32 v[150:151], v[150:151], v[170:171]\n"
      "v_pk_add_f32 v[162:163], v[162:163], v[114:115]\n"
      "s_add_u32 m0, s81, 16384\n"
      "s_add_u32 s96, s10, 589824\n"
      "buffer_load_dwordx4 v4, s[52:55], s96 offen lds\n"
      "s_waitcnt vmcnt(11)\n"
      "v_lshlrev_b32 v168, 16, v116\n"
      "v_lshlrev_b32 v169, 16, v117\n"
      "v_lshlrev_b32 v170, 16, v118\n"
      "v_lshlrev_b32 v171, 16, v119\n"
      "v_and_b32 v116, 0xffff0000, v116\n"
      "v_and_b32 v117, 0xffff0000, v117\n"
      "v_and_b32 v118, 0xffff0000, v118\n"
      "v_and_b32 v119, 0xffff0000, v119\n"
      "v_pk_add_f32 v[148:149], v[148:149], v[168:169]\n"
      "v_pk_add_f32 v[160:161], v[160:161], v[116:117]\n"
      "v_pk_add_f32 v[150:151], v[150:151], v[170:171]\n"
      "v_pk_add_f32 v[162:163], v[162:163], v[118:119]\n"
      "buffer_load_dwordx4 v5, s[52:55], s96 offen offset:1024 lds\n"
      "s_waitcnt vmcnt(11)\n"
      "v_lshlrev_b32 v168, 16, v120\n"
      "v_lshlrev_b32 v169, 16, v121\n"
      "v_lshlrev_b32 v170, 16, v122\n"
      "v_lshlrev_b32 v171, 16, v123\n"
      "v_and_b32 v120, 0xffff0000, v120\n"
      "v_and_b32 v121, 0xffff0000, v121\n"
      "v_and_b32 v122, 0xffff0000, v122\n"
      "v_and_b32 v123, 0xffff0000, v123\n"
      "v_pk_add_f32 v[148:149], v[148:149], v[168:169]\n"
      "v_pk_add_f32 v[160:161], v[160:161], v[120:121]\n"
      "v_pk_add_f32 v[150:151], v[150:151], v[170:171]\n"
      "v_pk_add_f32 v[162:163], v[162:163], v[122:123]\n"
      "buffer_load_dwordx4 v6, s[52:55], s96 offen offset:2048 lds\n"
      "s_waitcnt vmcnt(11)\n"
      "v_lshlrev_b32 v168, 16, v124\n"
      "v_lshlrev_b32 v169, 16, v125\n"
      "v_lshlrev_b32 v170, 16, v126\n"
      "v_lshlrev_b32 v171, 16, v127\n"
      "v_and_b32 v124, 0xffff0000, v124\n"
      "v_and_b32 v125, 0xffff0000, v125\n"
      "v_and_b32 v126, 0xffff0000, v126\n"
      "v_and_b32 v127, 0xffff0000, v127\n"
      "v_pk_add_f32 v[148:149], v[148:149], v[168:169]\n"
      "v_pk_add_f32 v[160:161], v[160:161], v[124:125]\n"
      "v_pk_add_f32 v[150:151], v[150:151], v[170:171]\n"
      "v_pk_add_f32 v[162:163], v[162:163], v[126:127]\n"
      "buffer_load_dwordx4 v7, s[52:55], s96 offen offset:3072 lds\n"
      "s_mov_b32 s8, 0\n"
      "Lrl_16_%=:\n"
      "s_waitcnt vmcnt(10)\n"
      "v_max3_u32 v168, v128, v129, v130\n"
      "v_max3_u32 v169, v131, v132, v133\n"
      "v_max3_u32 v168, v168, v134, v135\n"
      "s_waitcnt vmcnt(8)\n"
      "v_max3_u32 v169, v169, v136, v137\n"
      "v_max3_u32 v168, v168, v138, v139\n"
      "v_max3_u32 v169, v169, v140, v141\n"
      "v_max3_u32 v168, v168, v142, v143\n"
      "v_max_u32 v168, v168, v169\n"
      "v_cmp_eq_u32 vcc, -1, v168\n"
      "s_cbranch_vccz Lrb_17_%=\n"
      "s_add_u32 s95, s12, 6144\n"
      "buffer_load_dwordx4 v[128:131], v1, s[36:39], s95 offen sc1\n"
      "s_add_u32 s95, s12, 15360\n"
      "buffer_load_dwordx4 v[132:135], v1, s[36:39], s95 offen sc1\n"
      "s_add_u32 s95, s12, 24576\n"
      "buffer_load_dwordx4 v[136:139], v1, s[36:39], s95 offen sc1\n"
      "s_add_u32 s95, s12, 33792\n"
      "buffer_load_dwordx4 v[140:143], v1, s[36:39], s95 offen sc1\n"
      "s_add_u32 s8, s8, 1\n"
      "s_waitcnt vmcnt(0) lgkmcnt(0)\n"
      "s_cmp_lt_u32 s8, 40000\n"
      "s_cbranch_scc1 Lrl_16_%=\n"
      "Lrb_17_%=:\n"
      "v_lshlrev_b32 v168, 16, v128\n"
      "v_lshlrev_b32 v169, 16, v129\n"
      "v_lshlrev_b32 v170, 16, v130\n"
      "v_lshlrev_b32 v171, 16, v131\n"
      "s_add_u32 m0, s81, 32768\n"
      "s_add_u32 s96, s10, 1179648\n"
      "buffer_load_dwordx4 v4, s[52:55], s96 offen lds\n"
      "v_and_b32 v128, 0xffff0000, v128\n"
      "v_and_b32 v129, 0xffff0000, v129\n"
      "v_and_b32 v130, 0xffff0000, v130\n"
      "v_and_b32 v131, 0xffff0000, v131\n"
      "v_pk_add_f32 v[152:153], v[152:153], v[168:169]\n"
      "v_pk_add_f32 v[164:165], v[164:165], v[128:129]\n"
      "v_pk_add_f32 v[154:155], v[154:155], v[170:171]\n"
      "v_pk_add_f32 v[166:167], v[166:167], v[130:131]\n"
      "v_lshlrev_b32 v168, 16, v132\n"
      "v_lshlrev_b32 v169, 16, v133\n"
      "v_lshlrev_b32 v170, 16, v134\n"
      "v_lshlrev_b32 v171, 16, v135\n"
      "buffer_load_dwordx4 v5, s[52:55], s96 offen offset:1024 lds\n"
      "v_and_b32 v132, 0xffff0000, v132\n"
      "v_and_b32 v133, 0xffff0000, v133\n"
      "v_and_b32 v134, 0xffff0000, v134\n"
      "v_and_b32 v135, 0xffff0000, v135\n"
      "v_pk_add_f32 v[152:153], v[152:153], v[168:169]\n"
      "v_pk_add_f32 v[164:165], v[164:165], v[132:133]\n"
      "v_pk_add_f32 v[154:155], v[154:155], v[170:171]\n"
      "v_pk_add_f32 v[166:167], v[166:167], v[134:135]\n"
      "v_lshlrev_b32 v168, 16, v136\n"
      "v_lshlrev_b32 v169, 16, v137\n"
      "v_lshlrev_b32 v170, 16, v138\n"
      "v_lshlrev_b32 v171, 16, v139\n"
      "buffer_load_dwordx4 v6, s[52:55], s96 offen offset:2048 lds\n"
      "v_and_b32 v136, 0xffff0000, v136\n"
      "v_and_b32 v137, 0xffff0000, v137\n"
      "v_and_b32 v138, 0xffff0000, v138\n"
      "v_and_b32 v139, 0xffff0000, v139\n"
      "v_pk_add_f32 v[152:153], v[152:153], v[168:169]\n"
      "v_pk_add_f32 v[164:165], v[164:165], v[136:137]\n"
      "v_pk_add_f32 v[154:155], v[154:155], v[170:171]\n"
      "v_pk_add_f32 v[166:167], v[166:167], v[138:139]\n"
      "v_lshlrev_b32 v168, 16, v140\n"
      "v_lshlrev_b32 v169, 16, v141\n"
      "v_lshlrev_b32 v170, 16, v142\n"
      "v_lshlrev_b32 v171, 16, v143\n"
      "buffer_load_dwordx4 v7, s[52:55], s96 offen offset:3072 lds\n"
      "v_and_b32 v140, 0xffff0000, v140\n"
      "v_and_b32 v141, 0xffff0000, v141\n"
      "v_and_b32 v142, 0xffff0000, v142\n"
      "v_and_b32 v143, 0xffff0000, v143\n"
      "v_pk_add_f32 v[152:153], v[152:153], v[168:169]\n"
      "v_pk_add_f32 v[164:165], v[164:165], v[140:141]\n"
      "v_pk_add_f32 v[154:155], v[154:155], v[170:171]\n"
      "v_pk_add_f32 v[166:167], v[166:167], v[142:143]\n"
      "v_cvt_pk_bf16_f32 v24, v144, v156\n"
      "v_cvt_pk_bf16_f32 v25, v145, v157\n"
      "v_cvt_pk_bf16_f32 v26, v146, v158\n"
      "v_cvt_pk_bf16_f32 v27, v147, v159\n"
      "s_add_u32 m0, s81, 49152\n"
      "s_add_u32 s96, s10, 1769472\n"
      "buffer_load_dwordx4 v4, s[52:55], s96 offen lds\n"
      "v_cvt_pk_bf16_f32 v8, v148, v160\n"
      "v_cvt_pk_bf16_f32 v10, v149, v161\n"
      "v_cvt_pk_bf16_f32 v12, v150, v162\n"
      "v_cvt_pk_bf16_f32 v14, v151, v163\n"
      "v_cvt_pk_bf16_f32 v15, v152, v164\n"
      "v_cvt_pk_bf16_f32 v64, v153, v165\n"
      "v_cvt_pk_bf16_f32 v65, v154, v166\n"
      "v_cvt_pk_bf16_f32 v30, v155, v167\n"
      "v_lshlrev_b32 v144, 16, v24\n"
      "v_and_b32 v156, 0xffff0000, v24\n"
      "v_lshlrev_b32 v145, 16, v25\n"
      "v_and_b32 v157, 0xffff0000, v25\n"
      "buffer_load_dwordx4 v5, s[52:55], s96 offen offset:1024 lds\n"
      "v_lshlrev_b32 v146, 16, v26\n"
      "v_and_b32 v158, 0xffff0000, v26\n"
      "v_lshlrev_b32 v147, 16, v27\n"
      "v_and_b32 v159, 0xffff0000, v27\n"
      "v_lshlrev_b32 v148, 16, v8\n"
      "v_and_b32 v160, 0xffff0000, v8\n"
      "v_lshlrev_b32 v149, 16, v10\n"
      "v_and_b32 v161, 0xffff0000, v10\n"
      "v_lshlrev_b32 v150, 16, v12\n"
      "v_and_b32 v162, 0xffff0000, v12\n"
      "v_lshlrev_b32 v151, 16, v14\n"
      "v_and_b32 v163, 0xffff0000, v14\n"
      "buffer_load_dwordx4 v6, s[52:55], s96 offen offset:2048 lds\n"
      "v_lshlrev_b32 v152, 16, v15\n"
      "v_and_b32 v164, 0xffff0000, v15\n"
      "v_lshlrev_b32 v153, 16, v64\n"
      "v_and_b32 v165, 0xffff0000, v64\n"
      "v_lshlrev_b32 v154, 16, v65\n"
      "v_and_b32 v166, 0xffff0000, v65\n"
      "v_lshlrev_b32 v155, 16, v30\n"
      "v_and_b32 v167, 0xffff0000, v30\n"
      "v_pk_add_f32 v[168:169], v[144:145], v[146:147]\n"
      "v_pk_add_f32 v[170:171], v[148:149], v[150:151]\n"
      "Lpad5_%=:\n"
      "s_cbranch_execz Lret_%=\n"
      "v_pk_add_f32 v[168:169], v[168:169], v[152:153]\n"
      "v_pk_add_f32 v[170:171], v[170:171], v[154:155]\n"
      "buffer_load_dwordx4 v7, s[52:55], s96 offen offset:3072 lds\n"
      "v_pk_add_f32 v[168:169], v[168:169], v[156:157]\n"
      "v_pk_add_f32 v[170:171], v[170:171], v[158:159]\n"
      "v_pk_add_f32 v[168:169], v[168:169], v[160:161]\n"
      "v_pk_add_f32 v[170:171], v[170:171], v[162:163]\n"
      "v_pk_add_f32 v[168:169], v[168:169], v[164:165]\n"
      "v_pk_add_f32 v[170:171], v[170:171], v[166:167]\n"
      "v_pk_add_f32 v[168:169], v[168:169], v[170:171]\n"
      "v_add_f32 v168, v168, v169\n"
      "s_nop 1\n"
      "v_add_f32_dpp v168, v168, v168 row_ror:8 row_mask:0xf bank_mask:0xf\n"
      "v_mov_b32 v169, v168\n"
      "s_nop 1\n"
      "v_permlane16_swap_b32 v168, v169\n"
      "v_add_f32 v168, v168, v169\n"
      "s_add_u32 m0, s81, 65536\n"
      "s_add_u32 s96, s10, 2359296\n"
      "buffer_load_dwordx4 v4, s[52:55], s96 offen lds\n"
      "v_mov_b32 v169, v168\n"
      "s_nop 1\n"
      "v_permlane32_swap_b32 v168, v169\n"
      "v_add_f32 v168, v168, v169\n"
      "v_mul_f32 v168, 0xbbaaaaab, v168\n"
      "v_pk_add_f32 v[144:145], v[144:145], v[168:169] op_sel_hi:[1,0]\n"
      "v_pk_add_f32 v[146:147], v[146:147], v[168:169] op_sel_hi:[1,0]\n"
      "v_pk_add_f32 v[148:149], v[148:149], v[168:169] op_sel_hi:[1,0]\n"
      "v_pk_add_f32 v[150:151], v[150:151], v[168:169] op_sel_hi:[1,0]\n"
      "v_pk_add_f32 v[152:153], v[152:153], v[168:169] op_sel_hi:[1,0]\n"
      "v_pk_add_f32 v[154:155], v[154:155], v[168:169] op_sel_hi:[1,0]\n"
      "v_pk_add_f32 v[156:157], v[156:157], v[168:169] op_sel_hi:[1,0]\n"
      "v_pk_add_f32 v[158:159], v[158:159], v[168:169] op_sel_hi:[1,0]\n"
      "buffer_load_dwordx4 v5, s[52:55], s96 offen offset:1024 lds\n"
      "v_pk_add_f32 v[160:161], v[160:161], v[168:169] op_sel_hi:[1,0]\n"
      "v_pk_add_f32 v[162:163], v[162:163], v[168:169] op_sel_hi:[1,0]\n"
      "v_pk_add_f32 v[164:165], v[164:165], v[168:169] op_sel_hi:[1,0]\n"
      "v_pk_add_f32 v[166:167], v[166:167], v[168:169] op_sel_hi:[1,0]\n"
      "v_pk_mul_f32 v[172:173], v[144:145], v[144:145]\n"
      "v_pk_mul_f32 v[174:175], v[146:147], v[146:147]\n"
      "v_pk_fma_f32 v[172:173], v[148:149], v[148:149], v[172:173]\n"
      "v_pk_fma_f32 v[174:175], v[150:151], v[150:151], v[174:175]\n"
      "v_pk_fma_f32 v[172:173], v[152:153], v[152:153], v[172:173]\n"
      "v_pk_fma_f32 v[174:175], v[154:155], v[154:155], v[174:175]\n"
      "v_pk_fma_f32 v[172:173], v[156:157], v[156:157], v[172:173]\n"
      "v_pk_fma_f32 v[174:175], v[158:159], v[158:159], v[174:175]\n"
      "buffer_load_dwordx4 v6, s[52:55], s96 offen offset:2048 lds\n"
      "v_pk_fma_f32 v[172:173], v[160:161], v[160:161], v[172:173]\n"
      "v_pk_fma_f32 v[174:175], v[162:163], v[162:163], v[174:175]\n"
      "v_pk_fma_f32 v[172:173], v[164:165], v[164:165], v[172:173]\n"
      "v_pk_fma_f32 v[174:175], v[166:167], v[166:167], v[174:175]\n"
      "v_pk_add_f32 v[172:173], v[172:173], v[174:175]\n"
      "v_add_f32 v172, v172, v173\n"
      "s_nop 1\n"
      "v_add_f32_dpp v172, v172, v172 row_ror:8 row_mask:0xf bank_mask:0xf\n"
      "v_mov_b32 v173, v172\n"
      "s_nop 1\n"
      "v_permlane16_swap_b32 v172, v173\n"
      "v_add_f32 v172, v172, v173\n"
      "v_mov_b32 v173, v172\n"
      "s_nop 1\n"
      "v_permlane32_swap_b32 v172, v173\n"
      "buffer_load_dwordx4 v7, s[52:55], s96 offen offset:3072 lds\n"
      "v_add_f32 v172, v172, v173\n"
      "v_mov_b32 v169, v172\n"
      "ds_write_b64 v67, v[168:169]\n"
      "s_waitcnt lgkmcnt(0)\n"
      "s_barrier\n"
      "ds_read_b64 v[176:177], v66\n"
      "ds_read_b64 v[178:179], v66 offset:64\n"
      "ds_read_b64 v[180:181], v66 offset:128\n"
      "s_waitcnt lgkmcnt(0)\n"
      "v_add_f32 v172, v176, v178\n"
      "v_add_f32 v173, v177, v179\n"
      "v_add_f32 v172, v172, v180\n"
      "v_add_f32 v173, v173, v181\n"
      "v_mul_f32 v172, 0x3eaaaaab, v172\n"
      "v_sub_f32 v176, v176, v172\n"
      "v_sub_f32 v178, v178, v172\n"
      "v_sub_f32 v180, v180, v172\n"
      "v_mul_f32 v174, v176, v176\n"
      "v_fmac_f32 v174, v178, v178\n"
      "v_fmac_f32 v174, v180, v180\n"
      "v_fmac_f32 v173, 0x43400000, v174\n"
      "v_mov_b32 v174, 0x3727c5ac\n"
      "v_fmac_f32 v174, 0x3ae38e39, v173\n"
      "v_rsq_f32 v174, v174\n"
      "v_sub_f32 v172, v172, v168\n"
      "v_mul_f32 v174, 0x3fb8aa3b, v174\n"
      "v_mul_f32 v172, v172, v174\n"
      "v_pk_fma_f32 v[144:145], v[144:145], v[174:175], v[172:173] op_sel_hi:[1,0,0]\n"
      "v_pk_fma_f32 v[146:147], v[146:147], v[174:175], v[172:173] op_sel_hi:[1,0,0]\n"
      "v_pk_fma_f32 v[156:157], v[156:157], v[174:175], v[172:173] op_sel_hi:[1,0,0]\n"
      "v_pk_fma_f32 v[158:159], v[158:159], v[174:175], v[172:173] op_sel_hi:[1,0,0]\n"
      "v_cvt_pk_bf16_f32 v96, v144, v156\n"
      "v_cvt_pk_bf16_f32 v97, v145, v157\n"
      "v_cvt_pk_bf16_f32 v98, v146, v158\n"
      "v_cvt_pk_bf16_f32 v99, v147, v159\n"
      "buffer_store_dwordx4 v[96:99], v78, s[36:39], s13 offen sc1\n"
      "s_add_u32 s13, s13, 24576\n"
      "v_pk_fma_f32 v[148:149], v[148:149], v[174:175], v[172:173] op_sel_hi:[1,0,0]\n"
      "v_pk_fma_f32 v[150:151], v[150:151], v[174:175], v[172:173] op_sel_hi:[1,0,0]\n"
      "v_pk_fma_f32 v[160:161], v[160:161], v[174:175], v[172:173] op_sel_hi:[1,0,0]\n"
      "v_pk_fma_f32 v[162:163], v[162:163], v[174:175], v[172:173] op_sel_hi:[1,0,0]\n"
      "v_cvt_pk_bf16_f32 v100, v148, v160\n"
      "v_cvt_pk_bf16_f32 v101, v149, v161\n"
      "v_cvt_pk_bf16_f32 v102, v150, v162\n"
      "v_cvt_pk_bf16_f32 v103, v151, v163\n"
      "buffer_store_dwordx4 v[100:103], v78, s[36:39], s13 offen sc1\n"
      "s_add_u32 s13, s13, 24576\n"
      "v_pk_fma_f32 v[152:153], v[152:153], v[174:175], v[172:173] op_sel_hi:[1,0,0]\n"
      "v_pk_fma_f32 v[154:155], v[154:155], v[174:175], v[172:173] op_sel_hi:[1,0,0]\n"
      "v_pk_fma_f32 v[164:165], v[164:165], v[174:175], v[172:173] op_sel_hi:[1,0,0]\n"
      "v_pk_fma_f32 v[166:167], v[166:167], v[174:175], v[172:173] op_sel_hi:[1,0,0]\n"
      "v_cvt_pk_bf16_f32 v104, v152, v164\n"
      "v_cvt_pk_bf16_f32 v105, v153, v165\n"
      "v_cvt_pk_bf16_f32 v106, v154, v166\n"
      "v_cvt_pk_bf16_f32 v107, v155, v167\n"
      "buffer_store_dwordx4 v[104:107], v78, s[36:39], s13 offen sc1\n"
      "s_bfe_u32 s31, s75, 0x30002\n"
      "s_lshl_b32 s90, s31, 2\n"
      "s_add_u32 s90, s90, s72\n"
      "s_bfe_u32 s91, s90, 0x10000\n"
      "s_lshl_b32 s91, s91, 9\n"
      "s_bfe_u32 s97, s90, 0x10004\n"
      "s_lshl_b32 s97, s97, 7\n"
      "s_add_u32 s91, s91, s97\n"
      "s_bfe_u32 s97, s90, 0x30001\n"
      "s_lshl_b32 s97, s97, 12\n"
      "s_add_u32 s91, s91, s97\n"
      "s_add_u32 s91, s91, 0x19000\n"
      "v_lshrrev_b32 v67, 3, v2\n"
      "v_min_u32 v67, 2, v67\n"
      "v_and_b32 v77, 3, v2\n"
      "v_lshlrev_b32 v77, 10, v77\n"
      "v_bfe_u32 v66, v2, 2, 1\n"
      "v_lshl_add_u32 v77, v66, 8, v77\n"
      "v_add_u32 v77, s91, v77\n"
      "v_lshl_add_u32 v77, v67, 15, v77\n"
      "s_mul_i32 s56, s90, 73728\n"
      "s_add_u32 s56, s56, 0xf00000\n"
      "s_mul_i32 s35, s90, 589824\n"
      "s_mul_i32 s97, s30, 73728\n"
      "s_add_u32 s35, s35, s97\n"
      "s_bfe_u32 s97, s9, 0x10002\n"
      "s_lshl_b32 s97, s97, 12\n"
      "s_add_u32 s35, s35, s97\n"
      "s_bfe_u32 s97, s9, 0x10001\n"
      "s_lshl_b32 s97, s97, 10\n"
      "s_add_u32 s35, s35, s97\n"
      "s_bfe_u32 s97, s9, 0x10000\n"
      "s_lshl_b32 s97, s97, 9\n"
      "s_add_u32 s35, s35, s97\n"
      "s_add_u32 s35, s35, 0x2300000\n"
      "v_bfe_u32 v72, v2, 2, 1\n"
      "v_lshlrev_b32 v72, 11, v72\n"
      "v_lshrrev_b32 v66, 5, v2\n"
      "v_lshl_add_u32 v72, v66, 8, v72\n"
      "v_bfe_u32 v66, v2, 4, 1\n"
      "v_lshl_add_u32 v72, v66, 7, v72\n"
      "v_bfe_u32 v66, v2, 3, 1\n"
      "v_lshl_add_u32 v72, v66, 6, v72\n"
      "v_and_b32 v66, 3, v2\n"
      "v_lshl_add_u32 v72, v66, 4, v72\n"
      "s_lshl_b32 s91, s28, 2\n"
      "s_add_u32 s91, s91, s72\n"
      "s_mul_i32 s59, s91, 294912\n"
      "s_mul_i32 s97, s30, 73728\n"
      "s_add_u32 s59, s59, s97\n"
      "s_add_u32 s59, s59, 0x2300000\n"
      "s_lshl_b32 s58, s72, 10\n"
      "s_lshl_b32 s97, s73, 12\n"
      "s_add_u32 s58, s58, s97\n"
      "s_add_u32 s58, s58, 0x3f00000\n"
      "s_bfe_u32 s93, s91, 0x20001\n"
      "s_lshl_b32 s93, s93, 10\n"
      "s_bfe_u32 s97, s28, 0x30001\n"
      "s_lshl_b32 s97, s97, 17\n"
      "s_add_u32 s93, s93, s97\n"
      "s_lshl_b32 s97, s30, 13\n"
      "s_add_u32 s93, s93, s97\n"
      "s_add_u32 s93, s93, 0x3f00000\n"
      "v_and_b32 v23, 3, v2\n"
      "v_lshlrev_b32 v23, 15, v23\n"
      "v_bfe_u32 v66, v2, 2, 1\n"
      "v_lshl_add_u32 v23, v66, 12, v23\n"
      "v_add_u32 v23, s93, v23\n"
      "v_bfe_u32 v73, v2, 2, 2\n"
      "v_mul_u32_u24 v73, 36864, v73\n"
      "v_lshrrev_b32 v67, 5, v2\n"
      "v_lshl_add_u32 v73, v67, 7, v73\n"
      "v_bfe_u32 v67, v2, 4, 1\n"
      "v_lshl_add_u32 v73, v67, 6, v73\n"
      "v_and_b32 v67, 3, v2\n"
      "v_lshl_add_u32 v73, v67, 4, v73\n"
      "s_mul_i32 s33, s28, 589824\n"
      "s_mul_i32 s97, s72, 147456\n"
      "s_add_u32 s33, s33, s97\n"
      "s_mul_i32 s97, s30, 9216\n"
      "s_add_u32 s33, s33, s97\n"
      "s_lshl_b32 s97, s29, 8\n"
      "s_add_u32 s33, s33, s97\n"
      "s_add_u32 s33, s33, 0x3500000\n"
      "s_add_u32 s18, s33, 4096\n"
      "s_add_u32 s19, s33, 8192\n"
      "s_mul_i32 s22, s28, 589824\n"
      "s_mul_i32 s97, s30, 147456\n"
      "s_add_u32 s22, s22, s97\n"
      "s_mul_i32 s97, s29, 36864\n"
      "s_add_u32 s22, s22, s97\n"
      "s_add_u32 s22, s22, 0x3500000\n"
      "s_add_u32 s12, s22, s78\n"
      "s_mul_i32 s97, s72, 9216\n"
      "s_add_u32 s22, s22, s97\n"
      "s_mul_i32 s21, s74, 147456\n"
      "s_bfe_u32 s97, s75, 0x40001\n"
      "s_lshl_b32 s97, s97, 12\n"
      "s_add_u32 s21, s21, s97\n"
      "s_bfe_u32 s97, s75, 0x10000\n"
      "s_lshl_b32 s97, s97, 10\n"
      "s_add_u32 s21, s21, s97\n"
      "s_bfe_u32 s97, s72, 0x10000\n"
      "s_lshl_b32 s97, s97, 11\n"
      "s_add_u32 s21, s21, s97\n"
      "s_bfe_u32 s97, s72, 0x10001\n"
      "s_lshl_b32 s97, s97, 9\n"
      "s_add_u32 s21, s21, s97\n"
      "s_add_u32 s21, s21, 0x1600000\n"
      "v_lshrrev_b32 v21, 5, v2\n"
      "v_mul_u32_u24 v21, 36864, v21\n"
      "v_and_b32 v66, 31, v2\n"
      "v_lshl_add_u32 v21, v66, 4, v21\n"
      "s_cmp_lt_u32 s75, 18\n"
      "s_cselect_b32 s90, 0, 0x40000000\n"
      "v_add_u32 v21, s90, v21\n"
      "v_min_u32 v67, 17, v2\n"
      "s_lshl_b32 s90, s74, 7\n"
      "s_add_u32 s90, s90, 0x51000\n"
      "v_lshl_add_u32 v22, v67, 10, s90\n"
      "s_mul_i32 s11, s9, 36864\n"
      "s_add_u32 s11, s11, s78\n"
      "s_add_u32 s11, s11, 0x1600000\n"
      "v_mov_b32 v16, -1\n"
      "v_mov_b32 v17, -1\n"
      "v_mov_b32 v18, -1\n"
      "v_mov_b32 v19, -1\n"
      "v_min_u32 v67, 53, v2\n"
      "s_mul_i32 s90, s30, 6912\n"
      "s_add_u32 s90, s90, 0x51000\n"
      "v_lshl_add_u32 v69, v67, 7, s90\n"
      "s_waitcnt vmcnt(0)\n"
      "buffer_store_dwordx2 v[74:75], v76, s[36:39], s16 offen sc1\n"
      "s_barrier\n"
      "s_branch Lfj_11_%=\n"
      "Lf3_12_%=:\n"
      "s_waitcnt vmcnt(3)\n"
      "ds_write_b128 v254, v[224:227]\n"
      "ds_write_b128 v254, v[228:231] offset:1024\n"
      "ds_read_b64_tr_b16 v[224:225], v255\n"
      "ds_read_b64_tr_b16 v[226:227], v255 offset:512\n"
      "ds_read_b64_tr_b16 v[228:229], v255 offset:64\n"
      "ds_read_b64_tr_b16 v[230:231], v255 offset:576\n"
      "ds_write_b128 v254, v[232:235]\n"
      "ds_write_b128 v254, v[236:239] offset:1024\n"
      "ds_read_b64_tr_b16 v[232:233], v255\n"
      "ds_read_b64_tr_b16 v[234:235], v255 offset:512\n"
      "ds_read_b64_tr_b16 v[236:237], v255 offset:64\n"
      "ds_read_b64_tr_b16 v[238:239], v255 offset:576\n"
      "ds_write_b128 v254, v[240:243]\n"
      "ds_write_b128 v254, v[244:247] offset:1024\n"
      "ds_read_b64_tr_b16 v[240:241], v255\n"
      "ds_read_b64_tr_b16 v[242:243], v255 offset:512\n"
      "ds_read_b64_tr_b16 v[244:245], v255 offset:64\n"
      "ds_read_b64_tr_b16 v[246:247], v255 offset:576\n"
      "ds_write_b128 v254, v[88:91]\n"
      "ds_write_b128 v254, v[92:95] offset:1024\n"
      "ds_read_b64_tr_b16 v[88:89], v255\n"
      "ds_read_b64_tr_b16 v[90:91], v255 offset:512\n"
      "ds_read_b64_tr_b16 v[92:93], v255 offset:64\n"
      "ds_read_b64_tr_b16 v[94:95], v255 offset:576\n"
      "s_waitcnt vmcnt(0)\n"
      "s_add_u32 m0, s81, 0\n"
      "s_add_u32 s96, s10, 0\n"
      "buffer_load_dwordx4 v4, s[52:55], s96 offen lds\n"
      "buffer_load_dwordx4 v5, s[52:55], s96 offen offset:1024 lds\n"
      "buffer_load_dwordx4 v6, s[52:55], s96 offen offset:2048 lds\n"
      "buffer_load_dwordx4 v7, s[52:55], s96 offen offset:3072 lds\n"
      "s_sleep 3\n"
      "s_add_u32 m0, s81, 16384\n"
      "s_add_u32 s96, s10, 589824\n"
      "buffer_load_dwordx4 v4, s[52:55], s96 offen lds\n"
      "buffer_load_dwordx4 v5, s[52:55], s96 offen offset:1024 lds\n"
      "buffer_load_dwordx4 v6, s[52:55], s96 offen offset:2048 lds\n"
      "buffer_load_dwordx4 v7, s[52:55], s96 offen offset:3072 lds\n"
      "s_sleep 3\n"
      "s_add_u32 m0, s81, 32768\n"
      "s_add_u32 s96, s10, 1179648\n"
      "buffer_load_dwordx4 v4, s[52:55], s96 offen lds\n"
      "buffer_load_dwordx4 v5, s[52:55], s96 offen offset:1024 lds\n"
      "buffer_load_dwordx4 v6, s[52:55], s96 offen offset:2048 lds\n"
      "buffer_load_dwordx4 v7, s[52:55], s96 offen offset:3072 lds\n"
      "s_sleep 3\n"
      "s_add_u32 m0, s81, 49152\n"
      "s_add_u32 s96, s10, 1769472\n"
      "buffer_load_dwordx4 v4, s[52:55], s96 offen lds\n"
      "buffer_load_dwordx4 v5, s[52:55], s96 offen offset:1024 lds\n"
      "buffer_load_dwordx4 v6, s[52:55], s96 offen offset:2048 lds\n"
      "buffer_load_dwordx4 v7, s[52:55], s96 offen offset:3072 lds\n"
      "s_sleep 3\n"
      "s_add_u32 m0, s81, 65536\n"
      "s_add_u32 s96, s10, 2359296\n"
      "buffer_load_dwordx4 v4, s[52:55], s96 offen lds\n"
      "buffer_load_dwordx4 v5, s[52:55], s96 offen offset:1024 lds\n"
      "buffer_load_dwordx4 v6, s[52:55], s96 offen offset:2048 lds\n"
      "buffer_load_dwordx4 v7, s[52:55], s96 offen offset:3072 lds\n"
      "s_sleep 3\n"
      "s_bfe_u32 s31, s75, 0x30002\n"
      "s_lshl_b32 s90, s31, 2\n"
      "s_add_u32 s90, s90, s72\n"
      "s_bfe_u32 s91, s90, 0x10000\n"
      "s_lshl_b32 s91, s91, 9\n"
      "s_bfe_u32 s97, s90, 0x10004\n"
      "s_lshl_b32 s97, s97, 7\n"
      "s_add_u32 s91, s91, s97\n"
      "s_bfe_u32 s97, s90, 0x30001\n"
      "s_lshl_b32 s97, s97, 12\n"
      "s_add_u32 s91, s91, s97\n"
      "s_add_u32 s91, s91, 0x19000\n"
      "v_lshrrev_b32 v67, 3, v2\n"
      "v_min_u32 v67, 2, v67\n"
      "v_and_b32 v77, 3, v2\n"
      "v_lshlrev_b32 v77, 10, v77\n"
      "v_bfe_u32 v66, v2, 2, 1\n"
      "v_lshl_add_u32 v77, v66, 8, v77\n"
      "v_add_u32 v77, s91, v77\n"
      "v_lshl_add_u32 v77, v67, 15, v77\n"
      "s_mul_i32 s56, s90, 73728\n"
      "s_add_u32 s56, s56, 0xf00000\n"
      "s_mul_i32 s35, s90, 589824\n"
      "s_mul_i32 s97, s30, 73728\n"
      "s_add_u32 s35, s35, s97\n"
      "s_bfe_u32 s97, s9, 0x10002\n"
      "s_lshl_b32 s97, s97, 12\n"
      "s_add_u32 s35, s35, s97\n"
      "s_bfe_u32 s97, s9, 0x10001\n"
      "s_lshl_b32 s97, s97, 10\n"
      "s_add_u32 s35, s35, s97\n"
      "s_bfe_u32 s97, s9, 0x10000\n"
      "s_lshl_b32 s97, s97, 9\n"
      "s_add_u32 s35, s35, s97\n"
      "s_add_u32 s35, s35, 0x2300000\n"
      "v_bfe_u32 v72, v2, 2, 1\n"
      "v_lshlrev_b32 v72, 11, v72\n"
      "v_lshrrev_b32 v66, 5, v2\n"
      "v_lshl_add_u32 v72, v66, 8, v72\n"
      "v_bfe_u32 v66, v2, 4, 1\n"
      "v_lshl_add_u32 v72, v66, 7, v72\n"
      "v_bfe_u32 v66, v2, 3, 1\n"
      "v_lshl_add_u32 v72, v66, 6, v72\n"
      "v_and_b32 v66, 3, v2\n"
      "v_lshl_add_u32 v72, v66, 4, v72\n"
      "s_lshl_b32 s91, s28, 2\n"
      "s_add_u32 s91, s91, s72\n"
      "s_mul_i32 s59, s91, 294912\n"
      "s_mul_i32 s97, s30, 73728\n"
      "s_add_u32 s59, s59, s97\n"
      "s_add_u32 s59, s59, 0x2300000\n"
      "s_lshl_b32 s58, s72, 10\n"
      "s_lshl_b32 s97, s73, 12\n"
      "s_add_u32 s58, s58, s97\n"
      "s_add_u32 s58, s58, 0x3f00000\n"
      "s_bfe_u32 s93, s91, 0x20001\n"
      "s_lshl_b32 s93, s93, 10\n"
      "s_bfe_u32 s97, s28, 0x30001\n"
      "s_lshl_b32 s97, s97, 17\n"
      "s_add_u32 s93, s93, s97\n"
      "s_lshl_b32 s97, s30, 13\n"
      "s_add_u32 s93, s93, s97\n"
      "s_add_u32 s93, s93, 0x3f00000\n"
      "v_and_b32 v23, 3, v2\n"
      "v_lshlrev_b32 v23, 15, v23\n"
      "v_bfe_u32 v66, v2, 2, 1\n"
      "v_lshl_add_u32 v23, v66, 12, v23\n"
      "v_add_u32 v23, s93, v23\n"
      "v_bfe_u32 v73, v2, 2, 2\n"
      "v_mul_u32_u24 v73, 36864, v73\n"
      "v_lshrrev_b32 v67, 5, v2\n"
      "v_lshl_add_u32 v73, v67, 7, v73\n"
      "v_bfe_u32 v67, v2, 4, 1\n"
      "v_lshl_add_u32 v73, v67, 6, v73\n"
      "v_and_b32 v67, 3, v2\n"
      "v_lshl_add_u32 v73, v67, 4, v73\n"
      "s_mul_i32 s33, s28, 589824\n"
      "s_mul_i32 s97, s72, 147456\n"
      "s_add_u32 s33, s33, s97\n"
      "s_mul_i32 s97, s30, 9216\n"
      "s_add_u32 s33, s33, s97\n"
      "s_lshl_b32 s97, s29, 8\n"
      "s_add_u32 s33, s33, s97\n"
      "s_add_u32 s33, s33, 0x3500000\n"
      "s_add_u32 s18, s33, 4096\n"
      "s_add_u32 s19, s33, 8192\n"
      "s_mul_i32 s22, s28, 589824\n"
      "s_mul_i32 s97, s30, 147456\n"
      "s_add_u32 s22, s22, s97\n"
      "s_mul_i32 s97, s29, 36864\n"
      "s_add_u32 s22, s22, s97\n"
      "s_add_u32 s22, s22, 0x3500000\n"
      "s_add_u32 s12, s22, s78\n"
      "s_mul_i32 s97, s72, 9216\n"
      "s_add_u32 s22, s22, s97\n"
      "s_mul_i32 s21, s74, 147456\n"
      "s_bfe_u32 s97, s75, 0x40001\n"
      "s_lshl_b32 s97, s97, 12\n"
      "s_add_u32 s21, s21, s97\n"
      "s_bfe_u32 s97, s75, 0x10000\n"
      "s_lshl_b32 s97, s97, 10\n"
      "s_add_u32 s21, s21, s97\n"
      "s_bfe_u32 s97, s72, 0x10000\n"
      "s_lshl_b32 s97, s97, 11\n"
      "s_add_u32 s21, s21, s97\n"
      "s_bfe_u32 s97, s72, 0x10001\n"
      "s_lshl_b32 s97, s97, 9\n"
      "s_add_u32 s21, s21, s97\n"
      "s_add_u32 s21, s21, 0x1600000\n"
      "v_lshrrev_b32 v21, 5, v2\n"
      "v_mul_u32_u24 v21, 36864, v21\n"
      "v_and_b32 v66, 31, v2\n"
      "v_lshl_add_u32 v21, v66, 4, v21\n"
      "s_cmp_lt_u32 s75, 18\n"
      "s_cselect_b32 s90, 0, 0x40000000\n"
      "v_add_u32 v21, s90, v21\n"
      "v_min_u32 v67, 17, v2\n"
      "s_lshl_b32 s90, s74, 7\n"
      "s_add_u32 s90, s90, 0x51000\n"
      "v_lshl_add_u32 v22, v67, 10, s90\n"
      "s_mul_i32 s11, s9, 36864\n"
      "s_add_u32 s11, s11, s78\n"
      "s_add_u32 s11, s11, 0x1600000\n"
      "v_mov_b32 v16, -1\n"
      "v_mov_b32 v17, -1\n"
      "v_mov_b32 v18, -1\n"
      "v_mov_b32 v19, -1\n"
      "v_min_u32 v67, 53, v2\n"
      "s_mul_i32 s90, s30, 6912\n"
      "s_add_u32 s90, s90, 0x51000\n"
      "v_lshl_add_u32 v69, v67, 7, s90\n"
      "s_getpc_b64 s[14:15]\n"
      "Lwh_18_%=:\n"
      "s_add_u32 s14, s14, Lwb_19_%=-Lwh_18_%=\n"
      "s_addc_u32 s15, s15, 0\n"
      "s_mov_b64 exec, 0\n"
      "s_branch Lpad6_%=\n"
      "Lwb_19_%=:\n"
      "s_mov_b64 exec, -1\n"
      "s_barrier\n"
      "s_waitcnt vmcnt(0) lgkmcnt(0)\n"
      "s_barrier\n"
      "Lfj_11_%=:\n"
      "s_nop 4\n"
      "s_add_u32 m0, s81, 81920\n"
      "s_add_u32 s96, s10, 2949120\n"
      "buffer_load_dwordx4 v4, s[52:55], s96 offen lds\n"
      "buffer_load_dwordx4 v5, s[52:55], s96 offen offset:1024 lds\n"
      "buffer_load_dwordx4 v6, s[52:55], s96 offen offset:2048 lds\n"
      "buffer_load_dwordx4 v7, s[52:55], s96 offen offset:3072 lds\n"
      "s_add_u32 m0, s81, 98304\n"
      "s_add_u32 s96, s10, 3538944\n"
      "buffer_load_dwordx4 v4, s[52:55], s96 offen lds\n"
      "buffer_load_dwordx4 v5, s[52:55], s96 offen offset:1024 lds\n"
      "buffer_load_dwordx4 v6, s[52:55], s96 offen offset:2048 lds\n"
      "buffer_load_dwordx4 v7, s[52:55], s96 offen offset:3072 lds\n"
      "s_add_u32 m0, s81, 114688\n"
      "s_add_u32 s96, s10, 4128768\n"
      "buffer_load_dwordx4 v4, s[52:55], s96 offen lds\n"
      "buffer_load_dwordx4 v5, s[52:55], s96 offen offset:1024 lds\n"
      "buffer_load_dwordx4 v6, s[52:55], s96 offen offset:2048 lds\n"
      "buffer_load_dwordx4 v7, s[52:55], s96 offen offset:3072 lds\n"
      "s_add_u32 m0, s81, 131072\n"
      "s_add_u32 s96, s10, 4718592\n"
      "buffer_load_dwordx4 v4, s[52:55], s96 offen lds\n"
      "buffer_load_dwordx4 v5, s[52:55], s96 offen offset:1024 lds\n"
      "buffer_load_dwordx4 v6, s[52:55], s96 offen offset:2048 lds\n"
      "buffer_load_dwordx4 v7, s[52:55], s96 offen offset:3072 lds\n"
      "s_mov_b32 s8, 0\n"
      "Lpoll_20_%=:\n"
      "buffer_load_dwordx2 v[70:71], v77, s[36:39], 0 offen sc1\n"
      "s_add_u32 s8, s8, 1\n"
      "s_waitcnt vmcnt(0) lgkmcnt(0)\n"
      "v_cmp_ne_u64 vcc, s[76:77], v[70:71]\n"
      "s_cbranch_vccz Lpolld_21_%=\n"
      "s_cmp_lt_u32 s8, 40000\n"
      "s_cbranch_scc1 Lpoll_20_%=\n"
      "Lpolld_21_%=:\n"
      "s_add_u32 s93, s56, 0\n"
      "buffer_load_dwordx4 a[0:3], v1, s[36:39], s93 offen sc1\n"
      "buffer_load_dwordx4 a[4:7], v1, s[36:39], s93 offen offset:1024 sc1\n"
      "buffer_load_dwordx4 a[8:11], v1, s[36:39], s93 offen offset:2048 sc1\n"
      "buffer_load_dwordx4 a[12:15], v1, s[36:39], s93 offen offset:3072 sc1\n"
      "s_add_u32 s93, s56, 4096\n"
      "buffer_load_dwordx4 a[16:19], v1, s[36:39], s93 offen sc1\n"
      "buffer_load_dwordx4 a[20:23], v1, s[36:39], s93 offen offset:1024 sc1\n"
      "buffer_load_dwordx4 a[24:27], v1, s[36:39], s93 offen offset:2048 sc1\n"
      "buffer_load_dwordx4 a[28:31], v1, s[36:39], s93 offen offset:3072 sc1\n"
      "s_add_u32 s93, s56, 8192\n"
      "buffer_load_dwordx4 a[32:35], v1, s[36:39], s93 offen sc1\n"
      "buffer_load_dwordx4 a[36:39], v1, s[36:39], s93 offen offset:1024 sc1\n"
      "buffer_load_dwordx4 a[40:43], v1, s[36:39], s93 offen offset:2048 sc1\n"
      "buffer_load_dwordx4 a[44:47], v1, s[36:39], s93 offen offset:3072 sc1\n"
      "s_add_u32 s93, s56, 12288\n"
      "buffer_load_dwordx4 a[48:51], v1, s[36:39], s93 offen sc1\n"
      "buffer_load_dwordx4 a[52:55], v1, s[36:39], s93 offen offset:1024 sc1\n"
      "buffer_load_dwordx4 a[56:59], v1, s[36:39], s93 offen offset:2048 sc1\n"
      "buffer_load_dwordx4 a[60:63], v1, s[36:39], s93 offen offset:3072 sc1\n"
      "v_xor_b32 v11, 0x0, v9\n"
      "ds_read_b64_tr_b16 v[32:33], v11\n"
      "ds_read_b64_tr_b16 v[34:35], v11 offset:512\n"
      "ds_read_b64_tr_b16 v[36:37], v11 offset:8192\n"
      "ds_read_b64_tr_b16 v[38:39], v11 offset:8704\n"
      "ds_read_b64_tr_b16 v[40:41], v11 offset:4096\n"
      "ds_read_b64_tr_b16 v[42:43], v11 offset:4608\n"
      "ds_read_b64_tr_b16 v[44:45], v11 offset:12288\n"
      "ds_read_b64_tr_b16 v[46:47], v11 offset:12800\n"
      "v_xor_b32 v13, 0x20, v9\n"
      "ds_read_b64_tr_b16 v[48:49], v13\n"
      "ds_read_b64_tr_b16 v[50:51], v13 offset:512\n"
      "ds_read_b64_tr_b16 v[52:53], v13 offset:8192\n"
      "ds_read_b64_tr_b16 v[54:55], v13 offset:8704\n"
      "s_waitcnt lgkmcnt(8)\n"
      "s_waitcnt vmcnt(12)\n"
      "v_mfma_f32_16x16x32_bf16 v[96:99], v[32:35], a[0:3], 0\n"
      "ds_read_b64_tr_b16 v[56:57], v13 offset:4096\n"
      "ds_read_b64_tr_b16 v[58:59], v13 offset:4608\n"
      "v_mfma_f32_16x16x32_bf16 v[96:99], v[36:39], a[4:7], v[96:99]\n"
      "s_add_u32 s93, s56, 16384\n"
      "buffer_load_dwordx4 a[64:67], v1, s[36:39], s93 offen sc1\n"
      "v_mfma_f32_16x16x32_bf16 v[100:103], v[32:35], a[8:11], 0\n"
      "v_mfma_f32_16x16x32_bf16 v[100:103], v[36:39], a[12:15], v[100:103]\n"
      "s_waitcnt vmcnt(9)\n"
      "v_mfma_f32_16x16x32_bf16 v[104:107], v[32:35], a[16:19], 0\n"
      "ds_read_b64_tr_b16 v[60:61], v13 offset:12288\n"
      "ds_read_b64_tr_b16 v[62:63], v13 offset:12800\n"
      "v_mfma_f32_16x16x32_bf16 v[104:107], v[36:39], a[20:23], v[104:107]\n"
      "buffer_load_dwordx4 a[68:71], v1, s[36:39], s93 offen offset:1024 sc1\n"
      "v_mfma_f32_16x16x32_bf16 v[108:111], v[32:35], a[24:27], 0\n"
      "v_mfma_f32_16x16x32_bf16 v[108:111], v[36:39], a[28:31], v[108:111]\n"
      "v_xor_b32 v11, 0x40, v9\n"
      "s_waitcnt lgkmcnt(8)\n"
      "v_mfma_f32_16x16x32_bf16 v[112:115], v[40:43], a[0:3], 0\n"
      "ds_read_b64_tr_b16 v[32:33], v11\n"
      "ds_read_b64_tr_b16 v[34:35], v11 offset:512\n"
      "v_mfma_f32_16x16x32_bf16 v[112:115], v[44:47], a[4:7], v[112:115]\n"
      "v_mfma_f32_16x16x32_bf16 v[116:119], v[40:43], a[8:11], 0\n"
      "buffer_load_dwordx4 a[72:75], v1, s[36:39], s93 offen offset:2048 sc1\n"
      "v_mfma_f32_16x16x32_bf16 v[116:119], v[44:47], a[12:15], v[116:119]\n"
      "v_mfma_f32_16x16x32_bf16 v[120:123], v[40:43], a[16:19], 0\n"
      "ds_read_b64_tr_b16 v[36:37], v11 offset:8192\n"
      "ds_read_b64_tr_b16 v[38:39], v11 offset:8704\n"
      "v_mfma_f32_16x16x32_bf16 v[120:123], v[44:47], a[20:23], v[120:123]\n"
      "v_mfma_f32_16x16x32_bf16 v[124:127], v[40:43], a[24:27], 0\n"
      "v_mfma_f32_16x16x32_bf16 v[124:127], v[44:47], a[28:31], v[124:127]\n"
      "s_waitcnt lgkmcnt(8)\n"
      "buffer_load_dwordx4 a[76:79], v1, s[36:39], s93 offen offset:3072 sc1\n"
      "v_mfma_f32_16x16x32_bf16 v[128:131], v[48:51], a[0:3], 0\n"
      "ds_read_b64_tr_b16 v[40:41], v11 offset:4096\n"
      "ds_read_b64_tr_b16 v[42:43], v11 offset:4608\n"
      "v_mfma_f32_16x16x32_bf16 v[128:131], v[52:55], a[4:7], v[128:131]\n"
      "v_mfma_f32_16x16x32_bf16 v[132:135], v[48:51], a[8:11], 0\n"
      "s_add_u32 s93, s21, 0\n"
      "v_mfma_f32_16x16x32_bf16 v[132:135], v[52:55], a[12:15], v[132:135]\n"
      "v_mfma_f32_16x16x32_bf16 v[136:139], v[48:51], a[16:19], 0\n"
      "ds_read_b64_tr_b16 v[44:45], v11 offset:12288\n"
      "ds_read_b64_tr_b16 v[46:47], v11 offset:12800\n"
      "v_mfma_f32_16x16x32_bf16 v[136:139], v[52:55], a[20:23], v[136:139]\n"
      "buffer_store_dwordx4 v[88:91], v21, s[36:39], s93 offen sc1\n"
      "v_mfma_f32_16x16x32_bf16 v[140:143], v[48:51], a[24:27], 0\n"
      "v_mfma_f32_16x16x32_bf16 v[140:143], v[52:55], a[28:31], v[140:143]\n"
      "v_xor_b32 v13, 0x60, v9\n"
      "s_waitcnt lgkmcnt(8)\n"
      "v_mfma_f32_16x16x32_bf16 v[144:147], v[56:59], a[0:3], 0\n"
      "ds_read_b64_tr_b16 v[48:49], v13\n"
      "ds_read_b64_tr_b16 v[50:51], v13 offset:512\n"
      "v_mfma_f32_16x16x32_bf16 v[144:147], v[60:63], a[4:7], v[144:147]\n"
      "v_mfma_f32_16x16x32_bf16 v[148:151], v[56:59], a[8:11], 0\n"
      "v_mfma_f32_16x16x32_bf16 v[148:151], v[60:63], a[12:15], v[148:151]\n"
      "v_mfma_f32_16x16x32_bf16 v[152:155], v[56:59], a[16:19], 0\n"
      "ds_read_b64_tr_b16 v[52:53], v13 offset:8192\n"
      "ds_read_b64_tr_b16 v[54:55], v13 offset:8704\n"
      "v_mfma_f32_16x16x32_bf16 v[152:155], v[60:63], a[20:23], v[152:155]\n"
      "s_add_u32 s93, s21, 73728\n"
      "v_mfma_f32_16x16x32_bf16 v[156:159], v[56:59], a[24:27], 0\n"
      "buffer_store_dwordx4 v[92:95], v21, s[36:39], s93 offen sc1\n"
      "v_mfma_f32_16x16x32_bf16 v[156:159], v[60:63], a[28:31], v[156:159]\n"
      "s_waitcnt lgkmcnt(8)\n"
      "v_mfma_f32_16x16x32_bf16 v[160:163], v[32:35], a[0:3], 0\n"
      "ds_read_b64_tr_b16 v[56:57], v13 offset:4096\n"
      "ds_read_b64_tr_b16 v[58:59], v13 offset:4608\n"
      "v_mfma_f32_16x16x32_bf16 v[160:163], v[36:39], a[4:7], v[160:163]\n"
      "v_mfma_f32_16x16x32_bf16 v[164:167], v[32:35], a[8:11], 0\n"
      "v_mfma_f32_16x16x32_bf16 v[164:167], v[36:39], a[12:15], v[164:167]\n"
      "v_mfma_f32_16x16x32_bf16 v[168:171], v[32:35], a[16:19], 0\n"
      "ds_read_b64_tr_b16 v[60:61], v13 offset:12288\n"
      "ds_read_b64_tr_b16 v[62:63], v13 offset:12800\n"
      "v_mfma_f32_16x16x32_bf16 v[168:171], v[36:39], a[20:23], v[168:171]\n"
      "v_mfma_f32_16x16x32_bf16 v[172:175], v[32:35], a[24:27], 0\n"
      "v_mfma_f32_16x16x32_bf16 v[172:175], v[36:39], a[28:31], v[172:175]\n"
      "v_xor_b32 v11, 0x4000, v9\n"
      "s_waitcnt lgkmcnt(8)\n"
      "v_mfma_f32_16x16x32_bf16 v[176:179], v[40:43], a[0:3], 0\n"
      "ds_read_b64_tr_b16 v[32:33], v11\n"
      "ds_read_b64_tr_b16 v[34:35], v11 offset:512\n"
      "v_mfma_f32_16x16x32_bf16 v[176:179], v[44:47], a[4:7], v[176:179]\n"
      "buffer_store_dwordx4 v[224:227], v248, s[68:71], s5 offen sc1\n"
      "v_mfma_f32_16x16x32_bf16 v[180:183], v[40:43], a[8:11], 0\n"
      "v_mfma_f32_16x16x32_bf16 v[180:183], v[44:47], a[12:15], v[180:183]\n"
      "v_mfma_f32_16x16x32_bf16 v[184:187], v[40:43], a[16:19], 0\n"
      "ds_read_b64_tr_b16 v[36:37], v11 offset:8192\n"
      "ds_read_b64_tr_b16 v[38:39], v11 offset:8704\n"
      "v_mfma_f32_16x16x32_bf16 v[184:187], v[44:47], a[20:23], v[184:187]\n"
      "v_mfma_f32_16x16x32_bf16 v[188:191], v[40:43], a[24:27], 0\n"
      "v_mfma_f32_16x16x32_bf16 v[188:191], v[44:47], a[28:31], v[188:191]\n"
      "s_waitcnt lgkmcnt(8)\n"
      "v_mfma_f32_16x16x32_bf16 v[192:195], v[48:51], a[0:3], 0\n"
      "ds_read_b64_tr_b16 v[40:41], v11 offset:4096\n"
      "Lpad6_%=:\n"
      "s_cbranch_execz Lpad7_%=\n"
      "ds_read_b64_tr_b16 v[42:43], v11 offset:4608\n"
      "v_mfma_f32_16x16x32_bf16 v[192:195], v[52:55], a[4:7], v[192:195]\n"
      "v_mfma_f32_16x16x32_bf16 v[196:199], v[48:51], a[8:11], 0\n"
      "buffer_store_dwordx4 v[228:231], v249, s[68:71], s5 offen sc1\n"
      "v_mfma_f32_16x16x32_bf16 v[196:199], v[52:55], a[12:15], v[196:199]\n"
      "v_mfma_f32_16x16x32_bf16 v[200:203], v[48:51], a[16:19], 0\n"
      "ds_read_b64_tr_b16 v[44:45], v11 offset:12288\n"
      "ds_read_b64_tr_b16 v[46:47], v11 offset:12800\n"
      "v_mfma_f32_16x16x32_bf16 v[200:203], v[52:55], a[20:23], v[200:203]\n"
      "v_mfma_f32_16x16x32_bf16 v[204:207], v[48:51], a[24:27], 0\n"
      "v_mfma_f32_16x16x32_bf16 v[204:207], v[52:55], a[28:31], v[204:207]\n"
      "v_xor_b32 v13, 0x4020, v9\n"
      "s_waitcnt lgkmcnt(8)\n"
      "v_mfma_f32_16x16x32_bf16 v[208:211], v[56:59], a[0:3], 0\n"
      "ds_read_b64_tr_b16 v[48:49], v13\n"
      "ds_read_b64_tr_b16 v[50:51], v13 offset:512\n"
      "v_mfma_f32_16x16x32_bf16 v[208:211], v[60:63], a[4:7], v[208:211]\n"
      "v_mfma_f32_16x16x32_bf16 v[212:215], v[56:59], a[8:11], 0\n"
      "s_add_u32 s93, s56, 20480\n"
      "v_mfma_f32_16x16x32_bf16 v[212:215], v[60:63], a[12:15], v[212:215]\n"
      "v_mfma_f32_16x16x32_bf16 v[216:219], v[56:59], a[16:19], 0\n"
      "ds_read_b64_tr_b16 v[52:53], v13 offset:8192\n"
      "ds_read_b64_tr_b16 v[54:55], v13 offset:8704\n"
      "v_mfma_f32_16x16x32_bf16 v[216:219], v[60:63], a[20:23], v[216:219]\n"
      "buffer_load_dwordx4 a[80:83], v1, s[36:39], s93 offen sc1\n"
      "v_mfma_f32_16x16x32_bf16 v[220:223], v[56:59], a[24:27], 0\n"
      "v_mfma_f32_16x16x32_bf16 v[220:223], v[60:63], a[28:31], v[220:223]\n"
      "s_waitcnt lgkmcnt(8)\n"
      "s_waitcnt vmcnt(13)\n"
      "v_mfma_f32_16x16x32_bf16 v[96:99], v[32:35], a[32:35], v[96:99]\n"
      "ds_read_b64_tr_b16 v[56:57], v13 offset:4096\n"
      "ds_read_b64_tr_b16 v[58:59], v13 offset:4608\n"
      "v_mfma_f32_16x16x32_bf16 v[96:99], v[36:39], a[36:39], v[96:99]\n"
      "buffer_load_dwordx4 a[84:87], v1, s[36:39], s93 offen offset:1024 sc1\n"
      "v_mfma_f32_16x16x32_bf16 v[100:103], v[32:35], a[40:43], v[100:103]\n"
      "v_mfma_f32_16x16x32_bf16 v[100:103], v[36:39], a[44:47], v[100:103]\n"
      "s_waitcnt vmcnt(10)\n"
      "v_mfma_f32_16x16x32_bf16 v[104:107], v[32:35], a[48:51], v[104:107]\n"
      "ds_read_b64_tr_b16 v[60:61], v13 offset:12288\n"
      "ds_read_b64_tr_b16 v[62:63], v13 offset:12800\n"
      "v_mfma_f32_16x16x32_bf16 v[104:107], v[36:39], a[52:55], v[104:107]\n"
      "v_mfma_f32_16x16x32_bf16 v[108:111], v[32:35], a[56:59], v[108:111]\n"
      "buffer_load_dwordx4 a[88:91], v1, s[36:39], s93 offen offset:2048 sc1\n"
      "v_mfma_f32_16x16x32_bf16 v[108:111], v[36:39], a[60:63], v[108:111]\n"
      "v_xor_b32 v11, 0x4040, v9\n"
      "s_waitcnt lgkmcnt(8)\n"
      "v_mfma_f32_16x16x32_bf16 v[112:115], v[40:43], a[32:35], v[112:115]\n"
      "ds_read_b64_tr_b16 v[32:33], v11\n"
      "ds_read_b64_tr_b16 v[34:35], v11 offset:512\n"
      "v_mfma_f32_16x16x32_bf16 v[112:115], v[44:47], a[36:39], v[112:115]\n"
      "v_mfma_f32_16x16x32_bf16 v[116:119], v[40:43], a[40:43], v[116:119]\n"
      "v_mfma_f32_16x16x32_bf16 v[116:119], v[44:47], a[44:47], v[116:119]\n"
      "buffer_load_dwordx4 a[92:95], v1, s[36:39], s93 offen offset:3072 sc1\n"
      "v_mfma_f32_16x16x32_bf16 v[120:123], v[40:43], a[48:51], v[120:123]\n"
      "ds_read_b64_tr_b16 v[36:37], v11 offset:8192\n"
      "ds_read_b64_tr_b16 v[38:39], v11 offset:8704\n"
      "v_mfma_f32_16x16x32_bf16 v[120:123], v[44:47], a[52:55], v[120:123]\n"
      "v_mfma_f32_16x16x32_bf16 v[124:127], v[40:43], a[56:59], v[124:127]\n"
      "v_mfma_f32_16x16x32_bf16 v[124:127], v[44:47], a[60:63], v[124:127]\n"
      "s_waitcnt lgkmcnt(8)\n"
      "v_mfma_f32_16x16x32_bf16 v[128:131], v[48:51], a[32:35], v[128:131]\n"
      "ds_read_b64_tr_b16 v[40:41], v11 offset:4096\n"
      "ds_read_b64_tr_b16 v[42:43], v11 offset:4608\n"
      "v_mfma_f32_16x16x32_bf16 v[128:131], v[52:55], a[36:39], v[128:131]\n"
      "buffer_store_dwordx4 v[232:235], v250, s[68:71], s5 offen sc1\n"
      "v_mfma_f32_16x16x32_bf16 v[132:135], v[48:51], a[40:43], v[132:135]\n"
      "v_mfma_f32_16x16x32_bf16 v[132:135], v[52:55], a[44:47], v[132:135]\n"
      "v_mfma_f32_16x16x32_bf16 v[136:139], v[48:51], a[48:51], v[136:139]\n"
      "ds_read_b64_tr_b16 v[44:45], v11 offset:12288\n"
      "ds_read_b64_tr_b16 v[46:47], v11 offset:12800\n"
      "v_mfma_f32_16x16x32_bf16 v[136:139], v[52:55], a[52:55], v[136:139]\n"
      "v_mfma_f32_16x16x32_bf16 v[140:143], v[48:51], a[56:59], v[140:143]\n"
      "v_mfma_f32_16x16x32_bf16 v[140:143], v[52:55], a[60:63], v[140:143]\n"
      "v_xor_b32 v13, 0x4060, v9\n"
      "s_waitcnt lgkmcnt(8)\n"
      "v_mfma_f32_16x16x32_bf16 v[144:147], v[56:59], a[32:35], v[144:147]\n"
      "ds_read_b64_tr_b16 v[48:49], v13\n"
      "ds_read_b64_tr_b16 v[50:51], v13 offset:512\n"
      "v_mfma_f32_16x16x32_bf16 v[144:147], v[60:63], a[36:39], v[144:147]\n"
      "v_mfma_f32_16x16x32_bf16 v[148:151], v[56:59], a[40:43], v[148:151]\n"
      "buffer_store_dwordx4 v[236:239], v251, s[68:71], s5 offen sc1\n"
      "v_mfma_f32_16x16x32_bf16 v[148:151], v[60:63], a[44:47], v[148:151]\n"
      "v_mfma_f32_16x16x32_bf16 v[152:155], v[56:59], a[48:51], v[152:155]\n"
      "ds_read_b64_tr_b16 v[52:53], v13 offset:8192\n"
      "ds_read_b64_tr_b16 v[54:55], v13 offset:8704\n"
      "v_mfma_f32_16x16x32_bf16 v[152:155], v[60:63], a[52:55], v[152:155]\n"
      "v_mfma_f32_16x16x32_bf16 v[156:159], v[56:59], a[56:59], v[156:159]\n"
      "v_mfma_f32_16x16x32_bf16 v[156:159], v[60:63], a[60:63], v[156:159]\n"
      "s_waitcnt lgkmcnt(8)\n"
      "v_mfma_f32_16x16x32_bf16 v[160:163], v[32:35], a[32:35], v[160:163]\n"
      "ds_read_b64_tr_b16 v[56:57], v13 offset:4096\n"
      "ds_read_b64_tr_b16 v[58:59], v13 offset:4608\n"
      "v_mfma_f32_16x16x32_bf16 v[160:163], v[36:39], a[36:39], v[160:163]\n"
      "v_mfma_f32_16x16x32_bf16 v[164:167], v[32:35], a[40:43], v[164:167]\n"
      "v_mfma_f32_16x16x32_bf16 v[164:167], v[36:39], a[44:47], v[164:167]\n"
      "v_mfma_f32_16x16x32_bf16 v[168:171], v[32:35], a[48:51], v[168:171]\n"
      "ds_read_b64_tr_b16 v[60:61], v13 offset:12288\n"
      "ds_read_b64_tr_b16 v[62:63], v13 offset:12800\n"
      "v_mfma_f32_16x16x32_bf16 v[168:171], v[36:39], a[52:55], v[168:171]\n"
      "buffer_store_dwordx4 v[240:243], v252, s[68:71], s5 offen sc1\n"
      "v_mfma_f32_16x16x32_bf16 v[172:175], v[32:35], a[56:59], v[172:175]\n"
      "v_mfma_f32_16x16x32_bf16 v[172:175], v[36:39], a[60:63], v[172:175]\n"
      "v_xor_b32 v11, 0x8000, v9\n"
      "s_waitcnt lgkmcnt(8)\n"
      "v_mfma_f32_16x16x32_bf16 v[176:179], v[40:43], a[32:35], v[176:179]\n"
      "ds_read_b64_tr_b16 v[32:33], v11\n"
      "ds_read_b64_tr_b16 v[34:35], v11 offset:512\n"
      "v_mfma_f32_16x16x32_bf16 v[176:179], v[44:47], a[36:39], v[176:179]\n"
      "v_mfma_f32_16x16x32_bf16 v[180:183], v[40:43], a[40:43], v[180:183]\n"
      "v_mfma_f32_16x16x32_bf16 v[180:183], v[44:47], a[44:47], v[180:183]\n"
      "v_mfma_f32_16x16x32_bf16 v[184:187], v[40:43], a[48:51], v[184:187]\n"
      "ds_read_b64_tr_b16 v[36:37], v11 offset:8192\n"
      "ds_read_b64_tr_b16 v[38:39], v11 offset:8704\n"
      "v_mfma_f32_16x16x32_bf16 v[184:187], v[44:47], a[52:55], v[184:187]\n"
      "v_mfma_f32_16x16x32_bf16 v[188:191], v[40:43], a[56:59], v[188:191]\n"
      "buffer_store_dwordx4 v[244:247], v253, s[68:71], s5 offen sc1\n"
      "v_mfma_f32_16x16x32_bf16 v[188:191], v[44:47], a[60:63], v[188:191]\n"
      "s_waitcnt lgkmcnt(8)\n"
      "v_mfma_f32_16x16x32_bf16 v[192:195], v[48:51], a[32:35], v[192:195]\n"
      "ds_read_b64_tr_b16 v[40:41], v11 offset:4096\n"
      "ds_read_b64_tr_b16 v[42:43], v11 offset:4608\n"
      "v_mfma_f32_16x16x32_bf16 v[192:195], v[52:55], a[36:39], v[192:195]\n"
      "v_mfma_f32_16x16x32_bf16 v[196:199], v[48:51], a[40:43], v[196:199]\n"
      "v_mfma_f32_16x16x32_bf16 v[196:199], v[52:55], a[44:47], v[196:199]\n"
      "v_mfma_f32_16x16x32_bf16 v[200:203], v[48:51], a[48:51], v[200:203]\n"
      "ds_read_b64_tr_b16 v[44:45], v11 offset:12288\n"
      "ds_read_b64_tr_b16 v[46:47], v11 offset:12800\n"
      "v_mfma_f32_16x16x32_bf16 v[200:203], v[52:55], a[52:55], v[200:203]\n"
      "v_mfma_f32_16x16x32_bf16 v[204:207], v[48:51], a[56:59], v[204:207]\n"
      "s_add_u32 s93, s56, 24576\n"
      "v_mfma_f32_16x16x32_bf16 v[204:207], v[52:55], a[60:63], v[204:207]\n"
      "v_xor_b32 v13, 0x8020, v9\n"
      "s_waitcnt lgkmcnt(8)\n"
      "v_mfma_f32_16x16x32_bf16 v[208:211], v[56:59], a[32:35], v[208:211]\n"
      "ds_read_b64_tr_b16 v[48:49], v13\n"
      "ds_read_b64_tr_b16 v[50:51], v13 offset:512\n"
      "v_mfma_f32_16x16x32_bf16 v[208:211], v[60:63], a[36:39], v[208:211]\n"
      "buffer_load_dwordx4 a[96:99], v1, s[36:39], s93 offen sc1\n"
      "v_mfma_f32_16x16x32_bf16 v[212:215], v[56:59], a[40:43], v[212:215]\n"
      "v_mfma_f32_16x16x32_bf16 v[212:215], v[60:63], a[44:47], v[212:215]\n"
      "v_mfma_f32_16x16x32_bf16 v[216:219], v[56:59], a[48:51], v[216:219]\n"
      "ds_read_b64_tr_b16 v[52:53], v13 offset:8192\n"
      "ds_read_b64_tr_b16 v[54:55], v13 offset:8704\n"
      "v_mfma_f32_16x16x32_bf16 v[216:219], v[60:63], a[52:55], v[216:219]\n"
      "buffer_load_dwordx4 a[100:103], v1, s[36:39], s93 offen offset:1024 sc1\n"
      "v_mfma_f32_16x16x32_bf16 v[220:223], v[56:59], a[56:59], v[220:223]\n"
      "v_mfma_f32_16x16x32_bf16 v[220:223], v[60:63], a[60:63], v[220:223]\n"
      "s_waitcnt lgkmcnt(8)\n"
      "s_waitcnt vmcnt(14)\n"
      "v_mfma_f32_16x16x32_bf16 v[96:99], v[32:35], a[64:67], v[96:99]\n"
      "ds_read_b64_tr_b16 v[56:57], v13 offset:4096\n"
      "ds_read_b64_tr_b16 v[58:59], v13 offset:4608\n"
      "v_mfma_f32_16x16x32_bf16 v[96:99], v[36:39], a[68:71], v[96:99]\n"
      "v_mfma_f32_16x16x32_bf16 v[100:103], v[32:35], a[72:75], v[100:103]\n"
      "buffer_load_dwordx4 a[104:107], v1, s[36:39], s93 offen offset:2048 sc1\n"
      "v_mfma_f32_16x16x32_bf16 v[100:103], v[36:39], a[76:79], v[100:103]\n"
      "s_waitcnt vmcnt(7)\n"
      "v_mfma_f32_16x16x32_bf16 v[104:107], v[32:35], a[80:83], v[104:107]\n"
      "ds_read_b64_tr_b16 v[60:61], v13 offset:12288\n"
      "ds_read_b64_tr_b16 v[62:63], v13 offset:12800\n"
      "v_mfma_f32_16x16x32_bf16 v[104:107], v[36:39], a[84:87], v[104:107]\n"
      "v_mfma_f32_16x16x32_bf16 v[108:111], v[32:35], a[88:91], v[108:111]\n"
      "v_mfma_f32_16x16x32_bf16 v[108:111], v[36:39], a[92:95], v[108:111]\n"
      "v_xor_b32 v11, 0x8040, v9\n"
      "s_waitcnt lgkmcnt(8)\n"
      "v_mfma_f32_16x16x32_bf16 v[112:115], v[40:43], a[64:67], v[112:115]\n"
      "ds_read_b64_tr_b16 v[32:33], v11\n"
      "ds_read_b64_tr_b16 v[34:35], v11 offset:512\n"
      "v_mfma_f32_16x16x32_bf16 v[112:115], v[44:47], a[68:71], v[112:115]\n"
      "buffer_load_dwordx4 a[108:111], v1, s[36:39], s93 offen offset:3072 sc1\n"
      "v_mfma_f32_16x16x32_bf16 v[116:119], v[40:43], a[72:75], v[116:119]\n"
      "s_add_u32 s93, s56, 28672\n"
      "v_mfma_f32_16x16x32_bf16 v[116:119], v[44:47], a[76:79], v[116:119]\n"
      "v_mfma_f32_16x16x32_bf16 v[120:123], v[40:43], a[80:83], v[120:123]\n"
      "ds_read_b64_tr_b16 v[36:37], v11 offset:8192\n"
      "ds_read_b64_tr_b16 v[38:39], v11 offset:8704\n"
      "v_mfma_f32_16x16x32_bf16 v[120:123], v[44:47], a[84:87], v[120:123]\n"
      "buffer_load_dwordx4 a[112:115], v1, s[36:39], s93 offen sc1\n"
      "v_mfma_f32_16x16x32_bf16 v[124:127], v[40:43], a[88:91], v[124:127]\n"
      "v_mfma_f32_16x16x32_bf16 v[124:127], v[44:47], a[92:95], v[124:127]\n"
      "s_waitcnt lgkmcnt(8)\n"
      "v_mfma_f32_16x16x32_bf16 v[128:131], v[48:51], a[64:67], v[128:131]\n"
      "ds_read_b64_tr_b16 v[40:41], v11 offset:4096\n"
      "ds_read_b64_tr_b16 v[42:43], v11 offset:4608\n"
      "v_mfma_f32_16x16x32_bf16 v[128:131], v[52:55], a[68:71], v[128:131]\n"
      "buffer_load_dwordx4 a[116:119], v1, s[36:39], s93 offen offset:1024 sc1\n"
      "v_mfma_f32_16x16x32_bf16 v[132:135], v[48:51], a[72:75], v[132:135]\n"
      "v_mfma_f32_16x16x32_bf16 v[132:135], v[52:55], a[76:79], v[132:135]\n"
      "v_mfma_f32_16x16x32_bf16 v[136:139], v[48:51], a[80:83], v[136:139]\n"
      "ds_read_b64_tr_b16 v[44:45], v11 offset:12288\n"
      "ds_read_b64_tr_b16 v[46:47], v11 offset:12800\n"
      "v_mfma_f32_16x16x32_bf16 v[136:139], v[52:55], a[84:87], v[136:139]\n"
      "v_mfma_f32_16x16x32_bf16 v[140:143], v[48:51], a[88:91], v[140:143]\n"
      "buffer_load_dwordx4 a[120:123], v1, s[36:39], s93 offen offset:2048 sc1\n"
      "v_mfma_f32_16x16x32_bf16 v[140:143], v[52:55], a[92:95], v[140:143]\n"
      "v_xor_b32 v13, 0x8060, v9\n"
      "s_waitcnt lgkmcnt(8)\n"
      "v_mfma_f32_16x16x32_bf16 v[144:147], v[56:59], a[64:67], v[144:147]\n"
      "ds_read_b64_tr_b16 v[48:49], v13\n"
      "ds_read_b64_tr_b16 v[50:51], v13 offset:512\n"
      "v_mfma_f32_16x16x32_bf16 v[144:147], v[60:63], a[68:71], v[144:147]\n"
      "v_mfma_f32_16x16x32_bf16 v[148:151], v[56:59], a[72:75], v[148:151]\n"
      "v_mfma_f32_16x16x32_bf16 v[148:151], v[60:63], a[76:79], v[148:151]\n"
      "buffer_load_dwordx4 a[124:127], v1, s[36:39], s93 offen offset:3072 sc1\n"
      "v_mfma_f32_16x16x32_bf16 v[152:155], v[56:59], a[80:83], v[152:155]\n"
      "ds_read_b64_tr_b16 v[52:53], v13 offset:8192\n"
      "ds_read_b64_tr_b16 v[54:55], v13 offset:8704\n"
      "v_mfma_f32_16x16x32_bf16 v[152:155], v[60:63], a[84:87], v[152:155]\n"
      "v_mfma_f32_16x16x32_bf16 v[156:159], v[56:59], a[88:91], v[156:159]\n"
      "s_add_u32 s93, s56, 32768\n"
      "v_mfma_f32_16x16x32_bf16 v[156:159], v[60:63], a[92:95], v[156:159]\n"
      "s_waitcnt lgkmcnt(8)\n"
      "v_mfma_f32_16x16x32_bf16 v[160:163], v[32:35], a[64:67], v[160:163]\n"
      "ds_read_b64_tr_b16 v[56:57], v13 offset:4096\n"
      "ds_read_b64_tr_b16 v[58:59], v13 offset:4608\n"
      "v_mfma_f32_16x16x32_bf16 v[160:163], v[36:39], a[68:71], v[160:163]\n"
      "buffer_load_dwordx4 a[128:131], v1, s[36:39], s93 offen sc1\n"
      "v_mfma_f32_16x16x32_bf16 v[164:167], v[32:35], a[72:75], v[164:167]\n"
      "v_mfma_f32_16x16x32_bf16 v[164:167], v[36:39], a[76:79], v[164:167]\n"
      "v_mfma_f32_16x16x32_bf16 v[168:171], v[32:35], a[80:83], v[168:171]\n"
      "ds_read_b64_tr_b16 v[60:61], v13 offset:12288\n"
      "ds_read_b64_tr_b16 v[62:63], v13 offset:12800\n"
      "v_mfma_f32_16x16x32_bf16 v[168:171], v[36:39], a[84:87], v[168:171]\n"
      "buffer_load_dwordx4 a[132:135], v1, s[36:39], s93 offen offset:1024 sc1\n"
      "v_mfma_f32_16x16x32_bf16 v[172:175], v[32:35], a[88:91], v[172:175]\n"
      "v_mfma_f32_16x16x32_bf16 v[172:175], v[36:39], a[92:95], v[172:175]\n"
      "v_xor_b32 v11, 0xc000, v9\n"
      "s_waitcnt lgkmcnt(8)\n"
      "v_mfma_f32_16x16x32_bf16 v[176:179], v[40:43], a[64:67], v[176:179]\n"
      "ds_read_b64_tr_b16 v[32:33], v11\n"
      "ds_read_b64_tr_b16 v[34:35], v11 offset:512\n"
      "v_mfma_f32_16x16x32_bf16 v[176:179], v[44:47], a[68:71], v[176:179]\n"
      "v_mfma_f32_16x16x32_bf16 v[180:183], v[40:43], a[72:75], v[180:183]\n"
      "buffer_load_dwordx4 a[136:139], v1, s[36:39], s93 offen offset:2048 sc1\n"
      "v_mfma_f32_16x16x32_bf16 v[180:183], v[44:47], a[76:79], v[180:183]\n"
      "v_mfma_f32_16x16x32_bf16 v[184:187], v[40:43], a[80:83], v[184:187]\n"
      "ds_read_b64_tr_b16 v[36:37], v11 offset:8192\n"
      "ds_read_b64_tr_b16 v[38:39], v11 offset:8704\n"
      "v_mfma_f32_16x16x32_bf16 v[184:187], v[44:47], a[84:87], v[184:187]\n"
      "v_mfma_f32_16x16x32_bf16 v[188:191], v[40:43], a[88:91], v[188:191]\n"
      "v_mfma_f32_16x16x32_bf16 v[188:191], v[44:47], a[92:95], v[188:191]\n"
      "s_waitcnt lgkmcnt(8)\n"
      "buffer_load_dwordx4 a[140:143], v1, s[36:39], s93 offen offset:3072 sc1\n"
      "v_mfma_f32_16x16x32_bf16 v[192:195], v[48:51], a[64:67], v[192:195]\n"
      "ds_read_b64_tr_b16 v[40:41], v11 offset:4096\n"
      "ds_read_b64_tr_b16 v[42:43], v11 offset:4608\n"
      "v_mfma_f32_16x16x32_bf16 v[192:195], v[52:55], a[68:71], v[192:195]\n"
      "v_mfma_f32_16x16x32_bf16 v[196:199], v[48:51], a[72:75], v[196:199]\n"
      "s_add_u32 s93, s56, 36864\n"
      "v_mfma_f32_16x16x32_bf16 v[196:199], v[52:55], a[76:79], v[196:199]\n"
      "v_mfma_f32_16x16x32_bf16 v[200:203], v[48:51], a[80:83], v[200:203]\n"
      "ds_read_b64_tr_b16 v[44:45], v11 offset:12288\n"
      "ds_read_b64_tr_b16 v[46:47], v11 offset:12800\n"
      "v_mfma_f32_16x16x32_bf16 v[200:203], v[52:55], a[84:87], v[200:203]\n"
      "buffer_load_dwordx4 a[144:147], v1, s[36:39], s93 offen sc1\n"
      "v_mfma_f32_16x16x32_bf16 v[204:207], v[48:51], a[88:91], v[204:207]\n"
      "v_mfma_f32_16x16x32_bf16 v[204:207], v[52:55], a[92:95], v[204:207]\n"
      "v_xor_b32 v13, 0xc020, v9\n"
      "s_waitcnt lgkmcnt(8)\n"
      "v_mfma_f32_16x16x32_bf16 v[208:211], v[56:59], a[64:67], v[208:211]\n"
      "ds_read_b64_tr_b16 v[48:49], v13\n"
      "ds_read_b64_tr_b16 v[50:51], v13 offset:512\n"
      "v_mfma_f32_16x16x32_bf16 v[208:211], v[60:63], a[68:71], v[208:211]\n"
      "buffer_load_dwordx4 a[148:151], v1, s[36:39], s93 offen offset:1024 sc1\n"
      "v_mfma_f32_16x16x32_bf16 v[212:215], v[56:59], a[72:75], v[212:215]\n"
      "v_mfma_f32_16x16x32_bf16 v[212:215], v[60:63], a[76:79], v[212:215]\n"
      "v_mfma_f32_16x16x32_bf16 v[216:219], v[56:59], a[80:83], v[216:219]\n"
      "ds_read_b64_tr_b16 v[52:53], v13 offset:8192\n"
      "ds_read_b64_tr_b16 v[54:55], v13 offset:8704\n"
      "v_mfma_f32_16x16x32_bf16 v[216:219], v[60:63], a[84:87], v[216:219]\n"
      "v_mfma_f32_16x16x32_bf16 v[220:223], v[56:59], a[88:91], v[220:223]\n"
      "buffer_load_dwordx4 a[152:155], v1, s[36:39], s93 offen offset:2048 sc1\n"
      "v_mfma_f32_16x16x32_bf16 v[220:223], v[60:63], a[92:95], v[220:223]\n"
      "s_waitcnt lgkmcnt(8)\n"
      "s_waitcnt vmcnt(11)\n"
      "v_mfma_f32_16x16x32_bf16 v[96:99], v[32:35], a[96:99], v[96:99]\n"
      "ds_read_b64_tr_b16 v[56:57], v13 offset:4096\n"
      "ds_read_b64_tr_b16 v[58:59], v13 offset:4608\n"
      "v_mfma_f32_16x16x32_bf16 v[96:99], v[36:39], a[100:103], v[96:99]\n"
      "v_mfma_f32_16x16x32_bf16 v[100:103], v[32:35], a[104:107], v[100:103]\n"
      "v_mfma_f32_16x16x32_bf16 v[100:103], v[36:39], a[108:111], v[100:103]\n"
      "buffer_load_dwordx4 a[156:159], v1, s[36:39], s93 offen offset:3072 sc1\n"
      "s_waitcnt vmcnt(8)\n"
      "v_mfma_f32_16x16x32_bf16 v[104:107], v[32:35], a[112:115], v[104:107]\n"
      "ds_read_b64_tr_b16 v[60:61], v13 offset:12288\n"
      "ds_read_b64_tr_b16 v[62:63], v13 offset:12800\n"
      "v_mfma_f32_16x16x32_bf16 v[104:107], v[36:39], a[116:119], v[104:107]\n"
      "v_mfma_f32_16x16x32_bf16 v[108:111], v[32:35], a[120:123], v[108:111]\n"
      "s_add_u32 s93, s56, 40960\n"
      "v_mfma_f32_16x16x32_bf16 v[108:111], v[36:39], a[124:127], v[108:111]\n"
      "v_xor_b32 v11, 0xc040, v9\n"
      "s_waitcnt lgkmcnt(8)\n"
      "v_mfma_f32_16x16x32_bf16 v[112:115], v[40:43], a[96:99], v[112:115]\n"
      "s_barrier\n"
      "s_add_u32 s95, s7, 0x50000\n"
      "buffer_store_dwordx2 v[74:75], v76, s[44:47], s95 offen sc1\n"
      "ds_read_b64_tr_b16 v[32:33], v11\n"
      "ds_read_b64_tr_b16 v[34:35], v11 offset:512\n"
      "v_mfma_f32_16x16x32_bf16 v[112:115], v[44:47], a[100:103], v[112:115]\n"
      "buffer_load_dwordx4 a[160:163], v1, s[36:39], s93 offen sc1\n"
      "v_mfma_f32_16x16x32_bf16 v[116:119], v[40:43], a[104:107], v[116:119]\n"
      "v_mfma_f32_16x16x32_bf16 v[116:119], v[44:47], a[108:111], v[116:119]\n"
      "v_mfma_f32_16x16x32_bf16 v[120:123], v[40:43], a[112:115], v[120:123]\n"
      "ds_read_b64_tr_b16 v[36:37], v11 offset:8192\n"
      "ds_read_b64_tr_b16 v[38:39], v11 offset:8704\n"
      "v_mfma_f32_16x16x32_bf16 v[120:123], v[44:47], a[116:119], v[120:123]\n"
      "buffer_load_dwordx4 a[164:167], v1, s[36:39], s93 offen offset:1024 sc1\n"
      "v_mfma_f32_16x16x32_bf16 v[124:127], v[40:43], a[120:123], v[124:127]\n"
      "v_mfma_f32_16x16x32_bf16 v[124:127], v[44:47], a[124:127], v[124:127]\n"
      "s_waitcnt lgkmcnt(8)\n"
      "v_mfma_f32_16x16x32_bf16 v[128:131], v[48:51], a[96:99], v[128:131]\n"
      "ds_read_b64_tr_b16 v[40:41], v11 offset:4096\n"
      "ds_read_b64_tr_b16 v[42:43], v11 offset:4608\n"
      "v_mfma_f32_16x16x32_bf16 v[128:131], v[52:55], a[100:103], v[128:131]\n"
      "v_mfma_f32_16x16x32_bf16 v[132:135], v[48:51], a[104:107], v[132:135]\n"
      "buffer_load_dwordx4 a[168:171], v1, s[36:39], s93 offen offset:2048 sc1\n"
      "v_mfma_f32_16x16x32_bf16 v[132:135], v[52:55], a[108:111], v[132:135]\n"
      "v_mfma_f32_16x16x32_bf16 v[136:139], v[48:51], a[112:115], v[136:139]\n"
      "ds_read_b64_tr_b16 v[44:45], v11 offset:12288\n"
      "ds_read_b64_tr_b16 v[46:47], v11 offset:12800\n"
      "v_mfma_f32_16x16x32_bf16 v[136:139], v[52:55], a[116:119], v[136:139]\n"
      "v_mfma_f32_16x16x32_bf16 v[140:143], v[48:51], a[120:123], v[140:143]\n"
      "v_mfma_f32_16x16x32_bf16 v[140:143], v[52:55], a[124:127], v[140:143]\n"
      "v_xor_b32 v13, 0xc060, v9\n"
      "s_waitcnt lgkmcnt(8)\n"
      "v_mfma_f32_16x16x32_bf16 v[144:147], v[56:59], a[96:99], v[144:147]\n"
      "ds_read_b64_tr_b16 v[48:49], v13\n"
      "ds_read_b64_tr_b16 v[50:51], v13 offset:512\n"
      "v_mfma_f32_16x16x32_bf16 v[144:147], v[60:63], a[100:103], v[144:147]\n"
      "buffer_load_dwordx4 a[172:175], v1, s[36:39], s93 offen offset:3072 sc1\n"
      "v_mfma_f32_16x16x32_bf16 v[148:151], v[56:59], a[104:107], v[148:151]\n"
      "s_add_u32 s93, s56, 45056\n"
      "v_mfma_f32_16x16x32_bf16 v[148:151], v[60:63], a[108:111], v[148:151]\n"
      "v_mfma_f32_16x16x32_bf16 v[152:155], v[56:59], a[112:115], v[152:155]\n"
      "ds_read_b64_tr_b16 v[52:53], v13 offset:8192\n"
      "ds_read_b64_tr_b16 v[54:55], v13 offset:8704\n"
      "v_mfma_f32_16x16x32_bf16 v[152:155], v[60:63], a[116:119], v[152:155]\n"
      "buffer_load_dwordx4 a[176:179], v1, s[36:39], s93 offen sc1\n"
      "v_mfma_f32_16x16x32_bf16 v[156:159], v[56:59], a[120:123], v[156:159]\n"
      "v_mfma_f32_16x16x32_bf16 v[156:159], v[60:63], a[124:127], v[156:159]\n"
      "s_waitcnt lgkmcnt(8)\n"
      "v_mfma_f32_16x16x32_bf16 v[160:163], v[32:35], a[96:99], v[160:163]\n"
      "ds_read_b64_tr_b16 v[56:57], v13 offset:4096\n"
      "ds_read_b64_tr_b16 v[58:59], v13 offset:4608\n"
      "v_mfma_f32_16x16x32_bf16 v[160:163], v[36:39], a[100:103], v[160:163]\n"
      "buffer_load_dwordx4 a[180:183], v1, s[36:39], s93 offen offset:1024 sc1\n"
      "v_mfma_f32_16x16x32_bf16 v[164:167], v[32:35], a[104:107], v[164:167]\n"
      "v_mfma_f32_16x16x32_bf16 v[164:167], v[36:39], a[108:111], v[164:167]\n"
      "v_mfma_f32_16x16x32_bf16 v[168:171], v[32:35], a[112:115], v[168:171]\n"
      "ds_read_b64_tr_b16 v[60:61], v13 offset:12288\n"
      "ds_read_b64_tr_b16 v[62:63], v13 offset:12800\n"
      "v_mfma_f32_16x16x32_bf16 v[168:171], v[36:39], a[116:119], v[168:171]\n"
      "v_mfma_f32_16x16x32_bf16 v[172:175], v[32:35], a[120:123], v[172:175]\n"
      "buffer_load_dwordx4 a[184:187], v1, s[36:39], s93 offen offset:2048 sc1\n"
      "v_mfma_f32_16x16x32_bf16 v[172:175], v[36:39], a[124:127], v[172:175]\n"
      "v_xor_b32 v11, 0x10000, v9\n"
      "s_waitcnt lgkmcnt(8)\n"
      "v_mfma_f32_16x16x32_bf16 v[176:179], v[40:43], a[96:99], v[176:179]\n"
      "ds_read_b64_tr_b16 v[32:33], v11\n"
      "ds_read_b64_tr_b16 v[34:35], v11 offset:512\n"
      "v_mfma_f32_16x16x32_bf16 v[176:179], v[44:47], a[100:103], v[176:179]\n"
      "v_mfma_f32_16x16x32_bf16 v[180:183], v[40:43], a[104:107], v[180:183]\n"
      "v_mfma_f32_16x16x32_bf16 v[180:183], v[44:47], a[108:111], v[180:183]\n"
      "buffer_load_dwordx4 a[188:191], v1, s[36:39], s93 offen offset:3072 sc1\n"
      "v_mfma_f32_16x16x32_bf16 v[184:187], v[40:43], a[112:115], v[184:187]\n"
      "ds_read_b64_tr_b16 v[36:37], v11 offset:8192\n"
      "ds_read_b64_tr_b16 v[38:39], v11 offset:8704\n"
      "v_mfma_f32_16x16x32_bf16 v[184:187], v[44:47], a[116:119], v[184:187]\n"
      "v_mfma_f32_16x16x32_bf16 v[188:191], v[40:43], a[120:123], v[188:191]\n"
      "s_add_u32 s93, s56, 49152\n"
      "v_mfma_f32_16x16x32_bf16 v[188:191], v[44:47], a[124:127], v[188:191]\n"
      "s_waitcnt lgkmcnt(8)\n"
      "v_mfma_f32_16x16x32_bf16 v[192:195], v[48:51], a[96:99], v[192:195]\n"
      "ds_read_b64_tr_b16 v[40:41], v11 offset:4096\n"
      "ds_read_b64_tr_b16 v[42:43], v11 offset:4608\n"
      "v_mfma_f32_16x16x32_bf16 v[192:195], v[52:55], a[100:103], v[192:195]\n"
      "buffer_load_dwordx4 a[192:195], v1, s[36:39], s93 offen sc1\n"
      "v_mfma_f32_16x16x32_bf16 v[196:199], v[48:51], a[104:107], v[196:199]\n"
      "v_mfma_f32_16x16x32_bf16 v[196:199], v[52:55], a[108:111], v[196:199]\n"
      "v_mfma_f32_16x16x32_bf16 v[200:203], v[48:51], a[112:115], v[200:203]\n"
      "ds_read_b64_tr_b16 v[44:45], v11 offset:12288\n"
      "ds_read_b64_tr_b16 v[46:47], v11 offset:12800\n"
      "v_mfma_f32_16x16x32_bf16 v[200:203], v[52:55], a[116:119], v[200:203]\n"
      "buffer_load_dwordx4 a[196:199], v1, s[36:39], s93 offen offset:1024 sc1\n"
      "v_mfma_f32_16x16x32_bf16 v[204:207], v[48:51], a[120:123], v[204:207]\n"
      "v_mfma_f32_16x16x32_bf16 v[204:207], v[52:55], a[124:127], v[204:207]\n"
      "v_xor_b32 v13, 0x10020, v9\n"
      "s_waitcnt lgkmcnt(8)\n"
      "v_mfma_f32_16x16x32_bf16 v[208:211], v[56:59], a[96:99], v[208:211]\n"
      "ds_read_b64_tr_b16 v[48:49], v13\n"
      "ds_read_b64_tr_b16 v[50:51], v13 offset:512\n"
      "v_mfma_f32_16x16x32_bf16 v[208:211], v[60:63], a[100:103], v[208:211]\n"
      "v_mfma_f32_16x16x32_bf16 v[212:215], v[56:59], a[104:107], v[212:215]\n"
      "buffer_load_dwordx4 a[200:203], v1, s[36:39], s93 offen offset:2048 sc1\n"
      "v_mfma_f32_16x16x32_bf16 v[212:215], v[60:63], a[108:111], v[212:215]\n"
      "v_mfma_f32_16x16x32_bf16 v[216:219], v[56:59], a[112:115], v[216:219]\n"
      "ds_read_b64_tr_b16 v[52:53], v13 offset:8192\n"
      "ds_read_b64_tr_b16 v[54:55], v13 offset:8704\n"
      "v_mfma_f32_16x16x32_bf16 v[216:219], v[60:63], a[116:119], v[216:219]\n"
      "v_mfma_f32_16x16x32_bf16 v[220:223], v[56:59], a[120:123], v[220:223]\n"
      "v_mfma_f32_16x16x32_bf16 v[220:223], v[60:63], a[124:127], v[220:223]\n"
      "s_waitcnt lgkmcnt(8)\n"
      "buffer_load_dwordx4 a[204:207], v1, s[36:39], s93 offen offset:3072 sc1\n"
      "s_waitcnt vmcnt(17)\n"
      "v_mfma_f32_16x16x32_bf16 v[96:99], v[32:35], a[128:131], v[96:99]\n"
      "ds_read_b64_tr_b16 v[56:57], v13 offset:4096\n"
      "ds_read_b64_tr_b16 v[58:59], v13 offset:4608\n"
      "v_mfma_f32_16x16x32_bf16 v[96:99], v[36:39], a[132:135], v[96:99]\n"
      "v_mfma_f32_16x16x32_bf16 v[100:103], v[32:35], a[136:139], v[100:103]\n"
      "s_add_u32 s93, s56, 53248\n"
      "v_mfma_f32_16x16x32_bf16 v[100:103], v[36:39], a[140:143], v[100:103]\n"
      "s_waitcnt vmcnt(13)\n"
      "v_mfma_f32_16x16x32_bf16 v[104:107], v[32:35], a[144:147], v[104:107]\n"
      "ds_read_b64_tr_b16 v[60:61], v13 offset:12288\n"
      "ds_read_b64_tr_b16 v[62:63], v13 offset:12800\n"
      "v_mfma_f32_16x16x32_bf16 v[104:107], v[36:39], a[148:151], v[104:107]\n"
      "buffer_load_dwordx4 a[208:211], v1, s[36:39], s93 offen sc1\n"
      "v_mfma_f32_16x16x32_bf16 v[108:111], v[32:35], a[152:155], v[108:111]\n"
      "v_mfma_f32_16x16x32_bf16 v[108:111], v[36:39], a[156:159], v[108:111]\n"
      "v_xor_b32 v11, 0x10040, v9\n"
      "s_waitcnt lgkmcnt(8)\n"
      "v_mfma_f32_16x16x32_bf16 v[112:115], v[40:43], a[128:131], v[112:115]\n"
      "ds_read_b64_tr_b16 v[32:33], v11\n"
      "ds_read_b64_tr_b16 v[34:35], v11 offset:512\n"
      "v_mfma_f32_16x16x32_bf16 v[112:115], v[44:47], a[132:135], v[112:115]\n"
      "buffer_load_dwordx4 a[212:215], v1, s[36:39], s93 offen offset:1024 sc1\n"
      "v_mfma_f32_16x16x32_bf16 v[116:119], v[40:43], a[136:139], v[116:119]\n"
      "v_mfma_f32_16x16x32_bf16 v[116:119], v[44:47], a[140:143], v[116:119]\n"
      "v_mfma_f32_16x16x32_bf16 v[120:123], v[40:43], a[144:147], v[120:123]\n"
      "ds_read_b64_tr_b16 v[36:37], v11 offset:8192\n"
      "ds_read_b64_tr_b16 v[38:39], v11 offset:8704\n"
      "v_mfma_f32_16x16x32_bf16 v[120:123], v[44:47], a[148:151], v[120:123]\n"
      "v_mfma_f32_16x16x32_bf16 v[124:127], v[40:43], a[152:155], v[124:127]\n"
      "buffer_load_dwordx4 a[216:219], v1, s[36:39], s93 offen offset:2048 sc1\n"
      "v_mfma_f32_16x16x32_bf16 v[124:127], v[44:47], a[156:159], v[124:127]\n"
      "s_waitcnt lgkmcnt(8)\n"
      "v_mfma_f32_16x16x32_bf16 v[128:131], v[48:51], a[128:131], v[128:131]\n"
      "ds_read_b64_tr_b16 v[40:41], v11 offset:4096\n"
      "ds_read_b64_tr_b16 v[42:43], v11 offset:4608\n"
      "v_mfma_f32_16x16x32_bf16 v[128:131], v[52:55], a[132:135], v[128:131]\n"
      "v_mfma_f32_16x16x32_bf16 v[132:135], v[48:51], a[136:139], v[132:135]\n"
      "v_mfma_f32_16x16x32_bf16 v[132:135], v[52:55], a[140:143], v[132:135]\n"
      "buffer_load_dwordx4 a[220:223], v1, s[36:39], s93 offen offset:3072 sc1\n"
      "v_mfma_f32_16x16x32_bf16 v[136:139], v[48:51], a[144:147], v[136:139]\n"
      "ds_read_b64_tr_b16 v[44:45], v11 offset:12288\n"
      "ds_read_b64_tr_b16 v[46:47], v11 offset:12800\n"
      "v_mfma_f32_16x16x32_bf16 v[136:139], v[52:55], a[148:151], v[136:139]\n"
      "v_mfma_f32_16x16x32_bf16 v[140:143], v[48:51], a[152:155], v[140:143]\n"
      "s_add_u32 s93, s56, 57344\n"
      "v_mfma_f32_16x16x32_bf16 v[140:143], v[52:55], a[156:159], v[140:143]\n"
      "v_xor_b32 v13, 0x10060, v9\n"
      "s_waitcnt lgkmcnt(8)\n"
      "v_mfma_f32_16x16x32_bf16 v[144:147], v[56:59], a[128:131], v[144:147]\n"
      "ds_read_b64_tr_b16 v[48:49], v13\n"
      "ds_read_b64_tr_b16 v[50:51], v13 offset:512\n"
      "v_mfma_f32_16x16x32_bf16 v[144:147], v[60:63], a[132:135], v[144:147]\n"
      "buffer_load_dwordx4 a[224:227], v1, s[36:39], s93 offen sc1\n"
      "v_mfma_f32_16x16x32_bf16 v[148:151], v[56:59], a[136:139], v[148:151]\n"
      "v_mfma_f32_16x16x32_bf16 v[148:151], v[60:63], a[140:143], v[148:151]\n"
      "v_mfma_f32_16x16x32_bf16 v[152:155], v[56:59], a[144:147], v[152:155]\n"
      "ds_read_b64_tr_b16 v[52:53], v13 offset:8192\n"
      "ds_read_b64_tr_b16 v[54:55], v13 offset:8704\n"
      "v_mfma_f32_16x16x32_bf16 v[152:155], v[60:63], a[148:151], v[152:155]\n"
      "buffer_load_dwordx4 a[228:231], v1, s[36:39], s93 offen offset:1024 sc1\n"
      "v_mfma_f32_16x16x32_bf16 v[156:159], v[56:59], a[152:155], v[156:159]\n"
      "v_mfma_f32_16x16x32_bf16 v[156:159], v[60:63], a[156:159], v[156:159]\n"
      "s_waitcnt lgkmcnt(8)\n"
      "v_mfma_f32_16x16x32_bf16 v[160:163], v[32:35], a[128:131], v[160:163]\n"
      "ds_read_b64_tr_b16 v[56:57], v13 offset:4096\n"
      "ds_read_b64_tr_b16 v[58:59], v13 offset:4608\n"
      "v_mfma_f32_16x16x32_bf16 v[160:163], v[36:39], a[132:135], v[160:163]\n"
      "v_mfma_f32_16x16x32_bf16 v[164:167], v[32:35], a[136:139], v[164:167]\n"
      "buffer_load_dwordx4 a[232:235], v1, s[36:39], s93 offen offset:2048 sc1\n"
      "v_mfma_f32_16x16x32_bf16 v[164:167], v[36:39], a[140:143], v[164:167]\n"
      "v_mfma_f32_16x16x32_bf16 v[168:171], v[32:35], a[144:147], v[168:171]\n"
      "ds_read_b64_tr_b16 v[60:61], v13 offset:12288\n"
      "ds_read_b64_tr_b16 v[62:63], v13 offset:12800\n"
      "v_mfma_f32_16x16x32_bf16 v[168:171], v[36:39], a[148:151], v[168:171]\n"
      "v_mfma_f32_16x16x32_bf16 v[172:175], v[32:35], a[152:155], v[172:175]\n"
      "v_mfma_f32_16x16x32_bf16 v[172:175], v[36:39], a[156:159], v[172:175]\n"
      "v_xor_b32 v11, 0x14000, v9\n"
      "s_waitcnt lgkmcnt(8)\n"
      "v_mfma_f32_16x16x32_bf16 v[176:179], v[40:43], a[128:131], v[176:179]\n"
      "ds_read_b64_tr_b16 v[32:33], v11\n"
      "ds_read_b64_tr_b16 v[34:35], v11 offset:512\n"
      "v_mfma_f32_16x16x32_bf16 v[176:179], v[44:47], a[132:135], v[176:179]\n"
      "buffer_load_dwordx4 a[236:239], v1, s[36:39], s93 offen offset:3072 sc1\n"
      "v_mfma_f32_16x16x32_bf16 v[180:183], v[40:43], a[136:139], v[180:183]\n"
      "s_add_u32 s93, s56, 61440\n"
      "v_mfma_f32_16x16x32_bf16 v[180:183], v[44:47], a[140:143], v[180:183]\n"
      "v_mfma_f32_16x16x32_bf16 v[184:187], v[40:43], a[144:147], v[184:187]\n"
      "ds_read_b64_tr_b16 v[36:37], v11 offset:8192\n"
      "ds_read_b64_tr_b16 v[38:39], v11 offset:8704\n"
      "v_mfma_f32_16x16x32_bf16 v[184:187], v[44:47], a[148:151], v[184:187]\n"
      "buffer_load_dwordx4 a[240:243], v1, s[36:39], s93 offen sc1\n"
      "v_mfma_f32_16x16x32_bf16 v[188:191], v[40:43], a[152:155], v[188:191]\n"
      "v_mfma_f32_16x16x32_bf16 v[188:191], v[44:47], a[156:159], v[188:191]\n"
      "s_waitcnt lgkmcnt(8)\n"
      "v_mfma_f32_16x16x32_bf16 v[192:195], v[48:51], a[128:131], v[192:195]\n"
      "ds_read_b64_tr_b16 v[40:41], v11 offset:4096\n"
      "ds_read_b64_tr_b16 v[42:43], v11 offset:4608\n"
      "v_mfma_f32_16x16x32_bf16 v[192:195], v[52:55], a[132:135], v[192:195]\n"
      "buffer_load_dwordx4 a[244:247], v1, s[36:39], s93 offen offset:1024 sc1\n"
      "v_mfma_f32_16x16x32_bf16 v[196:199], v[48:51], a[136:139], v[196:199]\n"
      "v_mfma_f32_16x16x32_bf16 v[196:199], v[52:55], a[140:143], v[196:199]\n"
      "v_mfma_f32_16x16x32_bf16 v[200:203], v[48:51], a[144:147], v[200:203]\n"
      "ds_read_b64_tr_b16 v[44:45], v11 offset:12288\n"
      "ds_read_b64_tr_b16 v[46:47], v11 offset:12800\n"
      "v_mfma_f32_16x16x32_bf16 v[200:203], v[52:55], a[148:151], v[200:203]\n"
      "v_mfma_f32_16x16x32_bf16 v[204:207], v[48:51], a[152:155], v[204:207]\n"
      "buffer_load_dwordx4 a[248:251], v1, s[36:39], s93 offen offset:2048 sc1\n"
      "v_mfma_f32_16x16x32_bf16 v[204:207], v[52:55], a[156:159], v[204:207]\n"
      "v_xor_b32 v13, 0x14020, v9\n"
      "s_waitcnt lgkmcnt(8)\n"
      "v_mfma_f32_16x16x32_bf16 v[208:211], v[56:59], a[128:131], v[208:211]\n"
      "ds_read_b64_tr_b16 v[48:49], v13\n"
      "ds_read_b64_tr_b16 v[50:51], v13 offset:512\n"
      "v_mfma_f32_16x16x32_bf16 v[208:211], v[60:63], a[132:135], v[208:211]\n"
      "v_mfma_f32_16x16x32_bf16 v[212:215], v[56:59], a[136:139], v[212:215]\n"
      "v_mfma_f32_16x16x32_bf16 v[212:215], v[60:63], a[140:143], v[212:215]\n"
      "buffer_load_dwordx4 a[252:255], v1, s[36:39], s93 offen offset:3072 sc1\n"
      "v_mfma_f32_16x16x32_bf16 v[216:219], v[56:59], a[144:147], v[216:219]\n"
      "ds_read_b64_tr_b16 v[52:53], v13 offset:8192\n"
      "ds_read_b64_tr_b16 v[54:55], v13 offset:8704\n"
      "v_mfma_f32_16x16x32_bf16 v[216:219], v[60:63], a[148:151], v[216:219]\n"
      "v_mfma_f32_16x16x32_bf16 v[220:223], v[56:59], a[152:155], v[220:223]\n"
      "s_add_u32 s93, s56, 65536\n"
      "v_mfma_f32_16x16x32_bf16 v[220:223], v[60:63], a[156:159], v[220:223]\n"
      "s_waitcnt lgkmcnt(8)\n"
      "s_waitcnt vmcnt(20)\n"
      "v_mfma_f32_16x16x32_bf16 v[96:99], v[32:35], a[160:163], v[96:99]\n"
      "ds_read_b64_tr_b16 v[56:57], v13 offset:4096\n"
      "ds_read_b64_tr_b16 v[58:59], v13 offset:4608\n"
      "Lpad7_%=:\n"
      "s_cbranch_execz Lpad8_%=\n"
      "v_mfma_f32_16x16x32_bf16 v[96:99], v[36:39], a[164:167], v[96:99]\n"
      "buffer_load_dwordx4 v[224:227], v1, s[36:39], s93 offen sc1\n"
      "v_mfma_f32_16x16x32_bf16 v[100:103], v[32:35], a[168:171], v[100:103]\n"
      "v_mfma_f32_16x16x32_bf16 v[100:103], v[36:39], a[172:175], v[100:103]\n"
      "s_waitcnt vmcnt(17)\n"
      "v_mfma_f32_16x16x32_bf16 v[104:107], v[32:35], a[176:179], v[104:107]\n"
      "ds_read_b64_tr_b16 v[60:61], v13 offset:12288\n"
      "ds_read_b64_tr_b16 v[62:63], v13 offset:12800\n"
      "v_mfma_f32_16x16x32_bf16 v[104:107], v[36:39], a[180:183], v[104:107]\n"
      "buffer_load_dwordx4 v[228:231], v1, s[36:39], s93 offen offset:1024 sc1\n"
      "v_mfma_f32_16x16x32_bf16 v[108:111], v[32:35], a[184:187], v[108:111]\n"
      "v_mfma_f32_16x16x32_bf16 v[108:111], v[36:39], a[188:191], v[108:111]\n"
      "v_xor_b32 v11, 0x14040, v9\n"
      "s_waitcnt lgkmcnt(8)\n"
      "v_mfma_f32_16x16x32_bf16 v[112:115], v[40:43], a[160:163], v[112:115]\n"
      "ds_read_b64_tr_b16 v[32:33], v11\n"
      "ds_read_b64_tr_b16 v[34:35], v11 offset:512\n"
      "v_mfma_f32_16x16x32_bf16 v[112:115], v[44:47], a[164:167], v[112:115]\n"
      "v_mfma_f32_16x16x32_bf16 v[116:119], v[40:43], a[168:171], v[116:119]\n"
      "buffer_load_dwordx4 v[232:235], v1, s[36:39], s93 offen offset:2048 sc1\n"
      "v_mfma_f32_16x16x32_bf16 v[116:119], v[44:47], a[172:175], v[116:119]\n"
      "v_mfma_f32_16x16x32_bf16 v[120:123], v[40:43], a[176:179], v[120:123]\n"
      "ds_read_b64_tr_b16 v[36:37], v11 offset:8192\n"
      "ds_read_b64_tr_b16 v[38:39], v11 offset:8704\n"
      "v_mfma_f32_16x16x32_bf16 v[120:123], v[44:47], a[180:183], v[120:123]\n"
      "v_mfma_f32_16x16x32_bf16 v[124:127], v[40:43], a[184:187], v[124:127]\n"
      "v_mfma_f32_16x16x32_bf16 v[124:127], v[44:47], a[188:191], v[124:127]\n"
      "s_waitcnt lgkmcnt(8)\n"
      "buffer_load_dwordx4 v[236:239], v1, s[36:39], s93 offen offset:3072 sc1\n"
      "v_mfma_f32_16x16x32_bf16 v[128:131], v[48:51], a[160:163], v[128:131]\n"
      "ds_read_b64_tr_b16 v[40:41], v11 offset:4096\n"
      "ds_read_b64_tr_b16 v[42:43], v11 offset:4608\n"
      "v_mfma_f32_16x16x32_bf16 v[128:131], v[52:55], a[164:167], v[128:131]\n"
      "v_mfma_f32_16x16x32_bf16 v[132:135], v[48:51], a[168:171], v[132:135]\n"
      "s_add_u32 s93, s56, 69632\n"
      "v_mfma_f32_16x16x32_bf16 v[132:135], v[52:55], a[172:175], v[132:135]\n"
      "v_mfma_f32_16x16x32_bf16 v[136:139], v[48:51], a[176:179], v[136:139]\n"
      "ds_read_b64_tr_b16 v[44:45], v11 offset:12288\n"
      "ds_read_b64_tr_b16 v[46:47], v11 offset:12800\n"
      "v_mfma_f32_16x16x32_bf16 v[136:139], v[52:55], a[180:183], v[136:139]\n"
      "buffer_load_dwordx4 v[240:243], v1, s[36:39], s93 offen sc1\n"
      "v_mfma_f32_16x16x32_bf16 v[140:143], v[48:51], a[184:187], v[140:143]\n"
      "v_mfma_f32_16x16x32_bf16 v[140:143], v[52:55], a[188:191], v[140:143]\n"
      "v_xor_b32 v13, 0x14060, v9\n"
      "s_waitcnt lgkmcnt(8)\n"
      "v_mfma_f32_16x16x32_bf16 v[144:147], v[56:59], a[160:163], v[144:147]\n"
      "ds_read_b64_tr_b16 v[48:49], v13\n"
      "ds_read_b64_tr_b16 v[50:51], v13 offset:512\n"
      "v_mfma_f32_16x16x32_bf16 v[144:147], v[60:63], a[164:167], v[144:147]\n"
      "buffer_load_dwordx4 v[244:247], v1, s[36:39], s93 offen offset:1024 sc1\n"
      "v_mfma_f32_16x16x32_bf16 v[148:151], v[56:59], a[168:171], v[148:151]\n"
      "v_mfma_f32_16x16x32_bf16 v[148:151], v[60:63], a[172:175], v[148:151]\n"
      "v_mfma_f32_16x16x32_bf16 v[152:155], v[56:59], a[176:179], v[152:155]\n"
      "ds_read_b64_tr_b16 v[52:53], v13 offset:8192\n"
      "ds_read_b64_tr_b16 v[54:55], v13 offset:8704\n"
      "v_mfma_f32_16x16x32_bf16 v[152:155], v[60:63], a[180:183], v[152:155]\n"
      "v_mfma_f32_16x16x32_bf16 v[156:159], v[56:59], a[184:187], v[156:159]\n"
      "buffer_load_dwordx4 v[248:251], v1, s[36:39], s93 offen offset:2048 sc1\n"
      "v_mfma_f32_16x16x32_bf16 v[156:159], v[60:63], a[188:191], v[156:159]\n"
      "s_waitcnt lgkmcnt(8)\n"
      "v_mfma_f32_16x16x32_bf16 v[160:163], v[32:35], a[160:163], v[160:163]\n"
      "ds_read_b64_tr_b16 v[56:57], v13 offset:4096\n"
      "ds_read_b64_tr_b16 v[58:59], v13 offset:4608\n"
      "v_mfma_f32_16x16x32_bf16 v[160:163], v[36:39], a[164:167], v[160:163]\n"
      "v_mfma_f32_16x16x32_bf16 v[164:167], v[32:35], a[168:171], v[164:167]\n"
      "v_mfma_f32_16x16x32_bf16 v[164:167], v[36:39], a[172:175], v[164:167]\n"
      "buffer_load_dwordx4 v[252:255], v1, s[36:39], s93 offen offset:3072 sc1\n"
      "v_mfma_f32_16x16x32_bf16 v[168:171], v[32:35], a[176:179], v[168:171]\n"
      "ds_read_b64_tr_b16 v[60:61], v13 offset:12288\n"
      "ds_read_b64_tr_b16 v[62:63], v13 offset:12800\n"
      "v_mfma_f32_16x16x32_bf16 v[168:171], v[36:39], a[180:183], v[168:171]\n"
      "v_mfma_f32_16x16x32_bf16 v[172:175], v[32:35], a[184:187], v[172:175]\n"
      "s_add_u32 m0, s81, 32768\n"
      "s_add_u32 s96, s10, 512\n"
      "v_mfma_f32_16x16x32_bf16 v[172:175], v[36:39], a[188:191], v[172:175]\n"
      "v_xor_b32 v11, 0x18000, v9\n"
      "s_waitcnt lgkmcnt(8)\n"
      "v_mfma_f32_16x16x32_bf16 v[176:179], v[40:43], a[160:163], v[176:179]\n"
      "ds_read_b64_tr_b16 v[32:33], v11\n"
      "ds_read_b64_tr_b16 v[34:35], v11 offset:512\n"
      "v_mfma_f32_16x16x32_bf16 v[176:179], v[44:47], a[164:167], v[176:179]\n"
      "buffer_load_dwordx4 v4, s[52:55], s96 offen lds\n"
      "v_mfma_f32_16x16x32_bf16 v[180:183], v[40:43], a[168:171], v[180:183]\n"
      "v_mfma_f32_16x16x32_bf16 v[180:183], v[44:47], a[172:175], v[180:183]\n"
      "v_mfma_f32_16x16x32_bf16 v[184:187], v[40:43], a[176:179], v[184:187]\n"
      "ds_read_b64_tr_b16 v[36:37], v11 offset:8192\n"
      "ds_read_b64_tr_b16 v[38:39], v11 offset:8704\n"
      "v_mfma_f32_16x16x32_bf16 v[184:187], v[44:47], a[180:183], v[184:187]\n"
      "buffer_load_dwordx4 v5, s[52:55], s96 offen offset:1024 lds\n"
      "v_mfma_f32_16x16x32_bf16 v[188:191], v[40:43], a[184:187], v[188:191]\n"
      "v_mfma_f32_16x16x32_bf16 v[188:191], v[44:47], a[188:191], v[188:191]\n"
      "s_waitcnt lgkmcnt(8)\n"
      "v_mfma_f32_16x16x32_bf16 v[192:195], v[48:51], a[160:163], v[192:195]\n"
      "ds_read_b64_tr_b16 v[40:41], v11 offset:4096\n"
      "ds_read_b64_tr_b16 v[42:43], v11 offset:4608\n"
      "v_mfma_f32_16x16x32_bf16 v[192:195], v[52:55], a[164:167], v[192:195]\n"
      "buffer_load_dwordx2 v[70:71], v22, s[36:39], 0 offen sc1\n"
      "v_mfma_f32_16x16x32_bf16 v[196:199], v[48:51], a[168:171], v[196:199]\n"
      "buffer_load_dwordx4 v6, s[52:55], s96 offen offset:2048 lds\n"
      "v_mfma_f32_16x16x32_bf16 v[196:199], v[52:55], a[172:175], v[196:199]\n"
      "v_mfma_f32_16x16x32_bf16 v[200:203], v[48:51], a[176:179], v[200:203]\n"
      "ds_read_b64_tr_b16 v[44:45], v11 offset:12288\n"
      "ds_read_b64_tr_b16 v[46:47], v11 offset:12800\n"
      "v_mfma_f32_16x16x32_bf16 v[200:203], v[52:55], a[180:183], v[200:203]\n"
      "v_mfma_f32_16x16x32_bf16 v[204:207], v[48:51], a[184:187], v[204:207]\n"
      "v_mfma_f32_16x16x32_bf16 v[204:207], v[52:55], a[188:191], v[204:207]\n"
      "v_xor_b32 v13, 0x1c000, v9\n"
      "s_waitcnt lgkmcnt(8)\n"
      "v_mfma_f32_16x16x32_bf16 v[208:211], v[56:59], a[160:163], v[208:211]\n"
      "ds_read_b64_tr_b16 v[48:49], v13\n"
      "ds_read_b64_tr_b16 v[50:51], v13 offset:512\n"
      "v_mfma_f32_16x16x32_bf16 v[208:211], v[60:63], a[164:167], v[208:211]\n"
      "buffer_load_dwordx4 v7, s[52:55], s96 offen offset:3072 lds\n"
      "v_mfma_f32_16x16x32_bf16 v[212:215], v[56:59], a[168:171], v[212:215]\n"
      "v_mfma_f32_16x16x32_bf16 v[212:215], v[60:63], a[172:175], v[212:215]\n"
      "v_mfma_f32_16x16x32_bf16 v[216:219], v[56:59], a[176:179], v[216:219]\n"
      "ds_read_b64_tr_b16 v[52:53], v13 offset:8192\n"
      "ds_read_b64_tr_b16 v[54:55], v13 offset:8704\n"
      "v_mfma_f32_16x16x32_bf16 v[216:219], v[60:63], a[180:183], v[216:219]\n"
      "v_mfma_f32_16x16x32_bf16 v[220:223], v[56:59], a[184:187], v[220:223]\n"
      "v_mfma_f32_16x16x32_bf16 v[220:223], v[60:63], a[188:191], v[220:223]\n"
      "s_waitcnt lgkmcnt(8)\n"
      "s_waitcnt vmcnt(25)\n"
      "v_mfma_f32_16x16x32_bf16 v[96:99], v[32:35], a[192:195], v[96:99]\n"
      "ds_read_b64_tr_b16 v[56:57], v13 offset:4096\n"
      "ds_read_b64_tr_b16 v[58:59], v13 offset:4608\n"
      "v_mfma_f32_16x16x32_bf16 v[96:99], v[36:39], a[196:199], v[96:99]\n"
      "v_mfma_f32_16x16x32_bf16 v[100:103], v[32:35], a[200:203], v[100:103]\n"
      "v_mfma_f32_16x16x32_bf16 v[100:103], v[36:39], a[204:207], v[100:103]\n"
      "s_waitcnt vmcnt(21)\n"
      "v_mfma_f32_16x16x32_bf16 v[104:107], v[32:35], a[208:211], v[104:107]\n"
      "ds_read_b64_tr_b16 v[60:61], v13 offset:12288\n"
      "ds_read_b64_tr_b16 v[62:63], v13 offset:12800\n"
      "v_mfma_f32_16x16x32_bf16 v[104:107], v[36:39], a[212:215], v[104:107]\n"
      "v_mfma_f32_16x16x32_bf16 v[108:111], v[32:35], a[216:219], v[108:111]\n"
      "v_mfma_f32_16x16x32_bf16 v[108:111], v[36:39], a[220:223], v[108:111]\n"
      "v_xor_b32 v11, 0x20000, v9\n"
      "s_waitcnt lgkmcnt(8)\n"
      "v_mfma_f32_16x16x32_bf16 v[112:115], v[40:43], a[192:195], v[112:115]\n"
      "s_barrier\n"
      "ds_read_b64_tr_b16 v[32:33], v11\n"
      "ds_read_b64_tr_b16 v[34:35], v11 offset:512\n"
      "v_mfma_f32_16x16x32_bf16 v[112:115], v[44:47], a[196:199], v[112:115]\n"
      "s_add_u32 m0, s81, 49152\n"
      "s_add_u32 s96, s10, 590336\n"
      "v_mfma_f32_16x16x32_bf16 v[116:119], v[40:43], a[200:203], v[116:119]\n"
      "buffer_load_dwordx4 v4, s[52:55], s96 offen lds\n"
      "v_mfma_f32_16x16x32_bf16 v[116:119], v[44:47], a[204:207], v[116:119]\n"
      "v_mfma_f32_16x16x32_bf16 v[120:123], v[40:43], a[208:211], v[120:123]\n"
      "ds_read_b64_tr_b16 v[36:37], v11 offset:8192\n"
      "ds_read_b64_tr_b16 v[38:39], v11 offset:8704\n"
      "v_mfma_f32_16x16x32_bf16 v[120:123], v[44:47], a[212:215], v[120:123]\n"
      "buffer_load_dwordx4 v5, s[52:55], s96 offen offset:1024 lds\n"
      "v_mfma_f32_16x16x32_bf16 v[124:127], v[40:43], a[216:219], v[124:127]\n"
      "v_mfma_f32_16x16x32_bf16 v[124:127], v[44:47], a[220:223], v[124:127]\n"
      "s_waitcnt lgkmcnt(8)\n"
      "s_waitcnt vmcnt(19)\n"
      "v_mfma_f32_16x16x32_bf16 v[96:99], v[48:51], a[224:227], v[96:99]\n"
      "ds_read_b64_tr_b16 v[40:41], v11 offset:4096\n"
      "ds_read_b64_tr_b16 v[42:43], v11 offset:4608\n"
      "v_mfma_f32_16x16x32_bf16 v[96:99], v[52:55], a[228:231], v[96:99]\n"
      "v_mfma_f32_16x16x32_bf16 v[100:103], v[48:51], a[232:235], v[100:103]\n"
      "v_mfma_f32_16x16x32_bf16 v[100:103], v[52:55], a[236:239], v[100:103]\n"
      "buffer_load_dwordx4 v6, s[52:55], s96 offen offset:2048 lds\n"
      "s_waitcnt vmcnt(16)\n"
      "v_mfma_f32_16x16x32_bf16 v[104:107], v[48:51], a[240:243], v[104:107]\n"
      "ds_read_b64_tr_b16 v[44:45], v11 offset:12288\n"
      "ds_read_b64_tr_b16 v[46:47], v11 offset:12800\n"
      "v_mfma_f32_16x16x32_bf16 v[104:107], v[52:55], a[244:247], v[104:107]\n"
      "v_mfma_f32_16x16x32_bf16 v[108:111], v[48:51], a[248:251], v[108:111]\n"
      "v_mfma_f32_16x16x32_bf16 v[108:111], v[52:55], a[252:255], v[108:111]\n"
      "v_xor_b32 v13, 0x18020, v9\n"
      "s_waitcnt lgkmcnt(8)\n"
      "v_mfma_f32_16x16x32_bf16 v[112:115], v[56:59], a[224:227], v[112:115]\n"
      "ds_read_b64_tr_b16 v[48:49], v13\n"
      "ds_read_b64_tr_b16 v[50:51], v13 offset:512\n"
      "v_mfma_f32_16x16x32_bf16 v[112:115], v[60:63], a[228:231], v[112:115]\n"
      "buffer_load_dwordx4 v7, s[52:55], s96 offen offset:3072 lds\n"
      "v_mfma_f32_16x16x32_bf16 v[116:119], v[56:59], a[232:235], v[116:119]\n"
      "v_mfma_f32_16x16x32_bf16 v[116:119], v[60:63], a[236:239], v[116:119]\n"
      "v_mfma_f32_16x16x32_bf16 v[120:123], v[56:59], a[240:243], v[120:123]\n"
      "ds_read_b64_tr_b16 v[52:53], v13 offset:8192\n"
      "ds_read_b64_tr_b16 v[54:55], v13 offset:8704\n"
      "v_mfma_f32_16x16x32_bf16 v[120:123], v[60:63], a[244:247], v[120:123]\n"
      "s_add_u32 m0, s81, 65536\n"
      "s_add_u32 s96, s10, 1180160\n"
      "v_mfma_f32_16x16x32_bf16 v[124:127], v[56:59], a[248:251], v[124:127]\n"
      "v_mfma_f32_16x16x32_bf16 v[124:127], v[60:63], a[252:255], v[124:127]\n"
      "s_waitcnt lgkmcnt(8)\n"
      "buffer_load_dwordx4 v4, s[52:55], s96 offen lds\n"
      "s_waitcnt vmcnt(14)\n"
      "v_mfma_f32_16x16x32_bf16 v[96:99], v[32:35], v[224:227], v[96:99]\n"
      "ds_read_b64_tr_b16 v[56:57], v13 offset:4096\n"
      "ds_read_b64_tr_b16 v[58:59], v13 offset:4608\n"
      "v_mfma_f32_16x16x32_bf16 v[96:99], v[36:39], v[228:231], v[96:99]\n"
      "v_mfma_f32_16x16x32_bf16 v[100:103], v[32:35], v[232:235], v[100:103]\n"
      "v_mfma_f32_16x16x32_bf16 v[100:103], v[36:39], v[236:239], v[100:103]\n"
      "s_waitcnt vmcnt(10)\n"
      "v_mfma_f32_16x16x32_bf16 v[104:107], v[32:35], v[240:243], v[104:107]\n"
      "ds_read_b64_tr_b16 v[60:61], v13 offset:12288\n"
      "ds_read_b64_tr_b16 v[62:63], v13 offset:12800\n"
      "v_mfma_f32_16x16x32_bf16 v[104:107], v[36:39], v[244:247], v[104:107]\n"
      "buffer_load_dwordx4 v5, s[52:55], s96 offen offset:1024 lds\n"
      "v_mfma_f32_16x16x32_bf16 v[108:111], v[32:35], v[248:251], v[108:111]\n"
      "v_mfma_f32_16x16x32_bf16 v[108:111], v[36:39], v[252:255], v[108:111]\n"
      "v_xor_b32 v11, 0x1c020, v9\n"
      "s_waitcnt lgkmcnt(8)\n"
      "v_mfma_f32_16x16x32_bf16 v[112:115], v[40:43], v[224:227], v[112:115]\n"
      "ds_read_b64_tr_b16 v[32:33], v11\n"
      "ds_read_b64_tr_b16 v[34:35], v11 offset:512\n"
      "v_mfma_f32_16x16x32_bf16 v[112:115], v[44:47], v[228:231], v[112:115]\n"
      "v_mfma_f32_16x16x32_bf16 v[116:119], v[40:43], v[232:235], v[116:119]\n"
      "v_mfma_f32_16x16x32_bf16 v[116:119], v[44:47], v[236:239], v[116:119]\n"
      "buffer_load_dwordx4 v6, s[52:55], s96 offen offset:2048 lds\n"
      "v_mfma_f32_16x16x32_bf16 v[120:123], v[40:43], v[240:243], v[120:123]\n"
      "ds_read_b64_tr_b16 v[36:37], v11 offset:8192\n"
      "ds_read_b64_tr_b16 v[38:39], v11 offset:8704\n"
      "v_mfma_f32_16x16x32_bf16 v[120:123], v[44:47], v[244:247], v[120:123]\n"
      "v_mfma_f32_16x16x32_bf16 v[124:127], v[40:43], v[248:251], v[124:127]\n"
      "v_mfma_f32_16x16x32_bf16 v[124:127], v[44:47], v[252:255], v[124:127]\n"
      "s_waitcnt lgkmcnt(8)\n"
      "v_mfma_f32_16x16x32_bf16 v[128:131], v[48:51], a[192:195], v[128:131]\n"
      "ds_read_b64_tr_b16 v[40:41], v11 offset:4096\n"
      "ds_read_b64_tr_b16 v[42:43], v11 offset:4608\n"
      "v_mfma_f32_16x16x32_bf16 v[128:131], v[52:55], a[196:199], v[128:131]\n"
      "buffer_load_dwordx4 v7, s[52:55], s96 offen offset:3072 lds\n"
      "v_mfma_f32_16x16x32_bf16 v[132:135], v[48:51], a[200:203], v[132:135]\n"
      "v_mfma_f32_16x16x32_bf16 v[132:135], v[52:55], a[204:207], v[132:135]\n"
      "v_mfma_f32_16x16x32_bf16 v[136:139], v[48:51], a[208:211], v[136:139]\n"
      "ds_read_b64_tr_b16 v[44:45], v11 offset:12288\n"
      "ds_read_b64_tr_b16 v[46:47], v11 offset:12800\n"
      "v_mfma_f32_16x16x32_bf16 v[136:139], v[52:55], a[212:215], v[136:139]\n"
      "s_add_u32 m0, s81, 81920\n"
      "s_add_u32 s96, s10, 1769984\n"
      "v_mfma_f32_16x16x32_bf16 v[140:143], v[48:51], a[216:219], v[140:143]\n"
      "v_mfma_f32_16x16x32_bf16 v[140:143], v[52:55], a[220:223], v[140:143]\n"
      "v_xor_b32 v13, 0x20020, v9\n"
      "s_waitcnt lgkmcnt(8)\n"
      "v_mfma_f32_16x16x32_bf16 v[144:147], v[56:59], a[192:195], v[144:147]\n"
      "ds_read_b64_tr_b16 v[48:49], v13\n"
      "ds_read_b64_tr_b16 v[50:51], v13 offset:512\n"
      "v_mfma_f32_16x16x32_bf16 v[144:147], v[60:63], a[196:199], v[144:147]\n"
      "buffer_load_dwordx4 v4, s[52:55], s96 offen lds\n"
      "v_mfma_f32_16x16x32_bf16 v[148:151], v[56:59], a[200:203], v[148:151]\n"
      "v_exp_f32 v96, -v96\n"
      "v_mfma_f32_16x16x32_bf16 v[148:151], v[60:63], a[204:207], v[148:151]\n"
      "v_exp_f32 v97, -v97\n"
      "v_mfma_f32_16x16x32_bf16 v[152:155], v[56:59], a[208:211], v[152:155]\n"
      "ds_read_b64_tr_b16 v[52:53], v13 offset:8192\n"
      "ds_read_b64_tr_b16 v[54:55], v13 offset:8704\n"
      "v_mfma_f32_16x16x32_bf16 v[152:155], v[60:63], a[212:215], v[152:155]\n"
      "buffer_load_dwordx4 v5, s[52:55], s96 offen offset:1024 lds\n"
      "v_add_f32 v96, 1.0, v96\n"
      "v_mfma_f32_16x16x32_bf16 v[156:159], v[56:59], a[216:219], v[156:159]\n"
      "v_exp_f32 v98, -v98\n"
      "v_mfma_f32_16x16x32_bf16 v[156:159], v[60:63], a[220:223], v[156:159]\n"
      "s_waitcnt lgkmcnt(8)\n"
      "v_add_f32 v97, 1.0, v97\n"
      "v_mfma_f32_16x16x32_bf16 v[128:131], v[32:35], a[224:227], v[128:131]\n"
      "ds_read_b64_tr_b16 v[56:57], v13 offset:4096\n"
      "ds_read_b64_tr_b16 v[58:59], v13 offset:4608\n"
      "v_mfma_f32_16x16x32_bf16 v[128:131], v[36:39], a[228:231], v[128:131]\n"
      "s_waitcnt vmcnt(12)\n"
      "v_cmp_ne_u64 vcc, s[76:77], v[70:71]\n"
      "v_mfma_f32_16x16x32_bf16 v[132:135], v[32:35], a[232:235], v[132:135]\n"
      "s_cbranch_vccnz Lsl_22_%=\n"
      "Lbk_23_%=:\n"
      "v_add_f32 v98, 1.0, v98\n"
      "v_mfma_f32_16x16x32_bf16 v[132:135], v[36:39], a[236:239], v[132:135]\n"
      "buffer_load_dwordx4 v6, s[52:55], s96 offen offset:2048 lds\n"
      "v_mfma_f32_16x16x32_bf16 v[136:139], v[32:35], a[240:243], v[136:139]\n"
      "ds_read_b64_tr_b16 v[60:61], v13 offset:12288\n"
      "ds_read_b64_tr_b16 v[62:63], v13 offset:12800\n"
      "v_mfma_f32_16x16x32_bf16 v[136:139], v[36:39], a[244:247], v[136:139]\n"
      "v_rcp_f32 v96, v96\n"
      "v_mfma_f32_16x16x32_bf16 v[140:143], v[32:35], a[248:251], v[140:143]\n"
      "v_exp_f32 v99, -v99\n"
      "v_mfma_f32_16x16x32_bf16 v[140:143], v[36:39], a[252:255], v[140:143]\n"
      "v_xor_b32 v11, 0x18040, v9\n"
      "s_waitcnt lgkmcnt(8)\n"
      "v_mfma_f32_16x16x32_bf16 v[144:147], v[40:43], a[224:227], v[144:147]\n"
      "ds_read_b64_tr_b16 v[32:33], v11\n"
      "ds_read_b64_tr_b16 v[34:35], v11 offset:512\n"
      "v_mfma_f32_16x16x32_bf16 v[144:147], v[44:47], a[228:231], v[144:147]\n"
      "buffer_load_dwordx4 v7, s[52:55], s96 offen offset:3072 lds\n"
      "v_mul_f32 v96, v96, v112\n"
      "v_mfma_f32_16x16x32_bf16 v[148:151], v[40:43], a[232:235], v[148:151]\n"
      "v_rcp_f32 v97, v97\n"
      "v_mfma_f32_16x16x32_bf16 v[148:151], v[44:47], a[236:239], v[148:151]\n"
      "v_exp_f32 v100, -v100\n"
      "v_mfma_f32_16x16x32_bf16 v[152:155], v[40:43], a[240:243], v[152:155]\n"
      "ds_read_b64_tr_b16 v[36:37], v11 offset:8192\n"
      "ds_read_b64_tr_b16 v[38:39], v11 offset:8704\n"
      "v_mfma_f32_16x16x32_bf16 v[152:155], v[44:47], a[244:247], v[152:155]\n"
      "s_add_u32 m0, s78, 147456\n"
      "s_add_u32 s96, s11, 0\n"
      "v_mfma_f32_16x16x32_bf16 v[156:159], v[40:43], a[248:251], v[156:159]\n"
      "v_rcp_f32 v98, v98\n"
      "v_mfma_f32_16x16x32_bf16 v[156:159], v[44:47], a[252:255], v[156:159]\n"
      "s_waitcnt lgkmcnt(8)\n"
      "buffer_load_dwordx4 v1, s[36:39], s96 offen sc1 lds\n"
      "v_mfma_f32_16x16x32_bf16 v[128:131], v[48:51], v[224:227], v[128:131]\n"
      "ds_read_b64_tr_b16 v[40:41], v11 offset:4096\n"
      "ds_read_b64_tr_b16 v[42:43], v11 offset:4608\n"
      "v_mfma_f32_16x16x32_bf16 v[128:131], v[52:55], v[228:231], v[128:131]\n"
      "v_exp_f32 v101, -v101\n"
      "v_mfma_f32_16x16x32_bf16 v[132:135], v[48:51], v[232:235], v[132:135]\n"
      "v_exp_f32 v102, -v102\n"
      "v_mfma_f32_16x16x32_bf16 v[132:135], v[52:55], v[236:239], v[132:135]\n"
      "s_add_u32 m0, s78, 151552\n"
      "s_add_u32 s96, s11, 4096\n"
      "v_mfma_f32_16x16x32_bf16 v[136:139], v[48:51], v[240:243], v[136:139]\n"
      "ds_read_b64_tr_b16 v[44:45], v11 offset:12288\n"
      "ds_read_b64_tr_b16 v[46:47], v11 offset:12800\n"
      "v_mfma_f32_16x16x32_bf16 v[136:139], v[52:55], v[244:247], v[136:139]\n"
      "buffer_load_dwordx4 v1, s[36:39], s96 offen sc1 lds\n"
      "v_add_f32 v99, 1.0, v99\n"
      "v_mfma_f32_16x16x32_bf16 v[140:143], v[48:51], v[248:251], v[140:143]\n"
      "v_rcp_f32 v99, v99\n"
      "v_mfma_f32_16x16x32_bf16 v[140:143], v[52:55], v[252:255], v[140:143]\n"
      "v_xor_b32 v13, 0x1c040, v9\n"
      "s_waitcnt lgkmcnt(8)\n"
      "v_mfma_f32_16x16x32_bf16 v[144:147], v[56:59], v[224:227], v[144:147]\n"
      "ds_read_b64_tr_b16 v[48:49], v13\n"
      "ds_read_b64_tr_b16 v[50:51], v13 offset:512\n"
      "v_mfma_f32_16x16x32_bf16 v[144:147], v[60:63], v[228:231], v[144:147]\n"
      "s_add_u32 m0, s78, 155648\n"
      "s_add_u32 s96, s11, 8192\n"
      "v_mfma_f32_16x16x32_bf16 v[148:151], v[56:59], v[232:235], v[148:151]\n"
      "v_exp_f32 v103, -v103\n"
      "v_mfma_f32_16x16x32_bf16 v[148:151], v[60:63], v[236:239], v[148:151]\n"
      "buffer_load_dwordx4 v1, s[36:39], s96 offen sc1 lds\n"
      "v_mul_f32 v97, v97, v113\n"
      "v_mfma_f32_16x16x32_bf16 v[152:155], v[56:59], v[240:243], v[152:155]\n"
      "ds_read_b64_tr_b16 v[52:53], v13 offset:8192\n"
      "ds_read_b64_tr_b16 v[54:55], v13 offset:8704\n"
      "v_mfma_f32_16x16x32_bf16 v[152:155], v[60:63], v[244:247], v[152:155]\n"
      "v_add_f32 v100, 1.0, v100\n"
      "v_mul_f32 v98, v98, v114\n"
      "v_mfma_f32_16x16x32_bf16 v[156:159], v[56:59], v[248:251], v[156:159]\n"
      "v_rcp_f32 v100, v100\n"
      "v_mfma_f32_16x16x32_bf16 v[156:159], v[60:63], v[252:255], v[156:159]\n"
      "s_waitcnt lgkmcnt(8)\n"
      "v_add_f32 v101, 1.0, v101\n"
      "v_mfma_f32_16x16x32_bf16 v[160:163], v[32:35], a[192:195], v[160:163]\n"
      "ds_read_b64_tr_b16 v[56:57], v13 offset:4096\n"
      "ds_read_b64_tr_b16 v[58:59], v13 offset:4608\n"
      "v_mfma_f32_16x16x32_bf16 v[160:163], v[36:39], a[196:199], v[160:163]\n"
      "s_add_u32 m0, s78, 159744\n"
      "s_add_u32 s96, s11, 12288\n"
      "v_mfma_f32_16x16x32_bf16 v[164:167], v[32:35], a[200:203], v[164:167]\n"
      "buffer_load_dwordx4 v1, s[36:39], s96 offen sc1 lds\n"
      "v_mul_f32 v99, v99, v115\n"
      "v_mfma_f32_16x16x32_bf16 v[164:167], v[36:39], a[204:207], v[164:167]\n"
      "v_rcp_f32 v101, v101\n"
      "v_mfma_f32_16x16x32_bf16 v[168:171], v[32:35], a[208:211], v[168:171]\n"
      "ds_read_b64_tr_b16 v[60:61], v13 offset:12288\n"
      "ds_read_b64_tr_b16 v[62:63], v13 offset:12800\n"
      "v_mfma_f32_16x16x32_bf16 v[168:171], v[36:39], a[212:215], v[168:171]\n"
      "s_add_u32 m0, s78, 0\n"
      "s_add_u32 s96, s11, 16384\n"
      "v_mfma_f32_16x16x32_bf16 v[172:175], v[32:35], a[216:219], v[172:175]\n"
      "v_add_f32 v102, 1.0, v102\n"
      "v_mul_f32 v100, v100, v116\n"
      "v_mfma_f32_16x16x32_bf16 v[172:175], v[36:39], a[220:223], v[172:175]\n"
      "v_xor_b32 v11, 0x20040, v9\n"
      "s_waitcnt lgkmcnt(8)\n"
      "v_mfma_f32_16x16x32_bf16 v[176:179], v[40:43], a[192:195], v[176:179]\n"
      "ds_read_b64_tr_b16 v[32:33], v11\n"
      "ds_read_b64_tr_b16 v[34:35], v11 offset:512\n"
      "v_mfma_f32_16x16x32_bf16 v[176:179], v[44:47], a[196:199], v[176:179]\n"
      "buffer_load_dwordx4 v1, s[36:39], s96 offen sc1 lds\n"
      "v_add_f32 v103, 1.0, v103\n"
      "v_mfma_f32_16x16x32_bf16 v[180:183], v[40:43], a[200:203], v[180:183]\n"
      "v_rcp_f32 v102, v102\n"
      "v_mfma_f32_16x16x32_bf16 v[180:183], v[44:47], a[204:207], v[180:183]\n"
      "s_add_u32 m0, s78, 4096\n"
      "s_add_u32 s96, s11, 20480\n"
      "v_mfma_f32_16x16x32_bf16 v[184:187], v[40:43], a[208:211], v[184:187]\n"
      "ds_read_b64_tr_b16 v[36:37], v11 offset:8192\n"
      "ds_read_b64_tr_b16 v[38:39], v11 offset:8704\n"
      "v_mfma_f32_16x16x32_bf16 v[184:187], v[44:47], a[212:215], v[184:187]\n"
      "buffer_load_dwordx4 v1, s[36:39], s96 offen sc1 lds\n"
      "v_mul_f32 v101, v101, v117\n"
      "v_mfma_f32_16x16x32_bf16 v[188:191], v[40:43], a[216:219], v[188:191]\n"
      "v_rcp_f32 v103, v103\n"
      "v_mfma_f32_16x16x32_bf16 v[188:191], v[44:47], a[220:223], v[188:191]\n"
      "s_waitcnt lgkmcnt(8)\n"
      "v_mul_f32 v102, v102, v118\n"
      "v_mfma_f32_16x16x32_bf16 v[160:163], v[48:51], a[224:227], v[160:163]\n"
      "ds_read_b64_tr_b16 v[40:41], v11 offset:4096\n"
      "ds_read_b64_tr_b16 v[42:43], v11 offset:4608\n"
      "v_mfma_f32_16x16x32_bf16 v[160:163], v[52:55], a[228:231], v[160:163]\n"
      "s_add_u32 m0, s78, 8192\n"
      "s_add_u32 s96, s11, 24576\n"
      "v_mfma_f32_16x16x32_bf16 v[164:167], v[48:51], a[232:235], v[164:167]\n"
      "v_exp_f32 v104, -v104\n"
      "v_mfma_f32_16x16x32_bf16 v[164:167], v[52:55], a[236:239], v[164:167]\n"
      "buffer_load_dwordx4 v1, s[36:39], s96 offen sc1 lds\n"
      "v_mul_f32 v103, v103, v119\n"
      "v_mfma_f32_16x16x32_bf16 v[168:171], v[48:51], a[240:243], v[168:171]\n"
      "ds_read_b64_tr_b16 v[44:45], v11 offset:12288\n"
      "ds_read_b64_tr_b16 v[46:47], v11 offset:12800\n"
      "v_mfma_f32_16x16x32_bf16 v[168:171], v[52:55], a[244:247], v[168:171]\n"
      "v_exp_f32 v105, -v105\n"
      "v_mfma_f32_16x16x32_bf16 v[172:175], v[48:51], a[248:251], v[172:175]\n"
      "v_exp_f32 v106, -v106\n"
      "v_mfma_f32_16x16x32_bf16 v[172:175], v[52:55], a[252:255], v[172:175]\n"
      "v_xor_b32 v13, 0x18060, v9\n"
      "s_waitcnt lgkmcnt(8)\n"
      "v_mfma_f32_16x16x32_bf16 v[176:179], v[56:59], a[224:227], v[176:179]\n"
      "ds_read_b64_tr_b16 v[48:49], v13\n"
      "ds_read_b64_tr_b16 v[50:51], v13 offset:512\n"
      "v_mfma_f32_16x16x32_bf16 v[176:179], v[60:63], a[228:231], v[176:179]\n"
      "s_add_u32 m0, s78, 12288\n"
      "s_add_u32 s96, s11, 28672\n"
      "v_mfma_f32_16x16x32_bf16 v[180:183], v[56:59], a[232:235], v[180:183]\n"
      "buffer_load_dwordx4 v1, s[36:39], s96 offen sc1 lds\n"
      "v_cvt_pk_bf16_f32 v112, v96, v97\n"
      "v_mfma_f32_16x16x32_bf16 v[180:183], v[60:63], a[236:239], v[180:183]\n"
      "v_exp_f32 v107, -v107\n"
      "v_mfma_f32_16x16x32_bf16 v[184:187], v[56:59], a[240:243], v[184:187]\n"
      "ds_read_b64_tr_b16 v[52:53], v13 offset:8192\n"
      "ds_read_b64_tr_b16 v[54:55], v13 offset:8704\n"
      "v_mfma_f32_16x16x32_bf16 v[184:187], v[60:63], a[244:247], v[184:187]\n"
      "s_add_u32 m0, s78, 16384\n"
      "s_add_u32 s96, s11, 32768\n"
      "v_mfma_f32_16x16x32_bf16 v[188:191], v[56:59], a[248:251], v[188:191]\n"
      "v_exp_f32 v108, -v108\n"
      "v_mfma_f32_16x16x32_bf16 v[188:191], v[60:63], a[252:255], v[188:191]\n"
      "s_waitcnt lgkmcnt(8)\n"
      "buffer_load_dwordx4 v1, s[36:39], s96 offen sc1 lds\n"
      "v_mfma_f32_16x16x32_bf16 v[160:163], v[32:35], v[224:227], v[160:163]\n"
      "ds_read_b64_tr_b16 v[56:57], v13 offset:4096\n"
      "ds_read_b64_tr_b16 v[58:59], v13 offset:4608\n"
      "v_mfma_f32_16x16x32_bf16 v[160:163], v[36:39], v[228:231], v[160:163]\n"
      "v_cvt_pk_bf16_f32 v114, v100, v101\n"
      "v_cvt_pk_bf16_f32 v113, v98, v99\n"
      "v_mfma_f32_16x16x32_bf16 v[164:167], v[32:35], v[232:235], v[164:167]\n"
      "v_exp_f32 v109, -v109\n"
      "v_mfma_f32_16x16x32_bf16 v[164:167], v[36:39], v[236:239], v[164:167]\n"
      "v_cvt_pk_bf16_f32 v115, v102, v103\n"
      "v_permlane16_swap_b32 v112, v114\n"
      "v_mfma_f32_16x16x32_bf16 v[168:171], v[32:35], v[240:243], v[168:171]\n"
      "ds_read_b64_tr_b16 v[60:61], v13 offset:12288\n"
      "ds_read_b64_tr_b16 v[62:63], v13 offset:12800\n"
      "v_mfma_f32_16x16x32_bf16 v[168:171], v[36:39], v[244:247], v[168:171]\n"
      "v_permlane16_swap_b32 v113, v115\n"
      "v_add_f32 v104, 1.0, v104\n"
      "v_mfma_f32_16x16x32_bf16 v[172:175], v[32:35], v[248:251], v[172:175]\n"
      "s_add_u32 s95, s35, 0\n"
      "buffer_store_dwordx4 v[112:115], v72, s[36:39], s95 offen sc1\n"
      "v_mfma_f32_16x16x32_bf16 v[172:175], v[36:39], v[252:255], v[172:175]\n"
      "v_xor_b32 v11, 0x1c060, v9\n"
      "s_waitcnt lgkmcnt(8)\n"
      "v_mfma_f32_16x16x32_bf16 v[176:179], v[40:43], v[224:227], v[176:179]\n"
      "ds_read_b64_tr_b16 v[32:33], v11\n"
      "ds_read_b64_tr_b16 v[34:35], v11 offset:512\n"
      "v_mfma_f32_16x16x32_bf16 v[176:179], v[44:47], v[228:231], v[176:179]\n"
      "v_rcp_f32 v104, v104\n"
      "v_mfma_f32_16x16x32_bf16 v[180:183], v[40:43], v[232:235], v[180:183]\n"
      "v_exp_f32 v110, -v110\n"
      "v_mfma_f32_16x16x32_bf16 v[180:183], v[44:47], v[236:239], v[180:183]\n"
      "v_exp_f32 v111, -v111\n"
      "v_mfma_f32_16x16x32_bf16 v[184:187], v[40:43], v[240:243], v[184:187]\n"
      "ds_read_b64_tr_b16 v[36:37], v11 offset:8192\n"
      "ds_read_b64_tr_b16 v[38:39], v11 offset:8704\n"
      "v_mfma_f32_16x16x32_bf16 v[184:187], v[44:47], v[244:247], v[184:187]\n"
      "v_add_f32 v105, 1.0, v105\n"
      "v_add_f32 v106, 1.0, v106\n"
      "v_mfma_f32_16x16x32_bf16 v[188:191], v[40:43], v[248:251], v[188:191]\n"
      "v_rcp_f32 v105, v105\n"
      "v_mfma_f32_16x16x32_bf16 v[188:191], v[44:47], v[252:255], v[188:191]\n"
      "s_waitcnt lgkmcnt(8)\n"
      "v_mul_f32 v104, v104, v120\n"
      "v_mfma_f32_16x16x32_bf16 v[192:195], v[48:51], a[192:195], v[192:195]\n"
      "ds_read_b64_tr_b16 v[40:41], v11 offset:4096\n"
      "ds_read_b64_tr_b16 v[42:43], v11 offset:4608\n"
      "v_mfma_f32_16x16x32_bf16 v[192:195], v[52:55], a[196:199], v[192:195]\n"
      "v_rcp_f32 v106, v106\n"
      "v_mfma_f32_16x16x32_bf16 v[196:199], v[48:51], a[200:203], v[196:199]\n"
      "v_add_f32 v107, 1.0, v107\n"
      "v_mul_f32 v105, v105, v121\n"
      "v_mfma_f32_16x16x32_bf16 v[196:199], v[52:55], a[204:207], v[196:199]\n"
      "v_rcp_f32 v107, v107\n"
      "v_mfma_f32_16x16x32_bf16 v[200:203], v[48:51], a[208:211], v[200:203]\n"
      "ds_read_b64_tr_b16 v[44:45], v11 offset:12288\n"
      "ds_read_b64_tr_b16 v[46:47], v11 offset:12800\n"
      "v_mfma_f32_16x16x32_bf16 v[200:203], v[52:55], a[212:215], v[200:203]\n"
      "v_add_f32 v108, 1.0, v108\n"
      "v_mul_f32 v106, v106, v122\n"
      "v_mfma_f32_16x16x32_bf16 v[204:207], v[48:51], a[216:219], v[204:207]\n"
      "v_rcp_f32 v108, v108\n"
      "v_mfma_f32_16x16x32_bf16 v[204:207], v[52:55], a[220:223], v[204:207]\n"
      "v_xor_b32 v13, 0x20060, v9\n"
      "s_waitcnt lgkmcnt(8)\n"
      "v_mfma_f32_16x16x32_bf16 v[208:211], v[56:59], a[192:195], v[208:211]\n"
      "s_waitcnt vmcnt(6)\n"
      "s_barrier\n"
      "ds_read_b64_tr_b16 v[48:49], v13\n"
      "ds_read_b64_tr_b16 v[50:51], v13 offset:512\n"
      "v_mfma_f32_16x16x32_bf16 v[208:211], v[60:63], a[196:199], v[208:211]\n"
      "s_add_u32 m0, s81, 98304\n"
      "s_add_u32 s96, s10, 2359808\n"
      "v_mfma_f32_16x16x32_bf16 v[212:215], v[56:59], a[200:203], v[212:215]\n"
      "buffer_load_dwordx4 v4, s[52:55], s96 offen lds\n"
      "v_add_f32 v109, 1.0, v109\n"
      "v_mfma_f32_16x16x32_bf16 v[212:215], v[60:63], a[204:207], v[212:215]\n"
      "v_rcp_f32 v109, v109\n"
      "v_mfma_f32_16x16x32_bf16 v[216:219], v[56:59], a[208:211], v[216:219]\n"
      "ds_read_b64_tr_b16 v[52:53], v13 offset:8192\n"
      "ds_read_b64_tr_b16 v[54:55], v13 offset:8704\n"
      "v_mfma_f32_16x16x32_bf16 v[216:219], v[60:63], a[212:215], v[216:219]\n"
      "buffer_load_dwordx4 v5, s[52:55], s96 offen offset:1024 lds\n"
      "v_mul_f32 v107, v107, v123\n"
      "v_mfma_f32_16x16x32_bf16 v[220:223], v[56:59], a[216:219], v[220:223]\n"
      "v_add_f32 v110, 1.0, v110\n"
      "v_mul_f32 v108, v108, v124\n"
      "v_mfma_f32_16x16x32_bf16 v[220:223], v[60:63], a[220:223], v[220:223]\n"
      "s_waitcnt lgkmcnt(8)\n"
      "v_add_f32 v111, 1.0, v111\n"
      "v_mfma_f32_16x16x32_bf16 v[192:195], v[32:35], a[224:227], v[192:195]\n"
      "ds_read_b64_tr_b16 v[56:57], v13 offset:4096\n"
      "ds_read_b64_tr_b16 v[58:59], v13 offset:4608\n"
      "v_mfma_f32_16x16x32_bf16 v[192:195], v[36:39], a[228:231], v[192:195]\n"
      "v_rcp_f32 v110, v110\n"
      "v_mfma_f32_16x16x32_bf16 v[196:199], v[32:35], a[232:235], v[196:199]\n"
      "v_rcp_f32 v111, v111\n"
      "v_mfma_f32_16x16x32_bf16 v[196:199], v[36:39], a[236:239], v[196:199]\n"
      "buffer_load_dwordx4 v6, s[52:55], s96 offen offset:2048 lds\n"
      "v_mul_f32 v109, v109, v125\n"
      "v_mfma_f32_16x16x32_bf16 v[200:203], v[32:35], a[240:243], v[200:203]\n"
      "ds_read_b64_tr_b16 v[60:61], v13 offset:12288\n"
      "ds_read_b64_tr_b16 v[62:63], v13 offset:12800\n"
      "v_mfma_f32_16x16x32_bf16 v[200:203], v[36:39], a[244:247], v[200:203]\n"
      "v_exp_f32 v128, -v128\n"
      "v_mfma_f32_16x16x32_bf16 v[204:207], v[32:35], a[248:251], v[204:207]\n"
      "v_exp_f32 v129, -v129\n"
      "v_mfma_f32_16x16x32_bf16 v[204:207], v[36:39], a[252:255], v[204:207]\n"
      "s_waitcnt lgkmcnt(8)\n"
      "v_mul_f32 v110, v110, v126\n"
      "v_mfma_f32_16x16x32_bf16 v[208:211], v[40:43], a[224:227], v[208:211]\n"
      "ds_read_b128 v[32:35], v29 offset:16384\n"
      "v_mul_f32 v111, v111, v127\n"
      "v_mfma_f32_16x16x32_bf16 v[208:211], v[44:47], a[228:231], v[208:211]\n"
      "buffer_load_dwordx4 v7, s[52:55], s96 offen offset:3072 lds\n"
      "v_cvt_pk_bf16_f32 v120, v104, v105\n"
      "v_mfma_f32_16x16x32_bf16 v[212:215], v[40:43], a[232:235], v[212:215]\n"
      "v_exp_f32 v130, -v130\n"
      "v_mfma_f32_16x16x32_bf16 v[212:215], v[44:47], a[236:239], v[212:215]\n"
      "v_exp_f32 v131, -v131\n"
      "v_mfma_f32_16x16x32_bf16 v[216:219], v[40:43], a[240:243], v[216:219]\n"
      "ds_read_b128 v[36:39], v29 offset:17408\n"
      "v_cvt_pk_bf16_f32 v122, v108, v109\n"
      "v_mfma_f32_16x16x32_bf16 v[216:219], v[44:47], a[244:247], v[216:219]\n"
      "v_exp_f32 v132, -v132\n"
      "v_mfma_f32_16x16x32_bf16 v[220:223], v[40:43], a[248:251], v[220:223]\n"
      "v_cvt_pk_bf16_f32 v121, v106, v107\n"
      "v_cvt_pk_bf16_f32 v123, v110, v111\n"
      "v_mfma_f32_16x16x32_bf16 v[220:223], v[44:47], a[252:255], v[220:223]\n"
      "Lpad8_%=:\n"
      "s_cbranch_execz Lpad9_%=\n"
      "s_waitcnt lgkmcnt(6)\n"
      "v_permlane16_swap_b32 v120, v122\n"
      "v_mfma_f32_16x16x32_bf16 v[192:195], v[48:51], v[224:227], v[192:195]\n"
      "ds_read_b128 v[40:43], v29 offset:18432\n"
      "v_permlane16_swap_b32 v121, v123\n"
      "v_mfma_f32_16x16x32_bf16 v[192:195], v[52:55], v[228:231], v[192:195]\n"
      "s_add_u32 s95, s35, 294912\n"
      "buffer_store_dwordx4 v[120:123], v72, s[36:39], s95 offen sc1\n"
      "v_mfma_f32_16x16x32_bf16 v[196:199], v[48:51], v[232:235], v[196:199]\n"
      "v_exp_f32 v133, -v133\n"
      "v_mfma_f32_16x16x32_bf16 v[196:199], v[52:55], v[236:239], v[196:199]\n"
      "v_exp_f32 v134, -v134\n"
      "v_mfma_f32_16x16x32_bf16 v[200:203], v[48:51], v[240:243], v[200:203]\n"
      "ds_read_b128 v[44:47], v29 offset:19456\n"
      "v_add_f32 v128, 1.0, v128\n"
      "v_mfma_f32_16x16x32_bf16 v[200:203], v[52:55], v[244:247], v[200:203]\n"
      "v_rcp_f32 v128, v128\n"
      "v_mfma_f32_16x16x32_bf16 v[204:207], v[48:51], v[248:251], v[204:207]\n"
      "v_exp_f32 v135, -v135\n"
      "v_mfma_f32_16x16x32_bf16 v[204:207], v[52:55], v[252:255], v[204:207]\n"
      "s_waitcnt lgkmcnt(4)\n"
      "v_add_f32 v129, 1.0, v129\n"
      "v_mfma_f32_16x16x32_bf16 v[208:211], v[56:59], v[224:227], v[208:211]\n"
      "ds_read_b128 v[48:51], v29 offset:20480\n"
      "v_add_f32 v130, 1.0, v130\n"
      "v_mfma_f32_16x16x32_bf16 v[208:211], v[60:63], v[228:231], v[208:211]\n"
      "v_rcp_f32 v129, v129\n"
      "v_mfma_f32_16x16x32_bf16 v[212:215], v[56:59], v[232:235], v[212:215]\n"
      "v_rcp_f32 v130, v130\n"
      "v_mfma_f32_16x16x32_bf16 v[212:215], v[60:63], v[236:239], v[212:215]\n"
      "v_mul_f32 v128, v128, v144\n"
      "v_add_f32 v131, 1.0, v131\n"
      "v_mfma_f32_16x16x32_bf16 v[216:219], v[56:59], v[240:243], v[216:219]\n"
      "ds_read_b128 v[52:55], v29 offset:21504\n"
      "v_mul_f32 v129, v129, v145\n"
      "v_mfma_f32_16x16x32_bf16 v[216:219], v[60:63], v[244:247], v[216:219]\n"
      "v_rcp_f32 v131, v131\n"
      "v_mfma_f32_16x16x32_bf16 v[220:223], v[56:59], v[248:251], v[220:223]\n"
      "v_add_f32 v132, 1.0, v132\n"
      "v_mul_f32 v130, v130, v146\n"
      "v_mfma_f32_16x16x32_bf16 v[220:223], v[60:63], v[252:255], v[220:223]\n"
      "s_waitcnt lgkmcnt(4)\n"
      "v_add_f32 v133, 1.0, v133\n"
      "v_mfma_f32_16x16x32_bf16 v[96:99], v[32:35], a[0:3], 0\n"
      "ds_read_b128 v[56:59], v29 offset:22528\n"
      "v_mul_f32 v131, v131, v147\n"
      "v_mfma_f32_16x16x32_bf16 v[96:99], v[36:39], a[4:7], v[96:99]\n"
      "s_getpc_b64 s[90:91]\n"
      "Lpc_24_%=:\n"
      "s_sub_u32 s90, s90, Lpc_24_%=-Lstart_%=\n"
      "s_subb_u32 s91, s91, 0\n"
      "global_load_dword v20, v79, s[90:91]\n"
      "v_mfma_f32_16x16x32_bf16 v[100:103], v[32:35], a[8:11], 0\n"
      "buffer_load_dwordx2 v[70:71], v69, s[36:39], 0 offen sc1\n"
      "v_add_f32 v134, 1.0, v134\n"
      "v_mfma_f32_16x16x32_bf16 v[100:103], v[36:39], a[12:15], v[100:103]\n"
      "v_rcp_f32 v132, v132\n"
      "v_mfma_f32_16x16x32_bf16 v[104:107], v[32:35], a[16:19], 0\n"
      "ds_read_b128 v[60:63], v29 offset:23552\n"
      "v_mul_f32 v132, v132, v148\n"
      "v_mfma_f32_16x16x32_bf16 v[104:107], v[36:39], a[20:23], v[104:107]\n"
      "v_rcp_f32 v133, v133\n"
      "v_mfma_f32_16x16x32_bf16 v[108:111], v[32:35], a[24:27], 0\n"
      "v_rcp_f32 v134, v134\n"
      "v_mfma_f32_16x16x32_bf16 v[108:111], v[36:39], a[28:31], v[108:111]\n"
      "s_waitcnt lgkmcnt(4)\n"
      "v_add_f32 v135, 1.0, v135\n"
      "v_mfma_f32_16x16x32_bf16 v[112:115], v[40:43], a[0:3], 0\n"
      "ds_read_b128 v[32:35], v29 offset:24576\n"
      "v_mul_f32 v133, v133, v149\n"
      "v_mfma_f32_16x16x32_bf16 v[112:115], v[44:47], a[4:7], v[112:115]\n"
      "v_rcp_f32 v135, v135\n"
      "v_mfma_f32_16x16x32_bf16 v[116:119], v[40:43], a[8:11], 0\n"
      "v_exp_f32 v136, -v136\n"
      "v_mfma_f32_16x16x32_bf16 v[116:119], v[44:47], a[12:15], v[116:119]\n"
      "v_exp_f32 v137, -v137\n"
      "v_mfma_f32_16x16x32_bf16 v[120:123], v[40:43], a[16:19], 0\n"
      "ds_read_b128 v[36:39], v29 offset:25600\n"
      "v_mul_f32 v134, v134, v150\n"
      "v_mfma_f32_16x16x32_bf16 v[120:123], v[44:47], a[20:23], v[120:123]\n"
      "v_exp_f32 v138, -v138\n"
      "v_mfma_f32_16x16x32_bf16 v[124:127], v[40:43], a[24:27], 0\n"
      "v_exp_f32 v139, -v139\n"
      "v_mfma_f32_16x16x32_bf16 v[124:127], v[44:47], a[28:31], v[124:127]\n"
      "s_waitcnt lgkmcnt(4)\n"
      "v_mul_f32 v135, v135, v151\n"
      "v_mfma_f32_16x16x32_bf16 v[96:99], v[48:51], a[32:35], v[96:99]\n"
      "ds_read_b128 v[40:43], v29 offset:26624\n"
      "v_cvt_pk_bf16_f32 v144, v128, v129\n"
      "v_mfma_f32_16x16x32_bf16 v[96:99], v[52:55], a[36:39], v[96:99]\n"
      "v_exp_f32 v140, -v140\n"
      "v_mfma_f32_16x16x32_bf16 v[100:103], v[48:51], a[40:43], v[100:103]\n"
      "v_cvt_pk_bf16_f32 v146, v132, v133\n"
      "v_cvt_pk_bf16_f32 v145, v130, v131\n"
      "v_mfma_f32_16x16x32_bf16 v[100:103], v[52:55], a[44:47], v[100:103]\n"
      "v_exp_f32 v141, -v141\n"
      "v_mfma_f32_16x16x32_bf16 v[104:107], v[48:51], a[48:51], v[104:107]\n"
      "ds_read_b128 v[44:47], v29 offset:27648\n"
      "v_cvt_pk_bf16_f32 v147, v134, v135\n"
      "v_mfma_f32_16x16x32_bf16 v[104:107], v[52:55], a[52:55], v[104:107]\n"
      "v_permlane16_swap_b32 v144, v146\n"
      "v_permlane16_swap_b32 v145, v147\n"
      "v_mfma_f32_16x16x32_bf16 v[108:111], v[48:51], a[56:59], v[108:111]\n"
      "s_add_u32 s95, s35, 8192\n"
      "buffer_store_dwordx4 v[144:147], v72, s[36:39], s95 offen sc1\n"
      "v_mfma_f32_16x16x32_bf16 v[108:111], v[52:55], a[60:63], v[108:111]\n"
      "s_waitcnt lgkmcnt(4)\n"
      "v_add_f32 v136, 1.0, v136\n"
      "v_mfma_f32_16x16x32_bf16 v[112:115], v[56:59], a[32:35], v[112:115]\n"
      "s_waitcnt vmcnt(9)\n"
      "s_barrier\n"
      "ds_read_b128 v[48:51], v29 offset:28672\n"
      "v_mfma_f32_16x16x32_bf16 v[112:115], v[60:63], a[36:39], v[112:115]\n"
      "s_add_u32 m0, s81, 114688\n"
      "s_add_u32 s96, s10, 2949632\n"
      "v_mfma_f32_16x16x32_bf16 v[116:119], v[56:59], a[40:43], v[116:119]\n"
      "buffer_load_dwordx4 v4, s[52:55], s96 offen lds\n"
      "v_add_f32 v137, 1.0, v137\n"
      "v_mfma_f32_16x16x32_bf16 v[116:119], v[60:63], a[44:47], v[116:119]\n"
      "v_rcp_f32 v136, v136\n"
      "v_mfma_f32_16x16x32_bf16 v[120:123], v[56:59], a[48:51], v[120:123]\n"
      "ds_read_b128 v[52:55], v29 offset:29696\n"
      "v_add_f32 v138, 1.0, v138\n"
      "v_mfma_f32_16x16x32_bf16 v[120:123], v[60:63], a[52:55], v[120:123]\n"
      "buffer_load_dwordx4 v5, s[52:55], s96 offen offset:1024 lds\n"
      "v_mul_f32 v136, v136, v152\n"
      "v_mfma_f32_16x16x32_bf16 v[124:127], v[56:59], a[56:59], v[124:127]\n"
      "v_rcp_f32 v137, v137\n"
      "v_mfma_f32_16x16x32_bf16 v[124:127], v[60:63], a[60:63], v[124:127]\n"
      "s_waitcnt lgkmcnt(4)\n"
      "v_add_f32 v139, 1.0, v139\n"
      "v_mfma_f32_16x16x32_bf16 v[96:99], v[32:35], a[64:67], v[96:99]\n"
      "ds_read_b128 v[56:59], v29 offset:30720\n"
      "v_mul_f32 v137, v137, v153\n"
      "v_mfma_f32_16x16x32_bf16 v[96:99], v[36:39], a[68:71], v[96:99]\n"
      "v_rcp_f32 v138, v138\n"
      "v_mfma_f32_16x16x32_bf16 v[100:103], v[32:35], a[72:75], v[100:103]\n"
      "v_rcp_f32 v139, v139\n"
      "v_mfma_f32_16x16x32_bf16 v[100:103], v[36:39], a[76:79], v[100:103]\n"
      "buffer_load_dwordx4 v6, s[52:55], s96 offen offset:2048 lds\n"
      "v_add_f32 v140, 1.0, v140\n"
      "v_mfma_f32_16x16x32_bf16 v[104:107], v[32:35], a[80:83], v[104:107]\n"
      "ds_read_b128 v[60:63], v29 offset:31744\n"
      "v_mul_f32 v138, v138, v154\n"
      "v_mfma_f32_16x16x32_bf16 v[104:107], v[36:39], a[84:87], v[104:107]\n"
      "v_exp_f32 v142, -v142\n"
      "v_mfma_f32_16x16x32_bf16 v[108:111], v[32:35], a[88:91], v[108:111]\n"
      "v_rcp_f32 v140, v140\n"
      "v_mfma_f32_16x16x32_bf16 v[108:111], v[36:39], a[92:95], v[108:111]\n"
      "s_waitcnt lgkmcnt(4)\n"
      "v_add_f32 v141, 1.0, v141\n"
      "v_mfma_f32_16x16x32_bf16 v[112:115], v[40:43], a[64:67], v[112:115]\n"
      "ds_read_b128 v[32:35], v1\n"
      "v_mul_f32 v139, v139, v155\n"
      "v_mfma_f32_16x16x32_bf16 v[112:115], v[44:47], a[68:71], v[112:115]\n"
      "buffer_load_dwordx4 v7, s[52:55], s96 offen offset:3072 lds\n"
      "v_add_f32 v142, 1.0, v142\n"
      "v_mfma_f32_16x16x32_bf16 v[116:119], v[40:43], a[72:75], v[116:119]\n"
      "v_exp_f32 v143, -v143\n"
      "v_mfma_f32_16x16x32_bf16 v[116:119], v[44:47], a[76:79], v[116:119]\n"
      "v_rcp_f32 v141, v141\n"
      "v_mfma_f32_16x16x32_bf16 v[120:123], v[40:43], a[80:83], v[120:123]\n"
      "ds_read_b128 v[36:39], v1 offset:1024\n"
      "v_mul_f32 v140, v140, v156\n"
      "v_mfma_f32_16x16x32_bf16 v[120:123], v[44:47], a[84:87], v[120:123]\n"
      "s_add_u32 m0, s81, 131072\n"
      "s_add_u32 s96, s10, 3539456\n"
      "v_mfma_f32_16x16x32_bf16 v[124:127], v[40:43], a[88:91], v[124:127]\n"
      "v_rcp_f32 v142, v142\n"
      "v_mfma_f32_16x16x32_bf16 v[124:127], v[44:47], a[92:95], v[124:127]\n"
      "s_waitcnt lgkmcnt(4)\n"
      "buffer_load_dwordx4 v4, s[52:55], s96 offen lds\n"
      "v_mfma_f32_16x16x32_bf16 v[96:99], v[48:51], a[96:99], v[96:99]\n"
      "ds_read_b128 v[40:43], v1 offset:2048\n"
      "v_add_f32 v143, 1.0, v143\n"
      "v_mfma_f32_16x16x32_bf16 v[96:99], v[52:55], a[100:103], v[96:99]\n"
      "v_rcp_f32 v143, v143\n"
      "v_mfma_f32_16x16x32_bf16 v[100:103], v[48:51], a[104:107], v[100:103]\n"
      "v_exp_f32 v160, -v160\n"
      "v_mfma_f32_16x16x32_bf16 v[100:103], v[52:55], a[108:111], v[100:103]\n"
      "v_exp_f32 v161, -v161\n"
      "v_mfma_f32_16x16x32_bf16 v[104:107], v[48:51], a[112:115], v[104:107]\n"
      "ds_read_b128 v[44:47], v1 offset:3072\n"
      "v_mul_f32 v141, v141, v157\n"
      "v_mfma_f32_16x16x32_bf16 v[104:107], v[52:55], a[116:119], v[104:107]\n"
      "buffer_load_dwordx4 v5, s[52:55], s96 offen offset:1024 lds\n"
      "v_mul_f32 v142, v142, v158\n"
      "v_mfma_f32_16x16x32_bf16 v[108:111], v[48:51], a[120:123], v[108:111]\n"
      "v_exp_f32 v162, -v162\n"
      "v_mfma_f32_16x16x32_bf16 v[108:111], v[52:55], a[124:127], v[108:111]\n"
      "s_waitcnt lgkmcnt(4)\n"
      "v_mul_f32 v143, v143, v159\n"
      "v_mfma_f32_16x16x32_bf16 v[112:115], v[56:59], a[96:99], v[112:115]\n"
      "ds_read_b128 v[48:51], v1 offset:4096\n"
      "v_cvt_pk_bf16_f32 v152, v136, v137\n"
      "v_mfma_f32_16x16x32_bf16 v[112:115], v[60:63], a[100:103], v[112:115]\n"
      "v_exp_f32 v163, -v163\n"
      "v_mfma_f32_16x16x32_bf16 v[116:119], v[56:59], a[104:107], v[116:119]\n"
      "v_exp_f32 v164, -v164\n"
      "v_mfma_f32_16x16x32_bf16 v[116:119], v[60:63], a[108:111], v[116:119]\n"
      "buffer_load_dwordx4 v6, s[52:55], s96 offen offset:2048 lds\n"
      "v_cvt_pk_bf16_f32 v154, v140, v141\n"
      "v_mfma_f32_16x16x32_bf16 v[120:123], v[56:59], a[112:115], v[120:123]\n"
      "ds_read_b128 v[52:55], v1 offset:5120\n"
      "v_cvt_pk_bf16_f32 v153, v138, v139\n"
      "v_mfma_f32_16x16x32_bf16 v[120:123], v[60:63], a[116:119], v[120:123]\n"
      "v_exp_f32 v165, -v165\n"
      "v_mfma_f32_16x16x32_bf16 v[124:127], v[56:59], a[120:123], v[124:127]\n"
      "v_cvt_pk_bf16_f32 v155, v142, v143\n"
      "v_permlane16_swap_b32 v152, v154\n"
      "v_mfma_f32_16x16x32_bf16 v[124:127], v[60:63], a[124:127], v[124:127]\n"
      "s_waitcnt lgkmcnt(4)\n"
      "v_permlane16_swap_b32 v153, v155\n"
      "v_mfma_f32_16x16x32_bf16 v[96:99], v[32:35], a[128:131], v[96:99]\n"
      "ds_read_b128 v[56:59], v1 offset:6144\n"
      "v_add_f32 v160, 1.0, v160\n"
      "v_mfma_f32_16x16x32_bf16 v[96:99], v[36:39], a[132:135], v[96:99]\n"
      "buffer_load_dwordx4 v7, s[52:55], s96 offen offset:3072 lds\n"
      "v_add_f32 v161, 1.0, v161\n"
      "v_mfma_f32_16x16x32_bf16 v[100:103], v[32:35], a[136:139], v[100:103]\n"
      "s_add_u32 s95, s35, 303104\n"
      "buffer_store_dwordx4 v[152:155], v72, s[36:39], s95 offen sc1\n"
      "v_mfma_f32_16x16x32_bf16 v[100:103], v[36:39], a[140:143], v[100:103]\n"
      "v_rcp_f32 v160, v160\n"
      "v_mfma_f32_16x16x32_bf16 v[104:107], v[32:35], a[144:147], v[104:107]\n"
      "ds_read_b128 v[60:63], v1 offset:7168\n"
      "v_add_f32 v162, 1.0, v162\n"
      "v_mfma_f32_16x16x32_bf16 v[104:107], v[36:39], a[148:151], v[104:107]\n"
      "v_rcp_f32 v161, v161\n"
      "v_mfma_f32_16x16x32_bf16 v[108:111], v[32:35], a[152:155], v[108:111]\n"
      "v_rcp_f32 v162, v162\n"
      "v_mfma_f32_16x16x32_bf16 v[108:111], v[36:39], a[156:159], v[108:111]\n"
      "s_waitcnt lgkmcnt(4)\n"
      "v_mul_f32 v160, v160, v176\n"
      "v_mfma_f32_16x16x32_bf16 v[112:115], v[40:43], a[128:131], v[112:115]\n"
      "ds_read_b128 v[32:35], v1 offset:8192\n"
      "v_add_f32 v163, 1.0, v163\n"
      "v_mfma_f32_16x16x32_bf16 v[112:115], v[44:47], a[132:135], v[112:115]\n"
      "v_rcp_f32 v163, v163\n"
      "v_mfma_f32_16x16x32_bf16 v[116:119], v[40:43], a[136:139], v[116:119]\n"
      "v_exp_f32 v166, -v166\n"
      "v_mfma_f32_16x16x32_bf16 v[116:119], v[44:47], a[140:143], v[116:119]\n"
      "v_exp_f32 v167, -v167\n"
      "v_mfma_f32_16x16x32_bf16 v[120:123], v[40:43], a[144:147], v[120:123]\n"
      "ds_read_b128 v[36:39], v1 offset:9216\n"
      "v_mul_f32 v161, v161, v177\n"
      "v_mfma_f32_16x16x32_bf16 v[120:123], v[44:47], a[148:151], v[120:123]\n"
      "v_add_f32 v164, 1.0, v164\n"
      "v_mul_f32 v162, v162, v178\n"
      "v_mfma_f32_16x16x32_bf16 v[124:127], v[40:43], a[152:155], v[124:127]\n"
      "v_rcp_f32 v164, v164\n"
      "v_mfma_f32_16x16x32_bf16 v[124:127], v[44:47], a[156:159], v[124:127]\n"
      "s_waitcnt lgkmcnt(4)\n"
      "v_add_f32 v165, 1.0, v165\n"
      "v_mfma_f32_16x16x32_bf16 v[96:99], v[48:51], a[160:163], v[96:99]\n"
      "ds_read_b128 v[40:43], v1 offset:10240\n"
      "v_mul_f32 v163, v163, v179\n"
      "v_mfma_f32_16x16x32_bf16 v[96:99], v[52:55], a[164:167], v[96:99]\n"
      "v_rcp_f32 v165, v165\n"
      "v_mfma_f32_16x16x32_bf16 v[100:103], v[48:51], a[168:171], v[100:103]\n"
      "v_add_f32 v166, 1.0, v166\n"
      "v_mul_f32 v164, v164, v180\n"
      "v_mfma_f32_16x16x32_bf16 v[100:103], v[52:55], a[172:175], v[100:103]\n"
      "v_rcp_f32 v166, v166\n"
      "v_mfma_f32_16x16x32_bf16 v[104:107], v[48:51], a[176:179], v[104:107]\n"
      "ds_read_b128 v[44:47], v1 offset:11264\n"
      "v_add_f32 v167, 1.0, v167\n"
      "v_mfma_f32_16x16x32_bf16 v[104:107], v[52:55], a[180:183], v[104:107]\n"
      "v_rcp_f32 v167, v167\n"
      "v_mfma_f32_16x16x32_bf16 v[108:111], v[48:51], a[184:187], v[108:111]\n"
      "v_exp_f32 v168, -v168\n"
      "v_mfma_f32_16x16x32_bf16 v[108:111], v[52:55], a[188:191], v[108:111]\n"
      "s_waitcnt lgkmcnt(4)\n"
      "v_mul_f32 v165, v165, v181\n"
      "v_mfma_f32_16x16x32_bf16 v[112:115], v[56:59], a[160:163], v[112:115]\n"
      "ds_read_b128 v[48:51], v1 offset:12288\n"
      "v_mul_f32 v166, v166, v182\n"
      "v_mfma_f32_16x16x32_bf16 v[112:115], v[60:63], a[164:167], v[112:115]\n"
      "v_exp_f32 v169, -v169\n"
      "v_mfma_f32_16x16x32_bf16 v[116:119], v[56:59], a[168:171], v[116:119]\n"
      "v_exp_f32 v170, -v170\n"
      "v_mfma_f32_16x16x32_bf16 v[116:119], v[60:63], a[172:175], v[116:119]\n"
      "v_exp_f32 v171, -v171\n"
      "v_mfma_f32_16x16x32_bf16 v[120:123], v[56:59], a[176:179], v[120:123]\n"
      "ds_read_b128 v[52:55], v1 offset:13312\n"
      "v_mul_f32 v167, v167, v183\n"
      "v_mfma_f32_16x16x32_bf16 v[120:123], v[60:63], a[180:183], v[120:123]\n"
      "v_exp_f32 v172, -v172\n"
      "v_mfma_f32_16x16x32_bf16 v[124:127], v[56:59], a[184:187], v[124:127]\n"
      "v_cvt_pk_bf16_f32 v176, v160, v161\n"
      "v_cvt_pk_bf16_f32 v178, v164, v165\n"
      "v_mfma_f32_16x16x32_bf16 v[124:127], v[60:63], a[188:191], v[124:127]\n"
      "s_waitcnt lgkmcnt(4)\n"
      "v_cvt_pk_bf16_f32 v177, v162, v163\n"
      "v_mfma_f32_16x16x32_bf16 v[96:99], v[32:35], a[192:195], v[96:99]\n"
      "ds_read_b128 v[56:59], v1 offset:14336\n"
      "v_cvt_pk_bf16_f32 v179, v166, v167\n"
      "v_mfma_f32_16x16x32_bf16 v[96:99], v[36:39], a[196:199], v[96:99]\n"
      "s_waitcnt vmcnt(10)\n"
      "v_cmp_ne_u64 vcc, s[76:77], v[70:71]\n"
      "v_mfma_f32_16x16x32_bf16 v[100:103], v[32:35], a[200:203], v[100:103]\n"
      "s_cbranch_vccnz Lsl_25_%=\n"
      "Lbk_26_%=:\n"
      "v_permlane16_swap_b32 v176, v178\n"
      "v_mfma_f32_16x16x32_bf16 v[100:103], v[36:39], a[204:207], v[100:103]\n"
      "v_exp_f32 v173, -v173\n"
      "v_mfma_f32_16x16x32_bf16 v[104:107], v[32:35], a[208:211], v[104:107]\n"
      "ds_read_b128 v[60:63], v1 offset:15360\n"
      "v_permlane16_swap_b32 v177, v179\n"
      "v_mfma_f32_16x16x32_bf16 v[104:107], v[36:39], a[212:215], v[104:107]\n"
      "s_add_u32 s95, s35, 16384\n"
      "buffer_store_dwordx4 v[176:179], v72, s[36:39], s95 offen sc1\n"
      "v_mfma_f32_16x16x32_bf16 v[108:111], v[32:35], a[216:219], v[108:111]\n"
      "v_exp_f32 v174, -v174\n"
      "v_mfma_f32_16x16x32_bf16 v[108:111], v[36:39], a[220:223], v[108:111]\n"
      "s_waitcnt lgkmcnt(4)\n"
      "v_add_f32 v168, 1.0, v168\n"
      "v_mfma_f32_16x16x32_bf16 v[112:115], v[40:43], a[192:195], v[112:115]\n"
      "ds_read_b128 v[32:35], v1 offset:16384\n"
      "v_add_f32 v169, 1.0, v169\n"
      "v_mfma_f32_16x16x32_bf16 v[112:115], v[44:47], a[196:199], v[112:115]\n"
      "v_rcp_f32 v168, v168\n"
      "v_mfma_f32_16x16x32_bf16 v[116:119], v[40:43], a[200:203], v[116:119]\n"
      "v_rcp_f32 v169, v169\n"
      "v_mfma_f32_16x16x32_bf16 v[116:119], v[44:47], a[204:207], v[116:119]\n"
      "v_exp_f32 v175, -v175\n"
      "v_mfma_f32_16x16x32_bf16 v[120:123], v[40:43], a[208:211], v[120:123]\n"
      "ds_read_b128 v[36:39], v1 offset:17408\n"
      "v_add_f32 v170, 1.0, v170\n"
      "v_mfma_f32_16x16x32_bf16 v[120:123], v[44:47], a[212:215], v[120:123]\n"
      "v_rcp_f32 v170, v170\n"
      "v_mfma_f32_16x16x32_bf16 v[124:127], v[40:43], a[216:219], v[124:127]\n"
      "v_mul_f32 v168, v168, v184\n"
      "v_add_f32 v171, 1.0, v171\n"
      "v_mfma_f32_16x16x32_bf16 v[124:127], v[44:47], a[220:223], v[124:127]\n"
      "s_waitcnt lgkmcnt(4)\n"
      "v_mul_f32 v169, v169, v185\n"
      "v_mfma_f32_16x16x32_bf16 v[96:99], v[48:51], a[224:227], v[96:99]\n"
      "ds_read_b128 v[40:43], v1 offset:18432\n"
      "v_add_f32 v172, 1.0, v172\n"
      "v_mfma_f32_16x16x32_bf16 v[96:99], v[52:55], a[228:231], v[96:99]\n"
      "v_rcp_f32 v171, v171\n"
      "v_mfma_f32_16x16x32_bf16 v[100:103], v[48:51], a[232:235], v[100:103]\n"
      "v_rcp_f32 v172, v172\n"
      "v_mfma_f32_16x16x32_bf16 v[100:103], v[52:55], a[236:239], v[100:103]\n"
      "v_mul_f32 v170, v170, v186\n"
      "v_add_f32 v173, 1.0, v173\n"
      "v_mfma_f32_16x16x32_bf16 v[104:107], v[48:51], a[240:243], v[104:107]\n"
      "ds_read_b128 v[44:47], v1 offset:19456\n"
      "v_mul_f32 v171, v171, v187\n"
      "v_mfma_f32_16x16x32_bf16 v[104:107], v[52:55], a[244:247], v[104:107]\n"
      "v_rcp_f32 v173, v173\n"
      "v_mfma_f32_16x16x32_bf16 v[108:111], v[48:51], a[248:251], v[108:111]\n"
      "v_add_f32 v174, 1.0, v174\n"
      "v_mul_f32 v172, v172, v188\n"
      "v_mfma_f32_16x16x32_bf16 v[108:111], v[52:55], a[252:255], v[108:111]\n"
      "v_xor_b32 v13, 0x8000, v9\n"
      "s_waitcnt lgkmcnt(4)\n"
      "v_mfma_f32_16x16x32_bf16 v[112:115], v[56:59], a[224:227], v[112:115]\n"
      "ds_read_b64_tr_b16 v[48:49], v13\n"
      "ds_read_b64_tr_b16 v[50:51], v13 offset:512\n"
      "v_mfma_f32_16x16x32_bf16 v[112:115], v[60:63], a[228:231], v[112:115]\n"
      "v_rcp_f32 v174, v174\n"
      "v_mfma_f32_16x16x32_bf16 v[116:119], v[56:59], a[232:235], v[116:119]\n"
      "v_exp_f32 v192, -v192\n"
      "v_mfma_f32_16x16x32_bf16 v[116:119], v[60:63], a[236:239], v[116:119]\n"
      "v_exp_f32 v193, -v193\n"
      "v_mfma_f32_16x16x32_bf16 v[120:123], v[56:59], a[240:243], v[120:123]\n"
      "ds_read_b64_tr_b16 v[52:53], v13 offset:8192\n"
      "ds_read_b64_tr_b16 v[54:55], v13 offset:8704\n"
      "v_mfma_f32_16x16x32_bf16 v[120:123], v[60:63], a[244:247], v[120:123]\n"
      "v_exp_f32 v194, -v194\n"
      "v_mfma_f32_16x16x32_bf16 v[124:127], v[56:59], a[248:251], v[124:127]\n"
      "v_add_f32 v175, 1.0, v175\n"
      "v_mul_f32 v173, v173, v189\n"
      "v_mfma_f32_16x16x32_bf16 v[124:127], v[60:63], a[252:255], v[124:127]\n"
      "s_waitcnt lgkmcnt(6)\n"
      "v_mul_f32 v174, v174, v190\n"
      "v_mfma_f32_16x16x32_bf16 v[96:99], v[32:35], v[224:227], v[96:99]\n"
      "ds_read_b64_tr_b16 v[56:57], v13 offset:4096\n"
      "ds_read_b64_tr_b16 v[58:59], v13 offset:4608\n"
      "v_mfma_f32_16x16x32_bf16 v[96:99], v[36:39], v[228:231], v[96:99]\n"
      "v_rcp_f32 v175, v175\n"
      "v_mfma_f32_16x16x32_bf16 v[100:103], v[32:35], v[232:235], v[100:103]\n"
      "v_exp_f32 v195, -v195\n"
      "v_mfma_f32_16x16x32_bf16 v[100:103], v[36:39], v[236:239], v[100:103]\n"
      "v_mul_f32 v175, v175, v191\n"
      "v_add_f32 v192, 1.0, v192\n"
      "v_mfma_f32_16x16x32_bf16 v[104:107], v[32:35], v[240:243], v[104:107]\n"
      "ds_read_b64_tr_b16 v[60:61], v13 offset:12288\n"
      "ds_read_b64_tr_b16 v[62:63], v13 offset:12800\n"
      "v_mfma_f32_16x16x32_bf16 v[104:107], v[36:39], v[244:247], v[104:107]\n"
      "v_rcp_f32 v192, v192\n"
      "v_mfma_f32_16x16x32_bf16 v[108:111], v[32:35], v[248:251], v[108:111]\n"
      "v_exp_f32 v196, -v196\n"
      "v_mfma_f32_16x16x32_bf16 v[108:111], v[36:39], v[252:255], v[108:111]\n"
      "v_xor_b32 v11, 0xc000, v9\n"
      "s_waitcnt lgkmcnt(8)\n"
      "v_mfma_f32_16x16x32_bf16 v[112:115], v[40:43], v[224:227], v[112:115]\n"
      "ds_read_b64_tr_b16 v[32:33], v11\n"
      "ds_read_b64_tr_b16 v[34:35], v11 offset:512\n"
      "v_mfma_f32_16x16x32_bf16 v[112:115], v[44:47], v[228:231], v[112:115]\n"
      "v_cvt_pk_bf16_f32 v184, v168, v169\n"
      "v_cvt_pk_bf16_f32 v186, v172, v173\n"
      "v_mfma_f32_16x16x32_bf16 v[116:119], v[40:43], v[232:235], v[116:119]\n"
      "v_exp_f32 v197, -v197\n"
      "v_mfma_f32_16x16x32_bf16 v[116:119], v[44:47], v[236:239], v[116:119]\n"
      "v_cvt_pk_bf16_f32 v185, v170, v171\n"
      "v_cvt_pk_bf16_f32 v187, v174, v175\n"
      "v_mfma_f32_16x16x32_bf16 v[120:123], v[40:43], v[240:243], v[120:123]\n"
      "ds_read_b64_tr_b16 v[36:37], v11 offset:8192\n"
      "ds_read_b64_tr_b16 v[38:39], v11 offset:8704\n"
      "v_mfma_f32_16x16x32_bf16 v[120:123], v[44:47], v[244:247], v[120:123]\n"
      "v_exp_f32 v198, -v198\n"
      "v_mfma_f32_16x16x32_bf16 v[124:127], v[40:43], v[248:251], v[124:127]\n"
      "v_permlane16_swap_b32 v184, v186\n"
      "v_permlane16_swap_b32 v185, v187\n"
      "v_mfma_f32_16x16x32_bf16 v[124:127], v[44:47], v[252:255], v[124:127]\n"
      "s_waitcnt lgkmcnt(8)\n"
      "v_add_f32 v193, 1.0, v193\n"
      "v_mfma_f32_16x16x32_bf16 v[128:131], v[48:51], a[0:3], 0\n"
      "ds_read_b64_tr_b16 v[40:41], v11 offset:4096\n"
      "ds_read_b64_tr_b16 v[42:43], v11 offset:4608\n"
      "v_mfma_f32_16x16x32_bf16 v[128:131], v[52:55], a[4:7], v[128:131]\n"
      "s_add_u32 s95, s35, 311296\n"
      "buffer_store_dwordx4 v[184:187], v72, s[36:39], s95 offen sc1\n"
      "v_mfma_f32_16x16x32_bf16 v[132:135], v[48:51], a[8:11], 0\n"
      "v_rcp_f32 v193, v193\n"
      "v_mfma_f32_16x16x32_bf16 v[132:135], v[52:55], a[12:15], v[132:135]\n"
      "v_exp_f32 v199, -v199\n"
      "v_mfma_f32_16x16x32_bf16 v[136:139], v[48:51], a[16:19], 0\n"
      "ds_read_b64_tr_b16 v[44:45], v11 offset:12288\n"
      "ds_read_b64_tr_b16 v[46:47], v11 offset:12800\n"
      "v_mfma_f32_16x16x32_bf16 v[136:139], v[52:55], a[20:23], v[136:139]\n"
      "v_add_f32 v194, 1.0, v194\n"
      "v_mul_f32 v192, v192, v208\n"
      "v_mfma_f32_16x16x32_bf16 v[140:143], v[48:51], a[24:27], 0\n"
      "v_rcp_f32 v194, v194\n"
      "v_mfma_f32_16x16x32_bf16 v[140:143], v[52:55], a[28:31], v[140:143]\n"
      "v_xor_b32 v13, 0x8020, v9\n"
      "s_waitcnt lgkmcnt(8)\n"
      "v_mfma_f32_16x16x32_bf16 v[144:147], v[56:59], a[0:3], 0\n"
      "s_barrier\n"
      "ds_read_b64_tr_b16 v[48:49], v13\n"
      "ds_read_b64_tr_b16 v[50:51], v13 offset:512\n"
      "v_mfma_f32_16x16x32_bf16 v[144:147], v[60:63], a[4:7], v[144:147]\n"
      "s_add_u32 m0, s81, 147456\n"
      "s_add_u32 s96, s10, 4129280\n"
      "v_mfma_f32_16x16x32_bf16 v[148:151], v[56:59], a[8:11], 0\n"
      "buffer_load_dwordx4 v4, s[52:55], s96 offen lds\n"
      "v_add_f32 v195, 1.0, v195\n"
      "v_mfma_f32_16x16x32_bf16 v[148:151], v[60:63], a[12:15], v[148:151]\n"
      "v_rcp_f32 v195, v195\n"
      "v_mfma_f32_16x16x32_bf16 v[152:155], v[56:59], a[16:19], 0\n"
      "ds_read_b64_tr_b16 v[52:53], v13 offset:8192\n"
      "ds_read_b64_tr_b16 v[54:55], v13 offset:8704\n"
      "v_mfma_f32_16x16x32_bf16 v[152:155], v[60:63], a[20:23], v[152:155]\n"
      "buffer_load_dwordx4 v5, s[52:55], s96 offen offset:1024 lds\n"
      "v_mul_f32 v193, v193, v209\n"
      "v_mfma_f32_16x16x32_bf16 v[156:159], v[56:59], a[24:27], 0\n"
      "v_add_f32 v196, 1.0, v196\n"
      "v_mul_f32 v194, v194, v210\n"
      "v_mfma_f32_16x16x32_bf16 v[156:159], v[60:63], a[28:31], v[156:159]\n"
      "s_waitcnt lgkmcnt(8)\n"
      "v_add_f32 v197, 1.0, v197\n"
      "v_mfma_f32_16x16x32_bf16 v[128:131], v[32:35], a[32:35], v[128:131]\n"
      "ds_read_b64_tr_b16 v[56:57], v13 offset:4096\n"
      "ds_read_b64_tr_b16 v[58:59], v13 offset:4608\n"
      "v_mfma_f32_16x16x32_bf16 v[128:131], v[36:39], a[36:39], v[128:131]\n"
      "s_waitcnt vmcnt(4)\n"
      "buffer_store_dwordx2 v[74:75], v76, s[36:39], s58 offen sc1\n"
      "v_mfma_f32_16x16x32_bf16 v[132:135], v[32:35], a[40:43], v[132:135]\n"
      "v_rcp_f32 v196, v196\n"
      "v_mfma_f32_16x16x32_bf16 v[132:135], v[36:39], a[44:47], v[132:135]\n"
      "buffer_load_dwordx4 v6, s[52:55], s96 offen offset:2048 lds\n"
      "v_mul_f32 v195, v195, v211\n"
      "v_mfma_f32_16x16x32_bf16 v[136:139], v[32:35], a[48:51], v[136:139]\n"
      "ds_read_b64_tr_b16 v[60:61], v13 offset:12288\n"
      "ds_read_b64_tr_b16 v[62:63], v13 offset:12800\n"
      "v_mfma_f32_16x16x32_bf16 v[136:139], v[36:39], a[52:55], v[136:139]\n"
      "v_rcp_f32 v197, v197\n"
      "v_mfma_f32_16x16x32_bf16 v[140:143], v[32:35], a[56:59], v[140:143]\n"
      "v_add_f32 v198, 1.0, v198\n"
      "v_mul_f32 v196, v196, v212\n"
      "v_mfma_f32_16x16x32_bf16 v[140:143], v[36:39], a[60:63], v[140:143]\n"
      "v_xor_b32 v11, 0xc020, v9\n"
      "s_waitcnt lgkmcnt(8)\n"
      "v_mfma_f32_16x16x32_bf16 v[144:147], v[40:43], a[32:35], v[144:147]\n"
      "ds_read_b64_tr_b16 v[32:33], v11\n"
      "ds_read_b64_tr_b16 v[34:35], v11 offset:512\n"
      "v_mfma_f32_16x16x32_bf16 v[144:147], v[44:47], a[36:39], v[144:147]\n"
      "buffer_load_dwordx4 v7, s[52:55], s96 offen offset:3072 lds\n"
      "v_add_f32 v199, 1.0, v199\n"
      "v_mfma_f32_16x16x32_bf16 v[148:151], v[40:43], a[40:43], v[148:151]\n"
      "v_rcp_f32 v198, v198\n"
      "v_mfma_f32_16x16x32_bf16 v[148:151], v[44:47], a[44:47], v[148:151]\n"
      "v_rcp_f32 v199, v199\n"
      "v_mfma_f32_16x16x32_bf16 v[152:155], v[40:43], a[48:51], v[152:155]\n"
      "ds_read_b64_tr_b16 v[36:37], v11 offset:8192\n"
      "ds_read_b64_tr_b16 v[38:39], v11 offset:8704\n"
      "v_mfma_f32_16x16x32_bf16 v[152:155], v[44:47], a[52:55], v[152:155]\n"
      "s_add_u32 m0, s81, 0\n"
      "s_add_u32 s96, s10, 4719104\n"
      "v_mfma_f32_16x16x32_bf16 v[156:159], v[40:43], a[56:59], v[156:159]\n"
      "v_exp_f32 v200, -v200\n"
      "v_mfma_f32_16x16x32_bf16 v[156:159], v[44:47], a[60:63], v[156:159]\n"
      "s_waitcnt lgkmcnt(8)\n"
      "buffer_load_dwordx4 v4, s[52:55], s96 offen lds\n"
      "v_mfma_f32_16x16x32_bf16 v[160:163], v[48:51], a[0:3], 0\n"
      "ds_read_b64_tr_b16 v[40:41], v11 offset:4096\n"
      "ds_read_b64_tr_b16 v[42:43], v11 offset:4608\n"
      "v_mfma_f32_16x16x32_bf16 v[160:163], v[52:55], a[4:7], v[160:163]\n"
      "v_exp_f32 v201, -v201\n"
      "v_mfma_f32_16x16x32_bf16 v[164:167], v[48:51], a[8:11], 0\n"
      "v_exp_f32 v202, -v202\n"
      "v_mfma_f32_16x16x32_bf16 v[164:167], v[52:55], a[12:15], v[164:167]\n"
      "v_exp_f32 v203, -v203\n"
      "v_mfma_f32_16x16x32_bf16 v[168:171], v[48:51], a[16:19], 0\n"
      "ds_read_b64_tr_b16 v[44:45], v11 offset:12288\n"
      "ds_read_b64_tr_b16 v[46:47], v11 offset:12800\n"
      "v_mfma_f32_16x16x32_bf16 v[168:171], v[52:55], a[20:23], v[168:171]\n"
      "buffer_load_dwordx4 v5, s[52:55], s96 offen offset:1024 lds\n"
      "v_mul_f32 v197, v197, v213\n"
      "v_mfma_f32_16x16x32_bf16 v[172:175], v[48:51], a[24:27], 0\n"
      "v_mul_f32 v198, v198, v214\n"
      "v_mul_f32 v199, v199, v215\n"
      "v_mfma_f32_16x16x32_bf16 v[172:175], v[52:55], a[28:31], v[172:175]\n"
      "v_xor_b32 v13, 0x10000, v9\n"
      "s_waitcnt lgkmcnt(8)\n"
      "v_mfma_f32_16x16x32_bf16 v[176:179], v[56:59], a[0:3], 0\n"
      "ds_read_b64_tr_b16 v[48:49], v13\n"
      "ds_read_b64_tr_b16 v[50:51], v13 offset:512\n"
      "v_mfma_f32_16x16x32_bf16 v[176:179], v[60:63], a[4:7], v[176:179]\n"
      "v_exp_f32 v204, -v204\n"
      "v_mfma_f32_16x16x32_bf16 v[180:183], v[56:59], a[8:11], 0\n"
      "v_cvt_pk_bf16_f32 v208, v192, v193\n"
      "v_cvt_pk_bf16_f32 v210, v196, v197\n"
      "v_mfma_f32_16x16x32_bf16 v[180:183], v[60:63], a[12:15], v[180:183]\n"
      "buffer_load_dwordx4 v6, s[52:55], s96 offen offset:2048 lds\n"
      "v_cvt_pk_bf16_f32 v209, v194, v195\n"
      "v_mfma_f32_16x16x32_bf16 v[184:187], v[56:59], a[16:19], 0\n"
      "ds_read_b64_tr_b16 v[52:53], v13 offset:8192\n"
      "ds_read_b64_tr_b16 v[54:55], v13 offset:8704\n"
      "v_mfma_f32_16x16x32_bf16 v[184:187], v[60:63], a[20:23], v[184:187]\n"
      "v_exp_f32 v205, -v205\n"
      "v_mfma_f32_16x16x32_bf16 v[188:191], v[56:59], a[24:27], 0\n"
      "v_cvt_pk_bf16_f32 v211, v198, v199\n"
      "v_permlane16_swap_b32 v208, v210\n"
      "v_mfma_f32_16x16x32_bf16 v[188:191], v[60:63], a[28:31], v[188:191]\n"
      "s_waitcnt lgkmcnt(8)\n"
      "v_permlane16_swap_b32 v209, v211\n"
      "v_mfma_f32_16x16x32_bf16 v[160:163], v[32:35], a[32:35], v[160:163]\n"
      "ds_read_b64_tr_b16 v[56:57], v13 offset:4096\n"
      "ds_read_b64_tr_b16 v[58:59], v13 offset:4608\n"
      "v_mfma_f32_16x16x32_bf16 v[160:163], v[36:39], a[36:39], v[160:163]\n"
      "buffer_load_dwordx4 v7, s[52:55], s96 offen offset:3072 lds\n"
      "v_add_f32 v200, 1.0, v200\n"
      "v_mfma_f32_16x16x32_bf16 v[164:167], v[32:35], a[40:43], v[164:167]\n"
      "s_add_u32 s95, s35, 24576\n"
      "buffer_store_dwordx4 v[208:211], v72, s[36:39], s95 offen sc1\n"
      "v_mfma_f32_16x16x32_bf16 v[164:167], v[36:39], a[44:47], v[164:167]\n"
      "v_rcp_f32 v200, v200\n"
      "v_mfma_f32_16x16x32_bf16 v[168:171], v[32:35], a[48:51], v[168:171]\n"
      "ds_read_b64_tr_b16 v[60:61], v13 offset:12288\n"
      "ds_read_b64_tr_b16 v[62:63], v13 offset:12800\n"
      "v_mfma_f32_16x16x32_bf16 v[168:171], v[36:39], a[52:55], v[168:171]\n"
      "s_add_u32 m0, s81, 16384\n"
      "s_add_u32 s96, s89, 0\n"
      "v_mfma_f32_16x16x32_bf16 v[172:175], v[32:35], a[56:59], v[172:175]\n"
      "v_exp_f32 v206, -v206\n"
      "v_mfma_f32_16x16x32_bf16 v[172:175], v[36:39], a[60:63], v[172:175]\n"
      "v_xor_b32 v11, 0x10020, v9\n"
      "s_waitcnt lgkmcnt(8)\n"
      "v_mfma_f32_16x16x32_bf16 v[176:179], v[40:43], a[32:35], v[176:179]\n"
      "ds_read_b64_tr_b16 v[32:33], v11\n"
      "ds_read_b64_tr_b16 v[34:35], v11 offset:512\n"
      "v_mfma_f32_16x16x32_bf16 v[176:179], v[44:47], a[36:39], v[176:179]\n"
      "buffer_load_dwordx4 v1, s[36:39], s96 offen sc1 lds\n"
      "v_add_f32 v201, 1.0, v201\n"
      "v_mfma_f32_16x16x32_bf16 v[180:183], v[40:43], a[40:43], v[180:183]\n"
      "v_rcp_f32 v201, v201\n"
      "v_mfma_f32_16x16x32_bf16 v[180:183], v[44:47], a[44:47], v[180:183]\n"
      "v_exp_f32 v207, -v207\n"
      "v_mfma_f32_16x16x32_bf16 v[184:187], v[40:43], a[48:51], v[184:187]\n"
      "ds_read_b64_tr_b16 v[36:37], v11 offset:8192\n"
      "ds_read_b64_tr_b16 v[38:39], v11 offset:8704\n"
      "v_mfma_f32_16x16x32_bf16 v[184:187], v[44:47], a[52:55], v[184:187]\n"
      "buffer_load_dwordx4 v1, s[36:39], s96 offen offset:1024 sc1 lds\n"
      "v_add_f32 v202, 1.0, v202\n"
      "v_mfma_f32_16x16x32_bf16 v[188:191], v[40:43], a[56:59], v[188:191]\n"
      "v_rcp_f32 v202, v202\n"
      "v_mfma_f32_16x16x32_bf16 v[188:191], v[44:47], a[60:63], v[188:191]\n"
      "s_waitcnt lgkmcnt(8)\n"
      "v_mul_f32 v200, v200, v216\n"
      "v_mfma_f32_16x16x32_bf16 v[128:131], v[48:51], a[64:67], v[128:131]\n"
      "ds_read_b64_tr_b16 v[40:41], v11 offset:4096\n"
      "ds_read_b64_tr_b16 v[42:43], v11 offset:4608\n"
      "v_mfma_f32_16x16x32_bf16 v[128:131], v[52:55], a[68:71], v[128:131]\n"
      "v_add_f32 v203, 1.0, v203\n"
      "v_mul_f32 v201, v201, v217\n"
      "v_mfma_f32_16x16x32_bf16 v[132:135], v[48:51], a[72:75], v[132:135]\n"
      "v_rcp_f32 v203, v203\n"
      "v_mfma_f32_16x16x32_bf16 v[132:135], v[52:55], a[76:79], v[132:135]\n"
      "buffer_load_dwordx4 v1, s[36:39], s96 offen offset:2048 sc1 lds\n"
      "v_add_f32 v204, 1.0, v204\n"
      "Lpad9_%=:\n"
      "s_cbranch_execz Lpad10_%=\n"
      "v_mfma_f32_16x16x32_bf16 v[136:139], v[48:51], a[80:83], v[136:139]\n"
      "ds_read_b64_tr_b16 v[44:45], v11 offset:12288\n"
      "ds_read_b64_tr_b16 v[46:47], v11 offset:12800\n"
      "v_mfma_f32_16x16x32_bf16 v[136:139], v[52:55], a[84:87], v[136:139]\n"
      "v_rcp_f32 v204, v204\n"
      "v_mfma_f32_16x16x32_bf16 v[140:143], v[48:51], a[88:91], v[140:143]\n"
      "v_mul_f32 v202, v202, v218\n"
      "v_add_f32 v205, 1.0, v205\n"
      "v_mfma_f32_16x16x32_bf16 v[140:143], v[52:55], a[92:95], v[140:143]\n"
      "v_xor_b32 v13, 0x14000, v9\n"
      "s_waitcnt lgkmcnt(8)\n"
      "v_mfma_f32_16x16x32_bf16 v[144:147], v[56:59], a[64:67], v[144:147]\n"
      "ds_read_b64_tr_b16 v[48:49], v13\n"
      "ds_read_b64_tr_b16 v[50:51], v13 offset:512\n"
      "v_mfma_f32_16x16x32_bf16 v[144:147], v[60:63], a[68:71], v[144:147]\n"
      "buffer_load_dwordx4 v1, s[36:39], s96 offen offset:3072 sc1 lds\n"
      "v_mul_f32 v203, v203, v219\n"
      "v_mfma_f32_16x16x32_bf16 v[148:151], v[56:59], a[72:75], v[148:151]\n"
      "v_rcp_f32 v205, v205\n"
      "v_mfma_f32_16x16x32_bf16 v[148:151], v[60:63], a[76:79], v[148:151]\n"
      "v_add_f32 v206, 1.0, v206\n"
      "v_mul_f32 v204, v204, v220\n"
      "v_mfma_f32_16x16x32_bf16 v[152:155], v[56:59], a[80:83], v[152:155]\n"
      "ds_read_b64_tr_b16 v[52:53], v13 offset:8192\n"
      "ds_read_b64_tr_b16 v[54:55], v13 offset:8704\n"
      "v_mfma_f32_16x16x32_bf16 v[152:155], v[60:63], a[84:87], v[152:155]\n"
      "v_rcp_f32 v206, v206\n"
      "v_mfma_f32_16x16x32_bf16 v[156:159], v[56:59], a[88:91], v[156:159]\n"
      "v_exp_f32 v96, -v96\n"
      "v_mfma_f32_16x16x32_bf16 v[156:159], v[60:63], a[92:95], v[156:159]\n"
      "s_waitcnt lgkmcnt(8)\n"
      "v_add_f32 v207, 1.0, v207\n"
      "v_mfma_f32_16x16x32_bf16 v[160:163], v[32:35], a[64:67], v[160:163]\n"
      "ds_read_b64_tr_b16 v[56:57], v13 offset:4096\n"
      "ds_read_b64_tr_b16 v[58:59], v13 offset:4608\n"
      "v_mfma_f32_16x16x32_bf16 v[160:163], v[36:39], a[68:71], v[160:163]\n"
      "v_rcp_f32 v207, v207\n"
      "v_mfma_f32_16x16x32_bf16 v[164:167], v[32:35], a[72:75], v[164:167]\n"
      "v_exp_f32 v97, -v97\n"
      "v_mfma_f32_16x16x32_bf16 v[164:167], v[36:39], a[76:79], v[164:167]\n"
      "v_exp_f32 v98, -v98\n"
      "v_mfma_f32_16x16x32_bf16 v[168:171], v[32:35], a[80:83], v[168:171]\n"
      "ds_read_b64_tr_b16 v[60:61], v13 offset:12288\n"
      "ds_read_b64_tr_b16 v[62:63], v13 offset:12800\n"
      "v_mfma_f32_16x16x32_bf16 v[168:171], v[36:39], a[84:87], v[168:171]\n"
      "v_exp_f32 v99, -v99\n"
      "v_mfma_f32_16x16x32_bf16 v[172:175], v[32:35], a[88:91], v[172:175]\n"
      "v_mul_f32 v205, v205, v221\n"
      "v_mul_f32 v206, v206, v222\n"
      "v_mfma_f32_16x16x32_bf16 v[172:175], v[36:39], a[92:95], v[172:175]\n"
      "v_xor_b32 v11, 0x8040, v9\n"
      "s_waitcnt lgkmcnt(8)\n"
      "v_mfma_f32_16x16x32_bf16 v[176:179], v[40:43], a[64:67], v[176:179]\n"
      "ds_read_b64_tr_b16 v[32:33], v11\n"
      "ds_read_b64_tr_b16 v[34:35], v11 offset:512\n"
      "v_mfma_f32_16x16x32_bf16 v[176:179], v[44:47], a[68:71], v[176:179]\n"
      "v_mul_f32 v207, v207, v223\n"
      "v_add_f32 v96, 1.0, v96\n"
      "v_mfma_f32_16x16x32_bf16 v[180:183], v[40:43], a[72:75], v[180:183]\n"
      "v_rcp_f32 v96, v96\n"
      "v_mfma_f32_16x16x32_bf16 v[180:183], v[44:47], a[76:79], v[180:183]\n"
      "v_exp_f32 v100, -v100\n"
      "v_mfma_f32_16x16x32_bf16 v[184:187], v[40:43], a[80:83], v[184:187]\n"
      "ds_read_b64_tr_b16 v[36:37], v11 offset:8192\n"
      "ds_read_b64_tr_b16 v[38:39], v11 offset:8704\n"
      "v_mfma_f32_16x16x32_bf16 v[184:187], v[44:47], a[84:87], v[184:187]\n"
      "v_cvt_pk_bf16_f32 v216, v200, v201\n"
      "v_cvt_pk_bf16_f32 v218, v204, v205\n"
      "v_mfma_f32_16x16x32_bf16 v[188:191], v[40:43], a[88:91], v[188:191]\n"
      "v_exp_f32 v101, -v101\n"
      "v_mfma_f32_16x16x32_bf16 v[188:191], v[44:47], a[92:95], v[188:191]\n"
      "s_waitcnt lgkmcnt(8)\n"
      "v_cvt_pk_bf16_f32 v217, v202, v203\n"
      "v_mfma_f32_16x16x32_bf16 v[128:131], v[48:51], a[96:99], v[128:131]\n"
      "ds_read_b64_tr_b16 v[40:41], v11 offset:4096\n"
      "ds_read_b64_tr_b16 v[42:43], v11 offset:4608\n"
      "v_mfma_f32_16x16x32_bf16 v[128:131], v[52:55], a[100:103], v[128:131]\n"
      "v_cvt_pk_bf16_f32 v219, v206, v207\n"
      "v_permlane16_swap_b32 v216, v218\n"
      "v_mfma_f32_16x16x32_bf16 v[132:135], v[48:51], a[104:107], v[132:135]\n"
      "v_exp_f32 v102, -v102\n"
      "v_mfma_f32_16x16x32_bf16 v[132:135], v[52:55], a[108:111], v[132:135]\n"
      "v_permlane16_swap_b32 v217, v219\n"
      "v_add_f32 v97, 1.0, v97\n"
      "v_mfma_f32_16x16x32_bf16 v[136:139], v[48:51], a[112:115], v[136:139]\n"
      "ds_read_b64_tr_b16 v[44:45], v11 offset:12288\n"
      "ds_read_b64_tr_b16 v[46:47], v11 offset:12800\n"
      "v_mfma_f32_16x16x32_bf16 v[136:139], v[52:55], a[116:119], v[136:139]\n"
      "s_add_u32 s95, s35, 319488\n"
      "buffer_store_dwordx4 v[216:219], v72, s[36:39], s95 offen sc1\n"
      "v_mfma_f32_16x16x32_bf16 v[140:143], v[48:51], a[120:123], v[140:143]\n"
      "v_rcp_f32 v97, v97\n"
      "v_mfma_f32_16x16x32_bf16 v[140:143], v[52:55], a[124:127], v[140:143]\n"
      "v_xor_b32 v13, 0xc040, v9\n"
      "s_waitcnt lgkmcnt(8)\n"
      "v_mfma_f32_16x16x32_bf16 v[144:147], v[56:59], a[96:99], v[144:147]\n"
      "ds_read_b64_tr_b16 v[48:49], v13\n"
      "ds_read_b64_tr_b16 v[50:51], v13 offset:512\n"
      "v_mfma_f32_16x16x32_bf16 v[144:147], v[60:63], a[100:103], v[144:147]\n"
      "v_exp_f32 v103, -v103\n"
      "v_mfma_f32_16x16x32_bf16 v[148:151], v[56:59], a[104:107], v[148:151]\n"
      "v_add_f32 v98, 1.0, v98\n"
      "v_mul_f32 v96, v96, v112\n"
      "v_mfma_f32_16x16x32_bf16 v[148:151], v[60:63], a[108:111], v[148:151]\n"
      "v_rcp_f32 v98, v98\n"
      "v_mfma_f32_16x16x32_bf16 v[152:155], v[56:59], a[112:115], v[152:155]\n"
      "ds_read_b64_tr_b16 v[52:53], v13 offset:8192\n"
      "ds_read_b64_tr_b16 v[54:55], v13 offset:8704\n"
      "v_mfma_f32_16x16x32_bf16 v[152:155], v[60:63], a[116:119], v[152:155]\n"
      "v_add_f32 v99, 1.0, v99\n"
      "v_mul_f32 v97, v97, v113\n"
      "v_mfma_f32_16x16x32_bf16 v[156:159], v[56:59], a[120:123], v[156:159]\n"
      "v_rcp_f32 v99, v99\n"
      "v_mfma_f32_16x16x32_bf16 v[156:159], v[60:63], a[124:127], v[156:159]\n"
      "s_waitcnt lgkmcnt(8)\n"
      "v_add_f32 v100, 1.0, v100\n"
      "v_mfma_f32_16x16x32_bf16 v[192:195], v[32:35], a[0:3], 0\n"
      "ds_read_b64_tr_b16 v[56:57], v13 offset:4096\n"
      "ds_read_b64_tr_b16 v[58:59], v13 offset:4608\n"
      "v_mfma_f32_16x16x32_bf16 v[192:195], v[36:39], a[4:7], v[192:195]\n"
      "v_rcp_f32 v100, v100\n"
      "v_mfma_f32_16x16x32_bf16 v[196:199], v[32:35], a[8:11], 0\n"
      "v_mul_f32 v98, v98, v114\n"
      "v_add_f32 v101, 1.0, v101\n"
      "v_mfma_f32_16x16x32_bf16 v[196:199], v[36:39], a[12:15], v[196:199]\n"
      "v_rcp_f32 v101, v101\n"
      "v_mfma_f32_16x16x32_bf16 v[200:203], v[32:35], a[16:19], 0\n"
      "ds_read_b64_tr_b16 v[60:61], v13 offset:12288\n"
      "ds_read_b64_tr_b16 v[62:63], v13 offset:12800\n"
      "v_mfma_f32_16x16x32_bf16 v[200:203], v[36:39], a[20:23], v[200:203]\n"
      "v_mul_f32 v99, v99, v115\n"
      "v_add_f32 v102, 1.0, v102\n"
      "v_mfma_f32_16x16x32_bf16 v[204:207], v[32:35], a[24:27], 0\n"
      "v_rcp_f32 v102, v102\n"
      "v_mfma_f32_16x16x32_bf16 v[204:207], v[36:39], a[28:31], v[204:207]\n"
      "v_xor_b32 v11, 0x10040, v9\n"
      "s_waitcnt lgkmcnt(8)\n"
      "v_mfma_f32_16x16x32_bf16 v[208:211], v[40:43], a[0:3], 0\n"
      "s_waitcnt vmcnt(6)\n"
      "s_barrier\n"
      "ds_read_b64_tr_b16 v[32:33], v11\n"
      "ds_read_b64_tr_b16 v[34:35], v11 offset:512\n"
      "v_mfma_f32_16x16x32_bf16 v[208:211], v[44:47], a[4:7], v[208:211]\n"
      "v_exp_f32 v104, -v104\n"
      "v_mfma_f32_16x16x32_bf16 v[212:215], v[40:43], a[8:11], 0\n"
      "v_exp_f32 v105, -v105\n"
      "v_mfma_f32_16x16x32_bf16 v[212:215], v[44:47], a[12:15], v[212:215]\n"
      "v_mul_f32 v100, v100, v116\n"
      "v_add_f32 v103, 1.0, v103\n"
      "v_mfma_f32_16x16x32_bf16 v[216:219], v[40:43], a[16:19], 0\n"
      "ds_read_b64_tr_b16 v[36:37], v11 offset:8192\n"
      "ds_read_b64_tr_b16 v[38:39], v11 offset:8704\n"
      "v_mfma_f32_16x16x32_bf16 v[216:219], v[44:47], a[20:23], v[216:219]\n"
      "v_rcp_f32 v103, v103\n"
      "v_mfma_f32_16x16x32_bf16 v[220:223], v[40:43], a[24:27], 0\n"
      "v_exp_f32 v106, -v106\n"
      "v_mfma_f32_16x16x32_bf16 v[220:223], v[44:47], a[28:31], v[220:223]\n"
      "s_waitcnt lgkmcnt(8)\n"
      "v_mul_f32 v101, v101, v117\n"
      "v_mfma_f32_16x16x32_bf16 v[192:195], v[48:51], a[32:35], v[192:195]\n"
      "ds_read_b64_tr_b16 v[40:41], v11 offset:4096\n"
      "ds_read_b64_tr_b16 v[42:43], v11 offset:4608\n"
      "v_mfma_f32_16x16x32_bf16 v[192:195], v[52:55], a[36:39], v[192:195]\n"
      "v_exp_f32 v107, -v107\n"
      "v_mfma_f32_16x16x32_bf16 v[196:199], v[48:51], a[40:43], v[196:199]\n"
      "v_mul_f32 v102, v102, v118\n"
      "v_mul_f32 v103, v103, v119\n"
      "v_mfma_f32_16x16x32_bf16 v[196:199], v[52:55], a[44:47], v[196:199]\n"
      "v_exp_f32 v108, -v108\n"
      "v_mfma_f32_16x16x32_bf16 v[200:203], v[48:51], a[48:51], v[200:203]\n"
      "ds_read_b64_tr_b16 v[44:45], v11 offset:12288\n"
      "ds_read_b64_tr_b16 v[46:47], v11 offset:12800\n"
      "v_mfma_f32_16x16x32_bf16 v[200:203], v[52:55], a[52:55], v[200:203]\n"
      "v_cvt_pk_bf16_f32 v112, v96, v97\n"
      "v_cvt_pk_bf16_f32 v114, v100, v101\n"
      "v_mfma_f32_16x16x32_bf16 v[204:207], v[48:51], a[56:59], v[204:207]\n"
      "v_cvt_pk_bf16_f32 v113, v98, v99\n"
      "v_cvt_pk_bf16_f32 v115, v102, v103\n"
      "v_mfma_f32_16x16x32_bf16 v[204:207], v[52:55], a[60:63], v[204:207]\n"
      "v_xor_b32 v13, 0x18000, v9\n"
      "s_waitcnt lgkmcnt(8)\n"
      "v_mfma_f32_16x16x32_bf16 v[208:211], v[56:59], a[32:35], v[208:211]\n"
      "ds_read_b64_tr_b16 v[48:49], v13\n"
      "ds_read_b64_tr_b16 v[50:51], v13 offset:512\n"
      "v_mfma_f32_16x16x32_bf16 v[208:211], v[60:63], a[36:39], v[208:211]\n"
      "v_exp_f32 v109, -v109\n"
      "v_mfma_f32_16x16x32_bf16 v[212:215], v[56:59], a[40:43], v[212:215]\n"
      "v_permlane16_swap_b32 v112, v114\n"
      "v_permlane16_swap_b32 v113, v115\n"
      "v_mfma_f32_16x16x32_bf16 v[212:215], v[60:63], a[44:47], v[212:215]\n"
      "s_add_u32 s95, s35, 32768\n"
      "buffer_store_dwordx4 v[112:115], v72, s[36:39], s95 offen sc1\n"
      "v_mfma_f32_16x16x32_bf16 v[216:219], v[56:59], a[48:51], v[216:219]\n"
      "ds_read_b64_tr_b16 v[52:53], v13 offset:8192\n"
      "ds_read_b64_tr_b16 v[54:55], v13 offset:8704\n"
      "v_mfma_f32_16x16x32_bf16 v[216:219], v[60:63], a[52:55], v[216:219]\n"
      "v_exp_f32 v110, -v110\n"
      "v_mfma_f32_16x16x32_bf16 v[220:223], v[56:59], a[56:59], v[220:223]\n"
      "v_add_f32 v104, 1.0, v104\n"
      "v_add_f32 v105, 1.0, v105\n"
      "v_mfma_f32_16x16x32_bf16 v[220:223], v[60:63], a[60:63], v[220:223]\n"
      "s_waitcnt lgkmcnt(8)\n"
      "v_add_f32 v106, 1.0, v106\n"
      "v_mfma_f32_16x16x32_bf16 v[192:195], v[32:35], a[64:67], v[192:195]\n"
      "ds_read_b64_tr_b16 v[56:57], v13 offset:4096\n"
      "ds_read_b64_tr_b16 v[58:59], v13 offset:4608\n"
      "v_mfma_f32_16x16x32_bf16 v[192:195], v[36:39], a[68:71], v[192:195]\n"
      "buffer_load_dwordx2 v[70:71], v23, s[36:39], 0 offen sc1\n"
      "v_add_f32 v107, 1.0, v107\n"
      "v_mfma_f32_16x16x32_bf16 v[196:199], v[32:35], a[72:75], v[196:199]\n"
      "v_rcp_f32 v104, v104\n"
      "v_mfma_f32_16x16x32_bf16 v[196:199], v[36:39], a[76:79], v[196:199]\n"
      "v_rcp_f32 v105, v105\n"
      "v_mfma_f32_16x16x32_bf16 v[200:203], v[32:35], a[80:83], v[200:203]\n"
      "ds_read_b64_tr_b16 v[60:61], v13 offset:12288\n"
      "ds_read_b64_tr_b16 v[62:63], v13 offset:12800\n"
      "v_mfma_f32_16x16x32_bf16 v[200:203], v[36:39], a[84:87], v[200:203]\n"
      "v_rcp_f32 v106, v106\n"
      "v_mfma_f32_16x16x32_bf16 v[204:207], v[32:35], a[88:91], v[204:207]\n"
      "v_rcp_f32 v107, v107\n"
      "v_mfma_f32_16x16x32_bf16 v[204:207], v[36:39], a[92:95], v[204:207]\n"
      "v_xor_b32 v11, 0x1c000, v9\n"
      "s_waitcnt lgkmcnt(8)\n"
      "v_mfma_f32_16x16x32_bf16 v[208:211], v[40:43], a[64:67], v[208:211]\n"
      "ds_read_b64_tr_b16 v[32:33], v11\n"
      "ds_read_b64_tr_b16 v[34:35], v11 offset:512\n"
      "v_mfma_f32_16x16x32_bf16 v[208:211], v[44:47], a[68:71], v[208:211]\n"
      "v_exp_f32 v111, -v111\n"
      "v_mfma_f32_16x16x32_bf16 v[212:215], v[40:43], a[72:75], v[212:215]\n"
      "v_mul_f32 v104, v104, v120\n"
      "v_mul_f32 v105, v105, v121\n"
      "v_mfma_f32_16x16x32_bf16 v[212:215], v[44:47], a[76:79], v[212:215]\n"
      "v_add_f32 v108, 1.0, v108\n"
      "v_mul_f32 v106, v106, v122\n"
      "v_mfma_f32_16x16x32_bf16 v[216:219], v[40:43], a[80:83], v[216:219]\n"
      "ds_read_b64_tr_b16 v[36:37], v11 offset:8192\n"
      "ds_read_b64_tr_b16 v[38:39], v11 offset:8704\n"
      "v_mfma_f32_16x16x32_bf16 v[216:219], v[44:47], a[84:87], v[216:219]\n"
      "v_rcp_f32 v108, v108\n"
      "v_mfma_f32_16x16x32_bf16 v[220:223], v[40:43], a[88:91], v[220:223]\n"
      "v_add_f32 v109, 1.0, v109\n"
      "v_mul_f32 v107, v107, v123\n"
      "v_mfma_f32_16x16x32_bf16 v[220:223], v[44:47], a[92:95], v[220:223]\n"
      "s_waitcnt lgkmcnt(8)\n"
      "v_add_f32 v110, 1.0, v110\n"
      "v_mfma_f32_16x16x32_bf16 v[128:131], v[48:51], a[128:131], v[128:131]\n"
      "ds_read_b64_tr_b16 v[40:41], v11 offset:4096\n"
      "ds_read_b64_tr_b16 v[42:43], v11 offset:4608\n"
      "v_mfma_f32_16x16x32_bf16 v[128:131], v[52:55], a[132:135], v[128:131]\n"
      "v_rcp_f32 v109, v109\n"
      "v_mfma_f32_16x16x32_bf16 v[132:135], v[48:51], a[136:139], v[132:135]\n"
      "v_rcp_f32 v110, v110\n"
      "v_mfma_f32_16x16x32_bf16 v[132:135], v[52:55], a[140:143], v[132:135]\n"
      "v_mul_f32 v108, v108, v124\n"
      "v_add_f32 v111, 1.0, v111\n"
      "v_mfma_f32_16x16x32_bf16 v[136:139], v[48:51], a[144:147], v[136:139]\n"
      "ds_read_b64_tr_b16 v[44:45], v11 offset:12288\n"
      "ds_read_b64_tr_b16 v[46:47], v11 offset:12800\n"
      "v_mfma_f32_16x16x32_bf16 v[136:139], v[52:55], a[148:151], v[136:139]\n"
      "v_rcp_f32 v111, v111\n"
      "v_mfma_f32_16x16x32_bf16 v[140:143], v[48:51], a[152:155], v[140:143]\n"
      "v_mul_f32 v109, v109, v125\n"
      "v_mul_f32 v110, v110, v126\n"
      "v_mfma_f32_16x16x32_bf16 v[140:143], v[52:55], a[156:159], v[140:143]\n"
      "v_xor_b32 v13, 0x20000, v9\n"
      "s_waitcnt lgkmcnt(8)\n"
      "v_mfma_f32_16x16x32_bf16 v[144:147], v[56:59], a[128:131], v[144:147]\n"
      "ds_read_b64_tr_b16 v[48:49], v13\n"
      "ds_read_b64_tr_b16 v[50:51], v13 offset:512\n"
      "v_mfma_f32_16x16x32_bf16 v[144:147], v[60:63], a[132:135], v[144:147]\n"
      "v_mul_f32 v111, v111, v127\n"
      "v_mfma_f32_16x16x32_bf16 v[148:151], v[56:59], a[136:139], v[148:151]\n"
      "v_mfma_f32_16x16x32_bf16 v[148:151], v[60:63], a[140:143], v[148:151]\n"
      "v_cvt_pk_bf16_f32 v120, v104, v105\n"
      "v_cvt_pk_bf16_f32 v122, v108, v109\n"
      "v_mfma_f32_16x16x32_bf16 v[152:155], v[56:59], a[144:147], v[152:155]\n"
      "ds_read_b64_tr_b16 v[52:53], v13 offset:8192\n"
      "ds_read_b64_tr_b16 v[54:55], v13 offset:8704\n"
      "v_mfma_f32_16x16x32_bf16 v[152:155], v[60:63], a[148:151], v[152:155]\n"
      "v_cvt_pk_bf16_f32 v121, v106, v107\n"
      "v_cvt_pk_bf16_f32 v123, v110, v111\n"
      "v_mfma_f32_16x16x32_bf16 v[156:159], v[56:59], a[152:155], v[156:159]\n"
      "v_permlane16_swap_b32 v120, v122\n"
      "v_permlane16_swap_b32 v121, v123\n"
      "v_mfma_f32_16x16x32_bf16 v[156:159], v[60:63], a[156:159], v[156:159]\n"
      "s_waitcnt lgkmcnt(8)\n"
      "v_mfma_f32_16x16x32_bf16 v[128:131], v[32:35], a[160:163], v[128:131]\n"
      "ds_read_b64_tr_b16 v[56:57], v13 offset:4096\n"
      "ds_read_b64_tr_b16 v[58:59], v13 offset:4608\n"
      "v_mfma_f32_16x16x32_bf16 v[128:131], v[36:39], a[164:167], v[128:131]\n"
      "s_add_u32 s95, s35, 327680\n"
      "buffer_store_dwordx4 v[120:123], v72, s[36:39], s95 offen sc1\n"
      "v_mfma_f32_16x16x32_bf16 v[132:135], v[32:35], a[168:171], v[132:135]\n"
      "v_mfma_f32_16x16x32_bf16 v[132:135], v[36:39], a[172:175], v[132:135]\n"
      "v_mfma_f32_16x16x32_bf16 v[136:139], v[32:35], a[176:179], v[136:139]\n"
      "ds_read_b64_tr_b16 v[60:61], v13 offset:12288\n"
      "ds_read_b64_tr_b16 v[62:63], v13 offset:12800\n"
      "v_mfma_f32_16x16x32_bf16 v[136:139], v[36:39], a[180:183], v[136:139]\n"
      "v_mfma_f32_16x16x32_bf16 v[140:143], v[32:35], a[184:187], v[140:143]\n"
      "v_mfma_f32_16x16x32_bf16 v[140:143], v[36:39], a[188:191], v[140:143]\n"
      "v_xor_b32 v11, 0x8060, v9\n"
      "s_waitcnt lgkmcnt(8)\n"
      "v_mfma_f32_16x16x32_bf16 v[144:147], v[40:43], a[160:163], v[144:147]\n"
      "ds_read_b64_tr_b16 v[32:33], v11\n"
      "ds_read_b64_tr_b16 v[34:35], v11 offset:512\n"
      "v_mfma_f32_16x16x32_bf16 v[144:147], v[44:47], a[164:167], v[144:147]\n"
      "v_mfma_f32_16x16x32_bf16 v[148:151], v[40:43], a[168:171], v[148:151]\n"
      "v_mfma_f32_16x16x32_bf16 v[148:151], v[44:47], a[172:175], v[148:151]\n"
      "v_mfma_f32_16x16x32_bf16 v[152:155], v[40:43], a[176:179], v[152:155]\n"
      "ds_read_b64_tr_b16 v[36:37], v11 offset:8192\n"
      "ds_read_b64_tr_b16 v[38:39], v11 offset:8704\n"
      "v_mfma_f32_16x16x32_bf16 v[152:155], v[44:47], a[180:183], v[152:155]\n"
      "v_mfma_f32_16x16x32_bf16 v[156:159], v[40:43], a[184:187], v[156:159]\n"
      "v_mfma_f32_16x16x32_bf16 v[156:159], v[44:47], a[188:191], v[156:159]\n"
      "s_waitcnt lgkmcnt(8)\n"
      "v_mfma_f32_16x16x32_bf16 v[128:131], v[48:51], a[192:195], v[128:131]\n"
      "ds_read_b64_tr_b16 v[40:41], v11 offset:4096\n"
      "ds_read_b64_tr_b16 v[42:43], v11 offset:4608\n"
      "v_mfma_f32_16x16x32_bf16 v[128:131], v[52:55], a[196:199], v[128:131]\n"
      "s_waitcnt vmcnt(3)\n"
      "buffer_store_dwordx2 v[74:75], v76, s[36:39], s58 offen offset:256 sc1\n"
      "v_mfma_f32_16x16x32_bf16 v[132:135], v[48:51], a[200:203], v[132:135]\n"
      "v_mfma_f32_16x16x32_bf16 v[132:135], v[52:55], a[204:207], v[132:135]\n"
      "v_mfma_f32_16x16x32_bf16 v[136:139], v[48:51], a[208:211], v[136:139]\n"
      "ds_read_b64_tr_b16 v[44:45], v11 offset:12288\n"
      "ds_read_b64_tr_b16 v[46:47], v11 offset:12800\n"
      "v_mfma_f32_16x16x32_bf16 v[136:139], v[52:55], a[212:215], v[136:139]\n"
      "v_mfma_f32_16x16x32_bf16 v[140:143], v[48:51], a[216:219], v[140:143]\n"
      "v_mfma_f32_16x16x32_bf16 v[140:143], v[52:55], a[220:223], v[140:143]\n"
      "v_xor_b32 v13, 0xc060, v9\n"
      "s_waitcnt lgkmcnt(8)\n"
      "v_mfma_f32_16x16x32_bf16 v[144:147], v[56:59], a[192:195], v[144:147]\n"
      "ds_read_b64_tr_b16 v[48:49], v13\n"
      "ds_read_b64_tr_b16 v[50:51], v13 offset:512\n"
      "v_mfma_f32_16x16x32_bf16 v[144:147], v[60:63], a[196:199], v[144:147]\n"
      "v_mfma_f32_16x16x32_bf16 v[148:151], v[56:59], a[200:203], v[148:151]\n"
      "v_mfma_f32_16x16x32_bf16 v[148:151], v[60:63], a[204:207], v[148:151]\n"
      "v_mfma_f32_16x16x32_bf16 v[152:155], v[56:59], a[208:211], v[152:155]\n"
      "ds_read_b64_tr_b16 v[52:53], v13 offset:8192\n"
      "ds_read_b64_tr_b16 v[54:55], v13 offset:8704\n"
      "v_mfma_f32_16x16x32_bf16 v[152:155], v[60:63], a[212:215], v[152:155]\n"
      "v_mfma_f32_16x16x32_bf16 v[156:159], v[56:59], a[216:219], v[156:159]\n"
      "v_mfma_f32_16x16x32_bf16 v[156:159], v[60:63], a[220:223], v[156:159]\n"
      "s_waitcnt lgkmcnt(8)\n"
      "v_mfma_f32_16x16x32_bf16 v[96:99], v[32:35], a[0:3], 0\n"
      "ds_read_b64_tr_b16 v[56:57], v13 offset:4096\n"
      "ds_read_b64_tr_b16 v[58:59], v13 offset:4608\n"
      "v_mfma_f32_16x16x32_bf16 v[96:99], v[36:39], a[4:7], v[96:99]\n"
      "v_mfma_f32_16x16x32_bf16 v[100:103], v[32:35], a[8:11], 0\n"
      "v_mfma_f32_16x16x32_bf16 v[100:103], v[36:39], a[12:15], v[100:103]\n"
      "v_mfma_f32_16x16x32_bf16 v[104:107], v[32:35], a[16:19], 0\n"
      "ds_read_b64_tr_b16 v[60:61], v13 offset:12288\n"
      "ds_read_b64_tr_b16 v[62:63], v13 offset:12800\n"
      "v_mfma_f32_16x16x32_bf16 v[104:107], v[36:39], a[20:23], v[104:107]\n"
      "v_mfma_f32_16x16x32_bf16 v[108:111], v[32:35], a[24:27], 0\n"
      "v_mfma_f32_16x16x32_bf16 v[108:111], v[36:39], a[28:31], v[108:111]\n"
      "v_xor_b32 v11, 0x10060, v9\n"
      "s_waitcnt lgkmcnt(8)\n"
      "v_mfma_f32_16x16x32_bf16 v[112:115], v[40:43], a[0:3], 0\n"
      "ds_read_b64_tr_b16 v[32:33], v11\n"
      "ds_read_b64_tr_b16 v[34:35], v11 offset:512\n"
      "v_mfma_f32_16x16x32_bf16 v[112:115], v[44:47], a[4:7], v[112:115]\n"
      "v_mfma_f32_16x16x32_bf16 v[116:119], v[40:43], a[8:11], 0\n"
      "v_mfma_f32_16x16x32_bf16 v[116:119], v[44:47], a[12:15], v[116:119]\n"
      "v_mfma_f32_16x16x32_bf16 v[120:123], v[40:43], a[16:19], 0\n"
      "ds_read_b64_tr_b16 v[36:37], v11 offset:8192\n"
      "ds_read_b64_tr_b16 v[38:39], v11 offset:8704\n"
      "v_mfma_f32_16x16x32_bf16 v[120:123], v[44:47], a[20:23], v[120:123]\n"
      "v_mfma_f32_16x16x32_bf16 v[124:127], v[40:43], a[24:27], 0\n"
      "v_mfma_f32_16x16x32_bf16 v[124:127], v[44:47], a[28:31], v[124:127]\n"
      "s_waitcnt lgkmcnt(8)\n"
      "v_mfma_f32_16x16x32_bf16 v[96:99], v[48:51], a[32:35], v[96:99]\n"
      "ds_read_b64_tr_b16 v[40:41], v11 offset:4096\n"
      "ds_read_b64_tr_b16 v[42:43], v11 offset:4608\n"
      "v_mfma_f32_16x16x32_bf16 v[96:99], v[52:55], a[36:39], v[96:99]\n"
      "s_waitcnt vmcnt(2)\n"
      "v_cmp_ne_u64 vcc, s[76:77], v[70:71]\n"
      "v_mfma_f32_16x16x32_bf16 v[100:103], v[48:51], a[40:43], v[100:103]\n"
      "s_cbranch_vccnz Lsl_27_%=\n"
      "Lbk_28_%=:\n"
      "s_add_u32 s93, s59, 0\n"
      "v_mfma_f32_16x16x32_bf16 v[100:103], v[52:55], a[44:47], v[100:103]\n"
      "buffer_load_dwordx4 a[0:3], v1, s[36:39], s93 offen sc1\n"
      "v_mfma_f32_16x16x32_bf16 v[104:107], v[48:51], a[48:51], v[104:107]\n"
      "ds_read_b64_tr_b16 v[44:45], v11 offset:12288\n"
      "ds_read_b64_tr_b16 v[46:47], v11 offset:12800\n"
      "v_mfma_f32_16x16x32_bf16 v[104:107], v[52:55], a[52:55], v[104:107]\n"
      "v_mfma_f32_16x16x32_bf16 v[108:111], v[48:51], a[56:59], v[108:111]\n"
      "buffer_load_dwordx4 a[4:7], v1, s[36:39], s93 offen offset:1024 sc1\n"
      "v_mfma_f32_16x16x32_bf16 v[108:111], v[52:55], a[60:63], v[108:111]\n"
      "v_xor_b32 v13, 0x24000, v9\n"
      "s_waitcnt lgkmcnt(8)\n"
      "v_mfma_f32_16x16x32_bf16 v[112:115], v[56:59], a[32:35], v[112:115]\n"
      "ds_read_b64_tr_b16 v[48:49], v13\n"
      "ds_read_b64_tr_b16 v[50:51], v13 offset:512\n"
      "v_mfma_f32_16x16x32_bf16 v[112:115], v[60:63], a[36:39], v[112:115]\n"
      "v_mfma_f32_16x16x32_bf16 v[116:119], v[56:59], a[40:43], v[116:119]\n"
      "v_mfma_f32_16x16x32_bf16 v[116:119], v[60:63], a[44:47], v[116:119]\n"
      "v_mfma_f32_16x16x32_bf16 v[120:123], v[56:59], a[48:51], v[120:123]\n"
      "ds_read_b64_tr_b16 v[52:53], v13 offset:8192\n"
      "ds_read_b64_tr_b16 v[54:55], v13 offset:8704\n"
      "v_mfma_f32_16x16x32_bf16 v[120:123], v[60:63], a[52:55], v[120:123]\n"
      "buffer_load_dwordx4 a[8:11], v1, s[36:39], s93 offen offset:2048 sc1\n"
      "v_mfma_f32_16x16x32_bf16 v[124:127], v[56:59], a[56:59], v[124:127]\n"
      "v_mfma_f32_16x16x32_bf16 v[124:127], v[60:63], a[60:63], v[124:127]\n"
      "s_waitcnt lgkmcnt(8)\n"
      "v_mfma_f32_16x16x32_bf16 v[96:99], v[32:35], a[64:67], v[96:99]\n"
      "ds_read_b64_tr_b16 v[56:57], v13 offset:4096\n"
      "ds_read_b64_tr_b16 v[58:59], v13 offset:4608\n"
      "v_mfma_f32_16x16x32_bf16 v[96:99], v[36:39], a[68:71], v[96:99]\n"
      "v_mfma_f32_16x16x32_bf16 v[100:103], v[32:35], a[72:75], v[100:103]\n"
      "buffer_load_dwordx4 a[12:15], v1, s[36:39], s93 offen offset:3072 sc1\n"
      "v_mfma_f32_16x16x32_bf16 v[100:103], v[36:39], a[76:79], v[100:103]\n"
      "v_mfma_f32_16x16x32_bf16 v[104:107], v[32:35], a[80:83], v[104:107]\n"
      "ds_read_b64_tr_b16 v[60:61], v13 offset:12288\n"
      "ds_read_b64_tr_b16 v[62:63], v13 offset:12800\n"
      "v_mfma_f32_16x16x32_bf16 v[104:107], v[36:39], a[84:87], v[104:107]\n"
      "v_mfma_f32_16x16x32_bf16 v[108:111], v[32:35], a[88:91], v[108:111]\n"
      "s_add_u32 s93, s59, 4096\n"
      "v_mfma_f32_16x16x32_bf16 v[108:111], v[36:39], a[92:95], v[108:111]\n"
      "v_xor_b32 v11, 0x0, v9\n"
      "s_waitcnt lgkmcnt(8)\n"
      "v_mfma_f32_16x16x32_bf16 v[112:115], v[40:43], a[64:67], v[112:115]\n"
      "ds_read_b64_tr_b16 v[32:33], v11\n"
      "ds_read_b64_tr_b16 v[34:35], v11 offset:512\n"
      "v_mfma_f32_16x16x32_bf16 v[112:115], v[44:47], a[68:71], v[112:115]\n"
      "buffer_load_dwordx4 a[16:19], v1, s[36:39], s93 offen sc1\n"
      "v_mfma_f32_16x16x32_bf16 v[116:119], v[40:43], a[72:75], v[116:119]\n"
      "v_mfma_f32_16x16x32_bf16 v[116:119], v[44:47], a[76:79], v[116:119]\n"
      "v_mfma_f32_16x16x32_bf16 v[120:123], v[40:43], a[80:83], v[120:123]\n"
      "ds_read_b64_tr_b16 v[36:37], v11 offset:8192\n"
      "ds_read_b64_tr_b16 v[38:39], v11 offset:8704\n"
      "v_mfma_f32_16x16x32_bf16 v[120:123], v[44:47], a[84:87], v[120:123]\n"
      "v_mfma_f32_16x16x32_bf16 v[124:127], v[40:43], a[88:91], v[124:127]\n"
      "buffer_load_dwordx4 a[20:23], v1, s[36:39], s93 offen offset:1024 sc1\n"
      "v_mfma_f32_16x16x32_bf16 v[124:127], v[44:47], a[92:95], v[124:127]\n"
      "s_waitcnt lgkmcnt(8)\n"
      "v_mfma_f32_16x16x32_bf16 v[128:131], v[48:51], a[224:227], v[128:131]\n"
      "ds_read_b64_tr_b16 v[40:41], v11 offset:4096\n"
      "ds_read_b64_tr_b16 v[42:43], v11 offset:4608\n"
      "v_mfma_f32_16x16x32_bf16 v[128:131], v[52:55], a[228:231], v[128:131]\n"
      "v_mfma_f32_16x16x32_bf16 v[132:135], v[48:51], a[232:235], v[132:135]\n"
      "v_mfma_f32_16x16x32_bf16 v[132:135], v[52:55], a[236:239], v[132:135]\n"
      "v_mfma_f32_16x16x32_bf16 v[136:139], v[48:51], a[240:243], v[136:139]\n"
      "ds_read_b64_tr_b16 v[44:45], v11 offset:12288\n"
      "ds_read_b64_tr_b16 v[46:47], v11 offset:12800\n"
      "v_mfma_f32_16x16x32_bf16 v[136:139], v[52:55], a[244:247], v[136:139]\n"
      "buffer_load_dwordx4 a[24:27], v1, s[36:39], s93 offen offset:2048 sc1\n"
      "v_mfma_f32_16x16x32_bf16 v[140:143], v[48:51], a[248:251], v[140:143]\n"
      "v_mfma_f32_16x16x32_bf16 v[140:143], v[52:55], a[252:255], v[140:143]\n"
      "v_xor_b32 v13, 0x14020, v9\n"
      "s_waitcnt lgkmcnt(8)\n"
      "v_mfma_f32_16x16x32_bf16 v[144:147], v[56:59], a[224:227], v[144:147]\n"
      "s_barrier\n"
      "ds_read_b64_tr_b16 v[48:49], v13\n"
      "ds_read_b64_tr_b16 v[50:51], v13 offset:512\n"
      "v_mfma_f32_16x16x32_bf16 v[144:147], v[60:63], a[228:231], v[144:147]\n"
      "v_mfma_f32_16x16x32_bf16 v[148:151], v[56:59], a[232:235], v[148:151]\n"
      "buffer_load_dwordx4 a[28:31], v1, s[36:39], s93 offen offset:3072 sc1\n"
      "v_mfma_f32_16x16x32_bf16 v[148:151], v[60:63], a[236:239], v[148:151]\n"
      "v_mfma_f32_16x16x32_bf16 v[152:155], v[56:59], a[240:243], v[152:155]\n"
      "ds_read_b64_tr_b16 v[52:53], v13 offset:8192\n"
      "ds_read_b64_tr_b16 v[54:55], v13 offset:8704\n"
      "v_mfma_f32_16x16x32_bf16 v[152:155], v[60:63], a[244:247], v[152:155]\n"
      "v_mfma_f32_16x16x32_bf16 v[156:159], v[56:59], a[248:251], v[156:159]\n"
      "s_add_u32 m0, s81, 32768\n"
      "s_add_u32 s96, s89, 16384\n"
      "v_mfma_f32_16x16x32_bf16 v[156:159], v[60:63], a[252:255], v[156:159]\n"
      "s_waitcnt lgkmcnt(8)\n"
      "v_mfma_f32_16x16x32_bf16 v[128:131], v[32:35], v[224:227], v[128:131]\n"
      "ds_read_b64_tr_b16 v[56:57], v13 offset:4096\n"
      "ds_read_b64_tr_b16 v[58:59], v13 offset:4608\n"
      "v_mfma_f32_16x16x32_bf16 v[128:131], v[36:39], v[228:231], v[128:131]\n"
      "s_waitcnt vmcnt(9)\n"
      "buffer_store_dwordx2 v[74:75], v76, s[36:39], s58 offen offset:384 sc1\n"
      "v_mfma_f32_16x16x32_bf16 v[132:135], v[32:35], v[232:235], v[132:135]\n"
      "buffer_load_dwordx4 v1, s[36:39], s96 offen sc1 lds\n"
      "v_mfma_f32_16x16x32_bf16 v[132:135], v[36:39], v[236:239], v[132:135]\n"
      "v_mfma_f32_16x16x32_bf16 v[136:139], v[32:35], v[240:243], v[136:139]\n"
      "ds_read_b64_tr_b16 v[60:61], v13 offset:12288\n"
      "ds_read_b64_tr_b16 v[62:63], v13 offset:12800\n"
      "v_mfma_f32_16x16x32_bf16 v[136:139], v[36:39], v[244:247], v[136:139]\n"
      "v_mfma_f32_16x16x32_bf16 v[140:143], v[32:35], v[248:251], v[140:143]\n"
      "buffer_load_dwordx4 v1, s[36:39], s96 offen offset:1024 sc1 lds\n"
      "v_mfma_f32_16x16x32_bf16 v[140:143], v[36:39], v[252:255], v[140:143]\n"
      "v_xor_b32 v11, 0x18020, v9\n"
      "s_waitcnt lgkmcnt(8)\n"
      "v_mfma_f32_16x16x32_bf16 v[144:147], v[40:43], v[224:227], v[144:147]\n"
      "ds_read_b64_tr_b16 v[32:33], v11\n"
      "ds_read_b64_tr_b16 v[34:35], v11 offset:512\n"
      "v_mfma_f32_16x16x32_bf16 v[144:147], v[44:47], v[228:231], v[144:147]\n"
      "v_mfma_f32_16x16x32_bf16 v[148:151], v[40:43], v[232:235], v[148:151]\n"
      "v_mfma_f32_16x16x32_bf16 v[148:151], v[44:47], v[236:239], v[148:151]\n"
      "v_mfma_f32_16x16x32_bf16 v[152:155], v[40:43], v[240:243], v[152:155]\n"
      "ds_read_b64_tr_b16 v[36:37], v11 offset:8192\n"
      "ds_read_b64_tr_b16 v[38:39], v11 offset:8704\n"
      "v_mfma_f32_16x16x32_bf16 v[152:155], v[44:47], v[244:247], v[152:155]\n"
      "buffer_load_dwordx4 v1, s[36:39], s96 offen offset:2048 sc1 lds\n"
      "v_mfma_f32_16x16x32_bf16 v[156:159], v[40:43], v[248:251], v[156:159]\n"
      "v_mfma_f32_16x16x32_bf16 v[156:159], v[44:47], v[252:255], v[156:159]\n"
      "s_waitcnt lgkmcnt(8)\n"
      "v_mfma_f32_16x16x32_bf16 v[160:163], v[48:51], a[96:99], v[160:163]\n"
      "ds_read_b64_tr_b16 v[40:41], v11 offset:4096\n"
      "ds_read_b64_tr_b16 v[42:43], v11 offset:4608\n"
      "v_mfma_f32_16x16x32_bf16 v[160:163], v[52:55], a[100:103], v[160:163]\n"
      "v_mfma_f32_16x16x32_bf16 v[164:167], v[48:51], a[104:107], v[164:167]\n"
      "buffer_load_dwordx4 v1, s[36:39], s96 offen offset:3072 sc1 lds\n"
      "v_mfma_f32_16x16x32_bf16 v[164:167], v[52:55], a[108:111], v[164:167]\n"
      "v_mfma_f32_16x16x32_bf16 v[168:171], v[48:51], a[112:115], v[168:171]\n"
      "ds_read_b64_tr_b16 v[44:45], v11 offset:12288\n"
      "ds_read_b64_tr_b16 v[46:47], v11 offset:12800\n"
      "v_mfma_f32_16x16x32_bf16 v[168:171], v[52:55], a[116:119], v[168:171]\n"
      "v_mfma_f32_16x16x32_bf16 v[172:175], v[48:51], a[120:123], v[172:175]\n"
      "s_add_u32 s93, s59, 8192\n"
      "v_mfma_f32_16x16x32_bf16 v[172:175], v[52:55], a[124:127], v[172:175]\n"
      "v_xor_b32 v13, 0x1c020, v9\n"
      "s_waitcnt lgkmcnt(8)\n"
      "v_mfma_f32_16x16x32_bf16 v[176:179], v[56:59], a[96:99], v[176:179]\n"
      "ds_read_b64_tr_b16 v[48:49], v13\n"
      "ds_read_b64_tr_b16 v[50:51], v13 offset:512\n"
      "v_mfma_f32_16x16x32_bf16 v[176:179], v[60:63], a[100:103], v[176:179]\n"
      "buffer_load_dwordx4 a[32:35], v1, s[36:39], s93 offen sc1\n"
      "v_mfma_f32_16x16x32_bf16 v[180:183], v[56:59], a[104:107], v[180:183]\n"
      "v_exp_f32 v128, -v128\n"
      "v_mfma_f32_16x16x32_bf16 v[180:183], v[60:63], a[108:111], v[180:183]\n"
      "v_exp_f32 v129, -v129\n"
      "v_mfma_f32_16x16x32_bf16 v[184:187], v[56:59], a[112:115], v[184:187]\n"
      "ds_read_b64_tr_b16 v[52:53], v13 offset:8192\n"
      "ds_read_b64_tr_b16 v[54:55], v13 offset:8704\n"
      "v_mfma_f32_16x16x32_bf16 v[184:187], v[60:63], a[116:119], v[184:187]\n"
      "v_exp_f32 v130, -v130\n"
      "v_mfma_f32_16x16x32_bf16 v[188:191], v[56:59], a[120:123], v[188:191]\n"
      "buffer_load_dwordx4 a[36:39], v1, s[36:39], s93 offen offset:1024 sc1\n"
      "v_add_f32 v128, 1.0, v128\n"
      "v_mfma_f32_16x16x32_bf16 v[188:191], v[60:63], a[124:127], v[188:191]\n"
      "s_waitcnt lgkmcnt(8)\n"
      "v_add_f32 v129, 1.0, v129\n"
      "v_mfma_f32_16x16x32_bf16 v[160:163], v[32:35], a[128:131], v[160:163]\n"
      "ds_read_b64_tr_b16 v[56:57], v13 offset:4096\n"
      "ds_read_b64_tr_b16 v[58:59], v13 offset:4608\n"
      "v_mfma_f32_16x16x32_bf16 v[160:163], v[36:39], a[132:135], v[160:163]\n"
      "buffer_load_dwordx2 v[70:71], v23, s[36:39], 0 offen offset:256 sc1\n"
      "v_add_f32 v130, 1.0, v130\n"
      "v_mfma_f32_16x16x32_bf16 v[164:167], v[32:35], a[136:139], v[164:167]\n"
      "v_rcp_f32 v128, v128\n"
      "v_mfma_f32_16x16x32_bf16 v[164:167], v[36:39], a[140:143], v[164:167]\n"
      "v_exp_f32 v131, -v131\n"
      "v_mfma_f32_16x16x32_bf16 v[168:171], v[32:35], a[144:147], v[168:171]\n"
      "ds_read_b64_tr_b16 v[60:61], v13 offset:12288\n"
      "ds_read_b64_tr_b16 v[62:63], v13 offset:12800\n"
      "v_mfma_f32_16x16x32_bf16 v[168:171], v[36:39], a[148:151], v[168:171]\n"
      "buffer_load_dwordx4 a[40:43], v1, s[36:39], s93 offen offset:2048 sc1\n"
      "v_mul_f32 v128, v128, v144\n"
      "v_mfma_f32_16x16x32_bf16 v[172:175], v[32:35], a[152:155], v[172:175]\n"
      "v_rcp_f32 v129, v129\n"
      "v_mfma_f32_16x16x32_bf16 v[172:175], v[36:39], a[156:159], v[172:175]\n"
      "v_xor_b32 v11, 0x20020, v9\n"
      "s_waitcnt lgkmcnt(8)\n"
      "v_mfma_f32_16x16x32_bf16 v[176:179], v[40:43], a[128:131], v[176:179]\n"
      "ds_read_b64_tr_b16 v[32:33], v11\n"
      "ds_read_b64_tr_b16 v[34:35], v11 offset:512\n"
      "v_mfma_f32_16x16x32_bf16 v[176:179], v[44:47], a[132:135], v[176:179]\n"
      "v_exp_f32 v132, -v132\n"
      "v_mfma_f32_16x16x32_bf16 v[180:183], v[40:43], a[136:139], v[180:183]\n"
      "buffer_load_dwordx4 a[44:47], v1, s[36:39], s93 offen offset:3072 sc1\n"
      "v_add_f32 v131, 1.0, v131\n"
      "v_mfma_f32_16x16x32_bf16 v[180:183], v[44:47], a[140:143], v[180:183]\n"
      "v_rcp_f32 v130, v130\n"
      "v_mfma_f32_16x16x32_bf16 v[184:187], v[40:43], a[144:147], v[184:187]\n"
      "ds_read_b64_tr_b16 v[36:37], v11 offset:8192\n"
      "ds_read_b64_tr_b16 v[38:39], v11 offset:8704\n"
      "Lpad10_%=:\n"
      "s_cbranch_execz Lpad11_%=\n"
      "v_mfma_f32_16x16x32_bf16 v[184:187], v[44:47], a[148:151], v[184:187]\n"
      "v_exp_f32 v133, -v133\n"
      "v_mfma_f32_16x16x32_bf16 v[188:191], v[40:43], a[152:155], v[188:191]\n"
      "s_add_u32 m0, s81, 49152\n"
      "s_add_u32 s96, s89, 32768\n"
      "v_mfma_f32_16x16x32_bf16 v[188:191], v[44:47], a[156:159], v[188:191]\n"
      "s_waitcnt lgkmcnt(8)\n"
      "v_mul_f32 v129, v129, v145\n"
      "v_mfma_f32_16x16x32_bf16 v[160:163], v[48:51], a[160:163], v[160:163]\n"
      "ds_read_b64_tr_b16 v[40:41], v11 offset:4096\n"
      "ds_read_b64_tr_b16 v[42:43], v11 offset:4608\n"
      "v_mfma_f32_16x16x32_bf16 v[160:163], v[52:55], a[164:167], v[160:163]\n"
      "buffer_load_dwordx4 v1, s[36:39], s96 offen sc1 lds\n"
      "v_add_f32 v132, 1.0, v132\n"
      "v_mfma_f32_16x16x32_bf16 v[164:167], v[48:51], a[168:171], v[164:167]\n"
      "v_rcp_f32 v131, v131\n"
      "v_mfma_f32_16x16x32_bf16 v[164:167], v[52:55], a[172:175], v[164:167]\n"
      "v_exp_f32 v134, -v134\n"
      "v_mfma_f32_16x16x32_bf16 v[168:171], v[48:51], a[176:179], v[168:171]\n"
      "ds_read_b64_tr_b16 v[44:45], v11 offset:12288\n"
      "ds_read_b64_tr_b16 v[46:47], v11 offset:12800\n"
      "v_mfma_f32_16x16x32_bf16 v[168:171], v[52:55], a[180:183], v[168:171]\n"
      "v_rcp_f32 v132, v132\n"
      "v_mfma_f32_16x16x32_bf16 v[172:175], v[48:51], a[184:187], v[172:175]\n"
      "buffer_load_dwordx4 v1, s[36:39], s96 offen offset:1024 sc1 lds\n"
      "v_mul_f32 v130, v130, v146\n"
      "v_mfma_f32_16x16x32_bf16 v[172:175], v[52:55], a[188:191], v[172:175]\n"
      "v_xor_b32 v13, 0x24020, v9\n"
      "s_waitcnt lgkmcnt(8)\n"
      "v_mfma_f32_16x16x32_bf16 v[176:179], v[56:59], a[160:163], v[176:179]\n"
      "ds_read_b64_tr_b16 v[48:49], v13\n"
      "ds_read_b64_tr_b16 v[50:51], v13 offset:512\n"
      "v_mfma_f32_16x16x32_bf16 v[176:179], v[60:63], a[164:167], v[176:179]\n"
      "v_exp_f32 v135, -v135\n"
      "v_mfma_f32_16x16x32_bf16 v[180:183], v[56:59], a[168:171], v[180:183]\n"
      "v_add_f32 v133, 1.0, v133\n"
      "v_mul_f32 v131, v131, v147\n"
      "v_mfma_f32_16x16x32_bf16 v[180:183], v[60:63], a[172:175], v[180:183]\n"
      "v_rcp_f32 v133, v133\n"
      "v_mfma_f32_16x16x32_bf16 v[184:187], v[56:59], a[176:179], v[184:187]\n"
      "ds_read_b64_tr_b16 v[52:53], v13 offset:8192\n"
      "ds_read_b64_tr_b16 v[54:55], v13 offset:8704\n"
      "v_mfma_f32_16x16x32_bf16 v[184:187], v[60:63], a[180:183], v[184:187]\n"
      "buffer_load_dwordx4 v1, s[36:39], s96 offen offset:2048 sc1 lds\n"
      "v_add_f32 v134, 1.0, v134\n"
      "v_mfma_f32_16x16x32_bf16 v[188:191], v[56:59], a[184:187], v[188:191]\n"
      "v_rcp_f32 v134, v134\n"
      "v_mfma_f32_16x16x32_bf16 v[188:191], v[60:63], a[188:191], v[188:191]\n"
      "s_waitcnt lgkmcnt(8)\n"
      "v_mul_f32 v132, v132, v148\n"
      "v_mfma_f32_16x16x32_bf16 v[160:163], v[32:35], a[192:195], v[160:163]\n"
      "ds_read_b64_tr_b16 v[56:57], v13 offset:4096\n"
      "ds_read_b64_tr_b16 v[58:59], v13 offset:4608\n"
      "v_mfma_f32_16x16x32_bf16 v[160:163], v[36:39], a[196:199], v[160:163]\n"
      "v_exp_f32 v136, -v136\n"
      "v_mfma_f32_16x16x32_bf16 v[164:167], v[32:35], a[200:203], v[164:167]\n"
      "buffer_load_dwordx4 v1, s[36:39], s96 offen offset:3072 sc1 lds\n"
      "v_add_f32 v135, 1.0, v135\n"
      "v_mfma_f32_16x16x32_bf16 v[164:167], v[36:39], a[204:207], v[164:167]\n"
      "v_rcp_f32 v135, v135\n"
      "v_mfma_f32_16x16x32_bf16 v[168:171], v[32:35], a[208:211], v[168:171]\n"
      "ds_read_b64_tr_b16 v[60:61], v13 offset:12288\n"
      "ds_read_b64_tr_b16 v[62:63], v13 offset:12800\n"
      "v_mfma_f32_16x16x32_bf16 v[168:171], v[36:39], a[212:215], v[168:171]\n"
      "v_exp_f32 v137, -v137\n"
      "v_mfma_f32_16x16x32_bf16 v[172:175], v[32:35], a[216:219], v[172:175]\n"
      "s_add_u32 m0, s81, 65536\n"
      "s_add_u32 s96, s89, 49152\n"
      "v_mfma_f32_16x16x32_bf16 v[172:175], v[36:39], a[220:223], v[172:175]\n"
      "v_xor_b32 v11, 0x20, v9\n"
      "s_waitcnt lgkmcnt(8)\n"
      "v_mfma_f32_16x16x32_bf16 v[176:179], v[40:43], a[192:195], v[176:179]\n"
      "ds_read_b64_tr_b16 v[32:33], v11\n"
      "ds_read_b64_tr_b16 v[34:35], v11 offset:512\n"
      "v_mfma_f32_16x16x32_bf16 v[176:179], v[44:47], a[196:199], v[176:179]\n"
      "buffer_load_dwordx4 v1, s[36:39], s96 offen sc1 lds\n"
      "v_mul_f32 v133, v133, v149\n"
      "v_mfma_f32_16x16x32_bf16 v[180:183], v[40:43], a[200:203], v[180:183]\n"
      "v_exp_f32 v138, -v138\n"
      "v_mfma_f32_16x16x32_bf16 v[180:183], v[44:47], a[204:207], v[180:183]\n"
      "v_exp_f32 v139, -v139\n"
      "v_mfma_f32_16x16x32_bf16 v[184:187], v[40:43], a[208:211], v[184:187]\n"
      "ds_read_b64_tr_b16 v[36:37], v11 offset:8192\n"
      "ds_read_b64_tr_b16 v[38:39], v11 offset:8704\n"
      "v_mfma_f32_16x16x32_bf16 v[184:187], v[44:47], a[212:215], v[184:187]\n"
      "v_mul_f32 v134, v134, v150\n"
      "v_mul_f32 v135, v135, v151\n"
      "v_mfma_f32_16x16x32_bf16 v[188:191], v[40:43], a[216:219], v[188:191]\n"
      "buffer_load_dwordx4 v1, s[36:39], s96 offen offset:1024 sc1 lds\n"
      "v_cvt_pk_bf16_f32 v144, v128, v129\n"
      "v_mfma_f32_16x16x32_bf16 v[188:191], v[44:47], a[220:223], v[188:191]\n"
      "s_waitcnt lgkmcnt(8)\n"
      "v_cvt_pk_bf16_f32 v146, v132, v133\n"
      "v_mfma_f32_16x16x32_bf16 v[160:163], v[48:51], a[224:227], v[160:163]\n"
      "ds_read_b64_tr_b16 v[40:41], v11 offset:4096\n"
      "ds_read_b64_tr_b16 v[42:43], v11 offset:4608\n"
      "v_mfma_f32_16x16x32_bf16 v[160:163], v[52:55], a[228:231], v[160:163]\n"
      "v_exp_f32 v140, -v140\n"
      "v_mfma_f32_16x16x32_bf16 v[164:167], v[48:51], a[232:235], v[164:167]\n"
      "v_cvt_pk_bf16_f32 v145, v130, v131\n"
      "v_cvt_pk_bf16_f32 v147, v134, v135\n"
      "v_mfma_f32_16x16x32_bf16 v[164:167], v[52:55], a[236:239], v[164:167]\n"
      "v_exp_f32 v141, -v141\n"
      "v_mfma_f32_16x16x32_bf16 v[168:171], v[48:51], a[240:243], v[168:171]\n"
      "ds_read_b64_tr_b16 v[44:45], v11 offset:12288\n"
      "ds_read_b64_tr_b16 v[46:47], v11 offset:12800\n"
      "v_mfma_f32_16x16x32_bf16 v[168:171], v[52:55], a[244:247], v[168:171]\n"
      "buffer_load_dwordx4 v1, s[36:39], s96 offen offset:2048 sc1 lds\n"
      "v_permlane16_swap_b32 v144, v146\n"
      "v_mfma_f32_16x16x32_bf16 v[172:175], v[48:51], a[248:251], v[172:175]\n"
      "v_permlane16_swap_b32 v145, v147\n"
      "v_add_f32 v136, 1.0, v136\n"
      "v_mfma_f32_16x16x32_bf16 v[172:175], v[52:55], a[252:255], v[172:175]\n"
      "v_xor_b32 v13, 0x14040, v9\n"
      "s_waitcnt lgkmcnt(8)\n"
      "v_mfma_f32_16x16x32_bf16 v[176:179], v[56:59], a[224:227], v[176:179]\n"
      "ds_read_b64_tr_b16 v[48:49], v13\n"
      "ds_read_b64_tr_b16 v[50:51], v13 offset:512\n"
      "v_mfma_f32_16x16x32_bf16 v[176:179], v[60:63], a[228:231], v[176:179]\n"
      "s_add_u32 s95, s35, 40960\n"
      "buffer_store_dwordx4 v[144:147], v72, s[36:39], s95 offen sc1\n"
      "v_mfma_f32_16x16x32_bf16 v[180:183], v[56:59], a[232:235], v[180:183]\n"
      "buffer_load_dwordx4 v1, s[36:39], s96 offen offset:3072 sc1 lds\n"
      "v_add_f32 v137, 1.0, v137\n"
      "v_mfma_f32_16x16x32_bf16 v[180:183], v[60:63], a[236:239], v[180:183]\n"
      "v_rcp_f32 v136, v136\n"
      "v_mfma_f32_16x16x32_bf16 v[184:187], v[56:59], a[240:243], v[184:187]\n"
      "ds_read_b64_tr_b16 v[52:53], v13 offset:8192\n"
      "ds_read_b64_tr_b16 v[54:55], v13 offset:8704\n"
      "v_mfma_f32_16x16x32_bf16 v[184:187], v[60:63], a[244:247], v[184:187]\n"
      "v_rcp_f32 v137, v137\n"
      "v_mfma_f32_16x16x32_bf16 v[188:191], v[56:59], a[248:251], v[188:191]\n"
      "s_add_u32 s93, s59, 12288\n"
      "v_add_f32 v138, 1.0, v138\n"
      "v_mfma_f32_16x16x32_bf16 v[188:191], v[60:63], a[252:255], v[188:191]\n"
      "s_waitcnt lgkmcnt(8)\n"
      "v_mul_f32 v136, v136, v152\n"
      "v_mfma_f32_16x16x32_bf16 v[160:163], v[32:35], v[224:227], v[160:163]\n"
      "ds_read_b64_tr_b16 v[56:57], v13 offset:4096\n"
      "ds_read_b64_tr_b16 v[58:59], v13 offset:4608\n"
      "v_mfma_f32_16x16x32_bf16 v[160:163], v[36:39], v[228:231], v[160:163]\n"
      "buffer_load_dwordx4 a[48:51], v1, s[36:39], s93 offen sc1\n"
      "v_add_f32 v139, 1.0, v139\n"
      "v_mfma_f32_16x16x32_bf16 v[164:167], v[32:35], v[232:235], v[164:167]\n"
      "v_rcp_f32 v138, v138\n"
      "v_mfma_f32_16x16x32_bf16 v[164:167], v[36:39], v[236:239], v[164:167]\n"
      "v_rcp_f32 v139, v139\n"
      "v_mfma_f32_16x16x32_bf16 v[168:171], v[32:35], v[240:243], v[168:171]\n"
      "ds_read_b64_tr_b16 v[60:61], v13 offset:12288\n"
      "ds_read_b64_tr_b16 v[62:63], v13 offset:12800\n"
      "v_mfma_f32_16x16x32_bf16 v[168:171], v[36:39], v[244:247], v[168:171]\n"
      "v_exp_f32 v142, -v142\n"
      "v_mfma_f32_16x16x32_bf16 v[172:175], v[32:35], v[248:251], v[172:175]\n"
      "buffer_load_dwordx4 a[52:55], v1, s[36:39], s93 offen offset:1024 sc1\n"
      "v_mul_f32 v137, v137, v153\n"
      "v_mfma_f32_16x16x32_bf16 v[172:175], v[36:39], v[252:255], v[172:175]\n"
      "v_xor_b32 v11, 0x14060, v9\n"
      "s_waitcnt lgkmcnt(8)\n"
      "v_mfma_f32_16x16x32_bf16 v[176:179], v[40:43], v[224:227], v[176:179]\n"
      "ds_read_b64_tr_b16 v[32:33], v11\n"
      "ds_read_b64_tr_b16 v[34:35], v11 offset:512\n"
      "v_mfma_f32_16x16x32_bf16 v[176:179], v[44:47], v[228:231], v[176:179]\n"
      "v_exp_f32 v143, -v143\n"
      "v_mfma_f32_16x16x32_bf16 v[180:183], v[40:43], v[232:235], v[180:183]\n"
      "v_add_f32 v140, 1.0, v140\n"
      "v_mul_f32 v138, v138, v154\n"
      "v_mfma_f32_16x16x32_bf16 v[180:183], v[44:47], v[236:239], v[180:183]\n"
      "v_rcp_f32 v140, v140\n"
      "v_mfma_f32_16x16x32_bf16 v[184:187], v[40:43], v[240:243], v[184:187]\n"
      "ds_read_b64_tr_b16 v[36:37], v11 offset:8192\n"
      "ds_read_b64_tr_b16 v[38:39], v11 offset:8704\n"
      "v_mfma_f32_16x16x32_bf16 v[184:187], v[44:47], v[244:247], v[184:187]\n"
      "buffer_load_dwordx4 a[56:59], v1, s[36:39], s93 offen offset:2048 sc1\n"
      "v_add_f32 v141, 1.0, v141\n"
      "v_mfma_f32_16x16x32_bf16 v[188:191], v[40:43], v[248:251], v[188:191]\n"
      "v_rcp_f32 v141, v141\n"
      "v_mfma_f32_16x16x32_bf16 v[188:191], v[44:47], v[252:255], v[188:191]\n"
      "s_waitcnt lgkmcnt(8)\n"
      "v_mul_f32 v139, v139, v155\n"
      "v_mfma_f32_16x16x32_bf16 v[192:195], v[48:51], a[96:99], v[192:195]\n"
      "ds_read_b64_tr_b16 v[40:41], v11 offset:4096\n"
      "ds_read_b64_tr_b16 v[42:43], v11 offset:4608\n"
      "v_mfma_f32_16x16x32_bf16 v[192:195], v[52:55], a[100:103], v[192:195]\n"
      "s_waitcnt vmcnt(14)\n"
      "v_cmp_ne_u64 vcc, s[76:77], v[70:71]\n"
      "v_mfma_f32_16x16x32_bf16 v[196:199], v[48:51], a[104:107], v[196:199]\n"
      "s_cbranch_vccnz Lsl_29_%=\n"
      "Lbk_30_%=:\n"
      "buffer_load_dwordx4 a[60:63], v1, s[36:39], s93 offen offset:3072 sc1\n"
      "v_mfma_f32_16x16x32_bf16 v[196:199], v[52:55], a[108:111], v[196:199]\n"
      "v_add_f32 v142, 1.0, v142\n"
      "v_mul_f32 v140, v140, v156\n"
      "v_mfma_f32_16x16x32_bf16 v[200:203], v[48:51], a[112:115], v[200:203]\n"
      "ds_read_b64_tr_b16 v[44:45], v11 offset:12288\n"
      "ds_read_b64_tr_b16 v[46:47], v11 offset:12800\n"
      "v_mfma_f32_16x16x32_bf16 v[200:203], v[52:55], a[116:119], v[200:203]\n"
      "v_rcp_f32 v142, v142\n"
      "v_mfma_f32_16x16x32_bf16 v[204:207], v[48:51], a[120:123], v[204:207]\n"
      "s_add_u32 s93, s59, 16384\n"
      "v_add_f32 v143, 1.0, v143\n"
      "v_mfma_f32_16x16x32_bf16 v[204:207], v[52:55], a[124:127], v[204:207]\n"
      "v_xor_b32 v13, 0x18040, v9\n"
      "s_waitcnt lgkmcnt(8)\n"
      "v_mfma_f32_16x16x32_bf16 v[208:211], v[56:59], a[96:99], v[208:211]\n"
      "ds_read_b64_tr_b16 v[48:49], v13\n"
      "ds_read_b64_tr_b16 v[50:51], v13 offset:512\n"
      "v_mfma_f32_16x16x32_bf16 v[208:211], v[60:63], a[100:103], v[208:211]\n"
      "buffer_load_dwordx4 a[64:67], v1, s[36:39], s93 offen sc1\n"
      "v_mul_f32 v141, v141, v157\n"
      "v_mfma_f32_16x16x32_bf16 v[212:215], v[56:59], a[104:107], v[212:215]\n"
      "v_rcp_f32 v143, v143\n"
      "v_mfma_f32_16x16x32_bf16 v[212:215], v[60:63], a[108:111], v[212:215]\n"
      "v_exp_f32 v160, -v160\n"
      "v_mfma_f32_16x16x32_bf16 v[216:219], v[56:59], a[112:115], v[216:219]\n"
      "ds_read_b64_tr_b16 v[52:53], v13 offset:8192\n"
      "ds_read_b64_tr_b16 v[54:55], v13 offset:8704\n"
      "v_mfma_f32_16x16x32_bf16 v[216:219], v[60:63], a[116:119], v[216:219]\n"
      "v_exp_f32 v161, -v161\n"
      "v_mfma_f32_16x16x32_bf16 v[220:223], v[56:59], a[120:123], v[220:223]\n"
      "buffer_load_dwordx4 a[68:71], v1, s[36:39], s93 offen offset:1024 sc1\n"
      "v_mul_f32 v142, v142, v158\n"
      "v_mfma_f32_16x16x32_bf16 v[220:223], v[60:63], a[124:127], v[220:223]\n"
      "s_waitcnt lgkmcnt(8)\n"
      "v_mul_f32 v143, v143, v159\n"
      "v_mfma_f32_16x16x32_bf16 v[96:99], v[32:35], a[96:99], v[96:99]\n"
      "ds_read_b64_tr_b16 v[56:57], v13 offset:4096\n"
      "ds_read_b64_tr_b16 v[58:59], v13 offset:4608\n"
      "v_mfma_f32_16x16x32_bf16 v[96:99], v[36:39], a[100:103], v[96:99]\n"
      "v_exp_f32 v162, -v162\n"
      "v_mfma_f32_16x16x32_bf16 v[100:103], v[32:35], a[104:107], v[100:103]\n"
      "v_exp_f32 v163, -v163\n"
      "v_mfma_f32_16x16x32_bf16 v[100:103], v[36:39], a[108:111], v[100:103]\n"
      "v_exp_f32 v164, -v164\n"
      "v_mfma_f32_16x16x32_bf16 v[104:107], v[32:35], a[112:115], v[104:107]\n"
      "ds_read_b64_tr_b16 v[60:61], v13 offset:12288\n"
      "ds_read_b64_tr_b16 v[62:63], v13 offset:12800\n"
      "v_mfma_f32_16x16x32_bf16 v[104:107], v[36:39], a[116:119], v[104:107]\n"
      "buffer_load_dwordx4 a[72:75], v1, s[36:39], s93 offen offset:2048 sc1\n"
      "v_cvt_pk_bf16_f32 v152, v136, v137\n"
      "v_mfma_f32_16x16x32_bf16 v[108:111], v[32:35], a[120:123], v[108:111]\n"
      "v_cvt_pk_bf16_f32 v154, v140, v141\n"
      "v_cvt_pk_bf16_f32 v153, v138, v139\n"
      "v_mfma_f32_16x16x32_bf16 v[108:111], v[36:39], a[124:127], v[108:111]\n"
      "v_xor_b32 v11, 0x1c040, v9\n"
      "s_waitcnt lgkmcnt(8)\n"
      "v_mfma_f32_16x16x32_bf16 v[112:115], v[40:43], a[96:99], v[112:115]\n"
      "ds_read_b64_tr_b16 v[32:33], v11\n"
      "ds_read_b64_tr_b16 v[34:35], v11 offset:512\n"
      "v_mfma_f32_16x16x32_bf16 v[112:115], v[44:47], a[100:103], v[112:115]\n"
      "v_exp_f32 v165, -v165\n"
      "v_mfma_f32_16x16x32_bf16 v[116:119], v[40:43], a[104:107], v[116:119]\n"
      "buffer_load_dwordx4 a[76:79], v1, s[36:39], s93 offen offset:3072 sc1\n"
      "v_cvt_pk_bf16_f32 v155, v142, v143\n"
      "v_mfma_f32_16x16x32_bf16 v[116:119], v[44:47], a[108:111], v[116:119]\n"
      "v_permlane16_swap_b32 v152, v154\n"
      "v_permlane16_swap_b32 v153, v155\n"
      "v_mfma_f32_16x16x32_bf16 v[120:123], v[40:43], a[112:115], v[120:123]\n"
      "ds_read_b64_tr_b16 v[36:37], v11 offset:8192\n"
      "ds_read_b64_tr_b16 v[38:39], v11 offset:8704\n"
      "v_mfma_f32_16x16x32_bf16 v[120:123], v[44:47], a[116:119], v[120:123]\n"
      "s_add_u32 s95, s35, 335872\n"
      "buffer_store_dwordx4 v[152:155], v72, s[36:39], s95 offen sc1\n"
      "v_mfma_f32_16x16x32_bf16 v[124:127], v[40:43], a[120:123], v[124:127]\n"
      "s_add_u32 s93, s59, 20480\n"
      "v_add_f32 v160, 1.0, v160\n"
      "v_mfma_f32_16x16x32_bf16 v[124:127], v[44:47], a[124:127], v[124:127]\n"
      "s_waitcnt lgkmcnt(8)\n"
      "v_add_f32 v161, 1.0, v161\n"
      "v_mfma_f32_16x16x32_bf16 v[192:195], v[48:51], a[128:131], v[192:195]\n"
      "ds_read_b64_tr_b16 v[40:41], v11 offset:4096\n"
      "ds_read_b64_tr_b16 v[42:43], v11 offset:4608\n"
      "v_mfma_f32_16x16x32_bf16 v[192:195], v[52:55], a[132:135], v[192:195]\n"
      "buffer_load_dwordx2 v[70:71], v23, s[36:39], 0 offen offset:384 sc1\n"
      "buffer_load_dwordx4 a[80:83], v1, s[36:39], s93 offen sc1\n"
      "v_mfma_f32_16x16x32_bf16 v[196:199], v[48:51], a[136:139], v[196:199]\n"
      "v_rcp_f32 v160, v160\n"
      "v_mfma_f32_16x16x32_bf16 v[196:199], v[52:55], a[140:143], v[196:199]\n"
      "v_rcp_f32 v161, v161\n"
      "v_mfma_f32_16x16x32_bf16 v[200:203], v[48:51], a[144:147], v[200:203]\n"
      "ds_read_b64_tr_b16 v[44:45], v11 offset:12288\n"
      "ds_read_b64_tr_b16 v[46:47], v11 offset:12800\n"
      "v_mfma_f32_16x16x32_bf16 v[200:203], v[52:55], a[148:151], v[200:203]\n"
      "v_exp_f32 v166, -v166\n"
      "v_mfma_f32_16x16x32_bf16 v[204:207], v[48:51], a[152:155], v[204:207]\n"
      "buffer_load_dwordx4 a[84:87], v1, s[36:39], s93 offen offset:1024 sc1\n"
      "v_add_f32 v162, 1.0, v162\n"
      "v_mfma_f32_16x16x32_bf16 v[204:207], v[52:55], a[156:159], v[204:207]\n"
      "v_xor_b32 v13, 0x20040, v9\n"
      "s_waitcnt lgkmcnt(8)\n"
      "v_mfma_f32_16x16x32_bf16 v[208:211], v[56:59], a[128:131], v[208:211]\n"
      "s_barrier\n"
      "ds_read_b64_tr_b16 v[48:49], v13\n"
      "ds_read_b64_tr_b16 v[50:51], v13 offset:512\n"
      "v_mfma_f32_16x16x32_bf16 v[208:211], v[60:63], a[132:135], v[208:211]\n"
      "v_rcp_f32 v162, v162\n"
      "v_mfma_f32_16x16x32_bf16 v[212:215], v[56:59], a[136:139], v[212:215]\n"
      "v_exp_f32 v167, -v167\n"
      "v_mfma_f32_16x16x32_bf16 v[212:215], v[60:63], a[140:143], v[212:215]\n"
      "v_mul_f32 v160, v160, v176\n"
      "v_add_f32 v163, 1.0, v163\n"
      "v_mfma_f32_16x16x32_bf16 v[216:219], v[56:59], a[144:147], v[216:219]\n"
      "ds_read_b64_tr_b16 v[52:53], v13 offset:8192\n"
      "ds_read_b64_tr_b16 v[54:55], v13 offset:8704\n"
      "v_mfma_f32_16x16x32_bf16 v[216:219], v[60:63], a[148:151], v[216:219]\n"
      "buffer_load_dwordx4 a[88:91], v1, s[36:39], s93 offen offset:2048 sc1\n"
      "v_mul_f32 v161, v161, v177\n"
      "v_mfma_f32_16x16x32_bf16 v[220:223], v[56:59], a[152:155], v[220:223]\n"
      "v_rcp_f32 v163, v163\n"
      "v_mfma_f32_16x16x32_bf16 v[220:223], v[60:63], a[156:159], v[220:223]\n"
      "s_waitcnt lgkmcnt(8)\n"
      "v_add_f32 v164, 1.0, v164\n"
      "v_mfma_f32_16x16x32_bf16 v[192:195], v[32:35], a[160:163], v[192:195]\n"
      "ds_read_b64_tr_b16 v[56:57], v13 offset:4096\n"
      "ds_read_b64_tr_b16 v[58:59], v13 offset:4608\n"
      "v_mfma_f32_16x16x32_bf16 v[192:195], v[36:39], a[164:167], v[192:195]\n"
      "v_rcp_f32 v164, v164\n"
      "v_mfma_f32_16x16x32_bf16 v[196:199], v[32:35], a[168:171], v[196:199]\n"
      "buffer_load_dwordx4 a[92:95], v1, s[36:39], s93 offen offset:3072 sc1\n"
      "v_mul_f32 v162, v162, v178\n"
      "v_mfma_f32_16x16x32_bf16 v[196:199], v[36:39], a[172:175], v[196:199]\n"
      "v_add_f32 v165, 1.0, v165\n"
      "v_mul_f32 v163, v163, v179\n"
      "v_mfma_f32_16x16x32_bf16 v[200:203], v[32:35], a[176:179], v[200:203]\n"
      "ds_read_b64_tr_b16 v[60:61], v13 offset:12288\n"
      "ds_read_b64_tr_b16 v[62:63], v13 offset:12800\n"
      "v_mfma_f32_16x16x32_bf16 v[200:203], v[36:39], a[180:183], v[200:203]\n"
      "v_rcp_f32 v165, v165\n"
      "v_mfma_f32_16x16x32_bf16 v[204:207], v[32:35], a[184:187], v[204:207]\n"
      "s_add_u32 m0, s81, 81920\n"
      "s_add_u32 s96, s89, 65536\n"
      "v_mfma_f32_16x16x32_bf16 v[204:207], v[36:39], a[188:191], v[204:207]\n"
      "v_xor_b32 v11, 0x24040, v9\n"
      "s_waitcnt lgkmcnt(8)\n"
      "v_mfma_f32_16x16x32_bf16 v[208:211], v[40:43], a[160:163], v[208:211]\n"
      "ds_read_b64_tr_b16 v[32:33], v11\n"
      "ds_read_b64_tr_b16 v[34:35], v11 offset:512\n"
      "v_mfma_f32_16x16x32_bf16 v[208:211], v[44:47], a[164:167], v[208:211]\n"
      "buffer_load_dwordx4 v1, s[36:39], s96 offen sc1 lds\n"
      "v_add_f32 v166, 1.0, v166\n"
      "v_mfma_f32_16x16x32_bf16 v[212:215], v[40:43], a[168:171], v[212:215]\n"
      "v_rcp_f32 v166, v166\n"
      "v_mfma_f32_16x16x32_bf16 v[212:215], v[44:47], a[172:175], v[212:215]\n"
      "v_exp_f32 v168, -v168\n"
      "v_mfma_f32_16x16x32_bf16 v[216:219], v[40:43], a[176:179], v[216:219]\n"
      "ds_read_b64_tr_b16 v[36:37], v11 offset:8192\n"
      "ds_read_b64_tr_b16 v[38:39], v11 offset:8704\n"
      "v_mfma_f32_16x16x32_bf16 v[216:219], v[44:47], a[180:183], v[216:219]\n"
      "v_exp_f32 v169, -v169\n"
      "v_mfma_f32_16x16x32_bf16 v[220:223], v[40:43], a[184:187], v[220:223]\n"
      "buffer_load_dwordx4 v1, s[36:39], s96 offen offset:1024 sc1 lds\n"
      "v_mul_f32 v164, v164, v180\n"
      "v_mfma_f32_16x16x32_bf16 v[220:223], v[44:47], a[188:191], v[220:223]\n"
      "s_waitcnt lgkmcnt(8)\n"
      "v_add_f32 v167, 1.0, v167\n"
      "v_mfma_f32_16x16x32_bf16 v[192:195], v[48:51], a[192:195], v[192:195]\n"
      "ds_read_b64_tr_b16 v[40:41], v11 offset:4096\n"
      "ds_read_b64_tr_b16 v[42:43], v11 offset:4608\n"
      "v_mfma_f32_16x16x32_bf16 v[192:195], v[52:55], a[196:199], v[192:195]\n"
      "v_rcp_f32 v167, v167\n"
      "v_mfma_f32_16x16x32_bf16 v[196:199], v[48:51], a[200:203], v[196:199]\n"
      "v_exp_f32 v170, -v170\n"
      "v_mfma_f32_16x16x32_bf16 v[196:199], v[52:55], a[204:207], v[196:199]\n"
      "v_exp_f32 v171, -v171\n"
      "v_mfma_f32_16x16x32_bf16 v[200:203], v[48:51], a[208:211], v[200:203]\n"
      "ds_read_b64_tr_b16 v[44:45], v11 offset:12288\n"
      "ds_read_b64_tr_b16 v[46:47], v11 offset:12800\n"
      "v_mfma_f32_16x16x32_bf16 v[200:203], v[52:55], a[212:215], v[200:203]\n"
      "buffer_load_dwordx4 v1, s[36:39], s96 offen offset:2048 sc1 lds\n"
      "v_mul_f32 v165, v165, v181\n"
      "v_mfma_f32_16x16x32_bf16 v[204:207], v[48:51], a[216:219], v[204:207]\n"
      "v_mul_f32 v166, v166, v182\n"
      "v_mul_f32 v167, v167, v183\n"
      "v_mfma_f32_16x16x32_bf16 v[204:207], v[52:55], a[220:223], v[204:207]\n"
      "v_xor_b32 v13, 0x40, v9\n"
      "s_waitcnt lgkmcnt(8)\n"
      "v_mfma_f32_16x16x32_bf16 v[208:211], v[56:59], a[192:195], v[208:211]\n"
      "ds_read_b64_tr_b16 v[48:49], v13\n"
      "ds_read_b64_tr_b16 v[50:51], v13 offset:512\n"
      "v_mfma_f32_16x16x32_bf16 v[208:211], v[60:63], a[196:199], v[208:211]\n"
      "v_exp_f32 v172, -v172\n"
      "v_mfma_f32_16x16x32_bf16 v[212:215], v[56:59], a[200:203], v[212:215]\n"
      "buffer_load_dwordx4 v1, s[36:39], s96 offen offset:3072 sc1 lds\n"
      "v_cvt_pk_bf16_f32 v176, v160, v161\n"
      "v_mfma_f32_16x16x32_bf16 v[212:215], v[60:63], a[204:207], v[212:215]\n"
      "v_cvt_pk_bf16_f32 v178, v164, v165\n"
      "v_cvt_pk_bf16_f32 v177, v162, v163\n"
      "v_mfma_f32_16x16x32_bf16 v[216:219], v[56:59], a[208:211], v[216:219]\n"
      "ds_read_b64_tr_b16 v[52:53], v13 offset:8192\n"
      "ds_read_b64_tr_b16 v[54:55], v13 offset:8704\n"
      "v_mfma_f32_16x16x32_bf16 v[216:219], v[60:63], a[212:215], v[216:219]\n"
      "v_exp_f32 v173, -v173\n"
      "v_mfma_f32_16x16x32_bf16 v[220:223], v[56:59], a[216:219], v[220:223]\n"
      "s_add_u32 s93, s59, 24576\n"
      "v_cvt_pk_bf16_f32 v179, v166, v167\n"
      "v_mfma_f32_16x16x32_bf16 v[220:223], v[60:63], a[220:223], v[220:223]\n"
      "s_waitcnt lgkmcnt(8)\n"
      "v_permlane16_swap_b32 v176, v178\n"
      "v_mfma_f32_16x16x32_bf16 v[192:195], v[32:35], a[224:227], v[192:195]\n"
      "ds_read_b64_tr_b16 v[56:57], v13 offset:4096\n"
      "ds_read_b64_tr_b16 v[58:59], v13 offset:4608\n"
      "v_mfma_f32_16x16x32_bf16 v[192:195], v[36:39], a[228:231], v[192:195]\n"
      "buffer_load_dwordx4 a[96:99], v1, s[36:39], s93 offen sc1\n"
      "v_permlane16_swap_b32 v177, v179\n"
      "v_mfma_f32_16x16x32_bf16 v[196:199], v[32:35], a[232:235], v[196:199]\n"
      "s_add_u32 s95, s35, 49152\n"
      "buffer_store_dwordx4 v[176:179], v72, s[36:39], s95 offen sc1\n"
      "v_mfma_f32_16x16x32_bf16 v[196:199], v[36:39], a[236:239], v[196:199]\n"
      "v_exp_f32 v174, -v174\n"
      "v_mfma_f32_16x16x32_bf16 v[200:203], v[32:35], a[240:243], v[200:203]\n"
      "ds_read_b64_tr_b16 v[60:61], v13 offset:12288\n"
      "ds_read_b64_tr_b16 v[62:63], v13 offset:12800\n"
      "v_mfma_f32_16x16x32_bf16 v[200:203], v[36:39], a[244:247], v[200:203]\n"
      "v_add_f32 v168, 1.0, v168\n"
      "v_add_f32 v169, 1.0, v169\n"
      "v_mfma_f32_16x16x32_bf16 v[204:207], v[32:35], a[248:251], v[204:207]\n"
      "buffer_load_dwordx4 a[100:103], v1, s[36:39], s93 offen offset:1024 sc1\n"
      "v_add_f32 v170, 1.0, v170\n"
      "v_mfma_f32_16x16x32_bf16 v[204:207], v[36:39], a[252:255], v[204:207]\n"
      "v_xor_b32 v11, 0x18060, v9\n"
      "s_waitcnt lgkmcnt(8)\n"
      "v_mfma_f32_16x16x32_bf16 v[208:211], v[40:43], a[224:227], v[208:211]\n"
      "ds_read_b64_tr_b16 v[32:33], v11\n"
      "ds_read_b64_tr_b16 v[34:35], v11 offset:512\n"
      "v_mfma_f32_16x16x32_bf16 v[208:211], v[44:47], a[228:231], v[208:211]\n"
      "v_rcp_f32 v168, v168\n"
      "v_mfma_f32_16x16x32_bf16 v[212:215], v[40:43], a[232:235], v[212:215]\n"
      "v_rcp_f32 v169, v169\n"
      "v_mfma_f32_16x16x32_bf16 v[212:215], v[44:47], a[236:239], v[212:215]\n"
      "v_rcp_f32 v170, v170\n"
      "v_mfma_f32_16x16x32_bf16 v[216:219], v[40:43], a[240:243], v[216:219]\n"
      "ds_read_b64_tr_b16 v[36:37], v11 offset:8192\n"
      "ds_read_b64_tr_b16 v[38:39], v11 offset:8704\n"
      "v_mfma_f32_16x16x32_bf16 v[216:219], v[44:47], a[244:247], v[216:219]\n"
      "buffer_load_dwordx4 a[104:107], v1, s[36:39], s93 offen offset:2048 sc1\n"
      "v_mul_f32 v168, v168, v184\n"
      "v_mfma_f32_16x16x32_bf16 v[220:223], v[40:43], a[248:251], v[220:223]\n"
      "v_exp_f32 v175, -v175\n"
      "v_mfma_f32_16x16x32_bf16 v[220:223], v[44:47], a[252:255], v[220:223]\n"
      "s_waitcnt lgkmcnt(8)\n"
      "v_add_f32 v171, 1.0, v171\n"
      "v_mfma_f32_16x16x32_bf16 v[192:195], v[48:51], v[224:227], v[192:195]\n"
      "ds_read_b64_tr_b16 v[40:41], v11 offset:4096\n"
      "ds_read_b64_tr_b16 v[42:43], v11 offset:4608\n"
      "v_mfma_f32_16x16x32_bf16 v[192:195], v[52:55], v[228:231], v[192:195]\n"
      "v_rcp_f32 v171, v171\n"
      "v_mfma_f32_16x16x32_bf16 v[196:199], v[48:51], v[232:235], v[196:199]\n"
      "buffer_load_dwordx4 a[108:111], v1, s[36:39], s93 offen offset:3072 sc1\n"
      "v_mul_f32 v169, v169, v185\n"
      "v_mfma_f32_16x16x32_bf16 v[196:199], v[52:55], v[236:239], v[196:199]\n"
      "v_add_f32 v172, 1.0, v172\n"
      "v_mul_f32 v170, v170, v186\n"
      "v_mfma_f32_16x16x32_bf16 v[200:203], v[48:51], v[240:243], v[200:203]\n"
      "ds_read_b64_tr_b16 v[44:45], v11 offset:12288\n"
      "ds_read_b64_tr_b16 v[46:47], v11 offset:12800\n"
      "v_mfma_f32_16x16x32_bf16 v[200:203], v[52:55], v[244:247], v[200:203]\n"
      "v_rcp_f32 v172, v172\n"
      "v_mfma_f32_16x16x32_bf16 v[204:207], v[48:51], v[248:251], v[204:207]\n"
      "s_add_u32 s93, s59, 28672\n"
      "v_add_f32 v173, 1.0, v173\n"
      "v_mfma_f32_16x16x32_bf16 v[204:207], v[52:55], v[252:255], v[204:207]\n"
      "v_xor_b32 v13, 0x1c060, v9\n"
      "s_waitcnt lgkmcnt(8)\n"
      "v_mfma_f32_16x16x32_bf16 v[208:211], v[56:59], v[224:227], v[208:211]\n"
      "ds_read_b64_tr_b16 v[48:49], v13\n"
      "ds_read_b64_tr_b16 v[50:51], v13 offset:512\n"
      "v_mfma_f32_16x16x32_bf16 v[208:211], v[60:63], v[228:231], v[208:211]\n"
      "buffer_load_dwordx4 a[112:115], v1, s[36:39], s93 offen sc1\n"
      "v_mul_f32 v171, v171, v187\n"
      "v_mfma_f32_16x16x32_bf16 v[212:215], v[56:59], v[232:235], v[212:215]\n"
      "v_rcp_f32 v173, v173\n"
      "v_mfma_f32_16x16x32_bf16 v[212:215], v[60:63], v[236:239], v[212:215]\n"
      "v_add_f32 v174, 1.0, v174\n"
      "v_mul_f32 v172, v172, v188\n"
      "v_mfma_f32_16x16x32_bf16 v[216:219], v[56:59], v[240:243], v[216:219]\n"
      "ds_read_b64_tr_b16 v[52:53], v13 offset:8192\n"
      "ds_read_b64_tr_b16 v[54:55], v13 offset:8704\n"
      "v_mfma_f32_16x16x32_bf16 v[216:219], v[60:63], v[244:247], v[216:219]\n"
      "buffer_load_dwordx4 a[116:119], v1, s[36:39], s93 offen offset:1024 sc1\n"
      "v_add_f32 v175, 1.0, v175\n"
      "v_mfma_f32_16x16x32_bf16 v[220:223], v[56:59], v[248:251], v[220:223]\n"
      "v_rcp_f32 v174, v174\n"
      "v_mfma_f32_16x16x32_bf16 v[220:223], v[60:63], v[252:255], v[220:223]\n"
      "s_waitcnt lgkmcnt(8)\n"
      "v_mul_f32 v173, v173, v189\n"
      "v_mfma_f32_16x16x32_bf16 v[96:99], v[32:35], a[128:131], v[96:99]\n"
      "ds_read_b64_tr_b16 v[56:57], v13 offset:4096\n"
      "ds_read_b64_tr_b16 v[58:59], v13 offset:4608\n"
      "v_mfma_f32_16x16x32_bf16 v[96:99], v[36:39], a[132:135], v[96:99]\n"
      "s_waitcnt vmcnt(15)\n"
      "v_cmp_ne_u64 vcc, s[76:77], v[70:71]\n"
      "v_mfma_f32_16x16x32_bf16 v[100:103], v[32:35], a[136:139], v[100:103]\n"
      "s_cbranch_vccnz Lsl_31_%=\n"
      "Lbk_32_%=:\n"
      "buffer_load_dwordx4 a[120:123], v1, s[36:39], s93 offen offset:2048 sc1\n"
      "v_mfma_f32_16x16x32_bf16 v[100:103], v[36:39], a[140:143], v[100:103]\n"
      "v_rcp_f32 v175, v175\n"
      "v_mfma_f32_16x16x32_bf16 v[104:107], v[32:35], a[144:147], v[104:107]\n"
      "ds_read_b64_tr_b16 v[60:61], v13 offset:12288\n"
      "ds_read_b64_tr_b16 v[62:63], v13 offset:12800\n"
      "v_mfma_f32_16x16x32_bf16 v[104:107], v[36:39], a[148:151], v[104:107]\n"
      "v_mul_f32 v174, v174, v190\n"
      "v_mul_f32 v175, v175, v191\n"
      "v_mfma_f32_16x16x32_bf16 v[108:111], v[32:35], a[152:155], v[108:111]\n"
      "v_mfma_f32_16x16x32_bf16 v[108:111], v[36:39], a[156:159], v[108:111]\n"
      "v_xor_b32 v11, 0x20060, v9\n"
      "s_waitcnt lgkmcnt(8)\n"
      "v_mfma_f32_16x16x32_bf16 v[112:115], v[40:43], a[128:131], v[112:115]\n"
      "ds_read_b64_tr_b16 v[32:33], v11\n"
      "ds_read_b64_tr_b16 v[34:35], v11 offset:512\n"
      "v_mfma_f32_16x16x32_bf16 v[112:115], v[44:47], a[132:135], v[112:115]\n"
      "buffer_load_dwordx4 a[124:127], v1, s[36:39], s93 offen offset:3072 sc1\n"
      "v_cvt_pk_bf16_f32 v184, v168, v169\n"
      "v_mfma_f32_16x16x32_bf16 v[116:119], v[40:43], a[136:139], v[116:119]\n"
      "v_exp_f32 v192, -v192\n"
      "v_mfma_f32_16x16x32_bf16 v[116:119], v[44:47], a[140:143], v[116:119]\n"
      "v_exp_f32 v193, -v193\n"
      "v_mfma_f32_16x16x32_bf16 v[120:123], v[40:43], a[144:147], v[120:123]\n"
      "ds_read_b64_tr_b16 v[36:37], v11 offset:8192\n"
      "ds_read_b64_tr_b16 v[38:39], v11 offset:8704\n"
      "v_mfma_f32_16x16x32_bf16 v[120:123], v[44:47], a[148:151], v[120:123]\n"
      "v_exp_f32 v194, -v194\n"
      "v_mfma_f32_16x16x32_bf16 v[124:127], v[40:43], a[152:155], v[124:127]\n"
      "v_exp_f32 v195, -v195\n"
      "v_mfma_f32_16x16x32_bf16 v[124:127], v[44:47], a[156:159], v[124:127]\n"
      "s_waitcnt lgkmcnt(8)\n"
      "v_cvt_pk_bf16_f32 v186, v172, v173\n"
      "v_mfma_f32_16x16x32_bf16 v[96:99], v[48:51], a[160:163], v[96:99]\n"
      "ds_read_b64_tr_b16 v[40:41], v11 offset:4096\n"
      "ds_read_b64_tr_b16 v[42:43], v11 offset:4608\n"
      "v_mfma_f32_16x16x32_bf16 v[96:99], v[52:55], a[164:167], v[96:99]\n"
      "s_add_u32 s93, s59, 32768\n"
      "buffer_load_dwordx4 a[128:131], v1, s[36:39], s93 offen sc1\n"
      "v_mfma_f32_16x16x32_bf16 v[100:103], v[48:51], a[168:171], v[100:103]\n"
      "v_exp_f32 v196, -v196\n"
      "v_mfma_f32_16x16x32_bf16 v[100:103], v[52:55], a[172:175], v[100:103]\n"
      "v_cvt_pk_bf16_f32 v185, v170, v171\n"
      "v_cvt_pk_bf16_f32 v187, v174, v175\n"
      "v_mfma_f32_16x16x32_bf16 v[104:107], v[48:51], a[176:179], v[104:107]\n"
      "ds_read_b64_tr_b16 v[44:45], v11 offset:12288\n"
      "ds_read_b64_tr_b16 v[46:47], v11 offset:12800\n"
      "v_mfma_f32_16x16x32_bf16 v[104:107], v[52:55], a[180:183], v[104:107]\n"
      "buffer_load_dwordx4 a[132:135], v1, s[36:39], s93 offen offset:1024 sc1\n"
      "v_permlane16_swap_b32 v184, v186\n"
      "v_mfma_f32_16x16x32_bf16 v[108:111], v[48:51], a[184:187], v[108:111]\n"
      "v_exp_f32 v197, -v197\n"
      "v_mfma_f32_16x16x32_bf16 v[108:111], v[52:55], a[188:191], v[108:111]\n"
      "v_xor_b32 v13, 0x24060, v9\n"
      "s_waitcnt lgkmcnt(8)\n"
      "v_mfma_f32_16x16x32_bf16 v[112:115], v[56:59], a[160:163], v[112:115]\n"
      "ds_read_b64_tr_b16 v[48:49], v13\n"
      "ds_read_b64_tr_b16 v[50:51], v13 offset:512\n"
      "v_mfma_f32_16x16x32_bf16 v[112:115], v[60:63], a[164:167], v[112:115]\n"
      "buffer_load_dwordx4 a[136:139], v1, s[36:39], s93 offen offset:2048 sc1\n"
      "v_permlane16_swap_b32 v185, v187\n"
      "v_mfma_f32_16x16x32_bf16 v[116:119], v[56:59], a[168:171], v[116:119]\n"
      "s_add_u32 s95, s35, 344064\n"
      "buffer_store_dwordx4 v[184:187], v72, s[36:39], s95 offen sc1\n"
      "v_mfma_f32_16x16x32_bf16 v[116:119], v[60:63], a[172:175], v[116:119]\n"
      "v_exp_f32 v198, -v198\n"
      "v_mfma_f32_16x16x32_bf16 v[120:123], v[56:59], a[176:179], v[120:123]\n"
      "ds_read_b64_tr_b16 v[52:53], v13 offset:8192\n"
      "ds_read_b64_tr_b16 v[54:55], v13 offset:8704\n"
      "v_mfma_f32_16x16x32_bf16 v[120:123], v[60:63], a[180:183], v[120:123]\n"
      "v_add_f32 v192, 1.0, v192\n"
      "v_add_f32 v193, 1.0, v193\n"
      "v_mfma_f32_16x16x32_bf16 v[124:127], v[56:59], a[184:187], v[124:127]\n"
      "v_rcp_f32 v192, v192\n"
      "v_mfma_f32_16x16x32_bf16 v[124:127], v[60:63], a[188:191], v[124:127]\n"
      "s_waitcnt lgkmcnt(8)\n"
      "buffer_load_dwordx4 a[140:143], v1, s[36:39], s93 offen offset:3072 sc1\n"
      "v_mfma_f32_16x16x32_bf16 v[96:99], v[32:35], a[192:195], v[96:99]\n"
      "ds_read_b64_tr_b16 v[56:57], v13 offset:4096\n"
      "ds_read_b64_tr_b16 v[58:59], v13 offset:4608\n"
      "v_mfma_f32_16x16x32_bf16 v[96:99], v[36:39], a[196:199], v[96:99]\n"
      "v_rcp_f32 v193, v193\n"
      "v_mfma_f32_16x16x32_bf16 v[100:103], v[32:35], a[200:203], v[100:103]\n"
      "s_add_u32 s93, s59, 36864\n"
      "v_add_f32 v194, 1.0, v194\n"
      "v_mfma_f32_16x16x32_bf16 v[100:103], v[36:39], a[204:207], v[100:103]\n"
      "v_rcp_f32 v194, v194\n"
      "v_mfma_f32_16x16x32_bf16 v[104:107], v[32:35], a[208:211], v[104:107]\n"
      "ds_read_b64_tr_b16 v[60:61], v13 offset:12288\n"
      "Lpad11_%=:\n"
      "s_cbranch_execz Lpad12_%=\n"
      "ds_read_b64_tr_b16 v[62:63], v13 offset:12800\n"
      "v_mfma_f32_16x16x32_bf16 v[104:107], v[36:39], a[212:215], v[104:107]\n"
      "buffer_load_dwordx4 a[144:147], v1, s[36:39], s93 offen sc1\n"
      "v_mul_f32 v192, v192, v208\n"
      "v_mfma_f32_16x16x32_bf16 v[108:111], v[32:35], a[216:219], v[108:111]\n"
      "v_exp_f32 v199, -v199\n"
      "v_mfma_f32_16x16x32_bf16 v[108:111], v[36:39], a[220:223], v[108:111]\n"
      "v_xor_b32 v11, 0x60, v9\n"
      "s_waitcnt lgkmcnt(8)\n"
      "v_mfma_f32_16x16x32_bf16 v[112:115], v[40:43], a[192:195], v[112:115]\n"
      "ds_read_b64_tr_b16 v[32:33], v11\n"
      "ds_read_b64_tr_b16 v[34:35], v11 offset:512\n"
      "v_mfma_f32_16x16x32_bf16 v[112:115], v[44:47], a[196:199], v[112:115]\n"
      "buffer_load_dwordx4 a[148:151], v1, s[36:39], s93 offen offset:1024 sc1\n"
      "v_add_f32 v195, 1.0, v195\n"
      "v_mfma_f32_16x16x32_bf16 v[116:119], v[40:43], a[200:203], v[116:119]\n"
      "v_rcp_f32 v195, v195\n"
      "v_mfma_f32_16x16x32_bf16 v[116:119], v[44:47], a[204:207], v[116:119]\n"
      "v_mul_f32 v193, v193, v209\n"
      "v_add_f32 v196, 1.0, v196\n"
      "v_mfma_f32_16x16x32_bf16 v[120:123], v[40:43], a[208:211], v[120:123]\n"
      "ds_read_b64_tr_b16 v[36:37], v11 offset:8192\n"
      "ds_read_b64_tr_b16 v[38:39], v11 offset:8704\n"
      "v_mfma_f32_16x16x32_bf16 v[120:123], v[44:47], a[212:215], v[120:123]\n"
      "v_rcp_f32 v196, v196\n"
      "v_mfma_f32_16x16x32_bf16 v[124:127], v[40:43], a[216:219], v[124:127]\n"
      "buffer_load_dwordx4 a[152:155], v1, s[36:39], s93 offen offset:2048 sc1\n"
      "v_mul_f32 v194, v194, v210\n"
      "v_mfma_f32_16x16x32_bf16 v[124:127], v[44:47], a[220:223], v[124:127]\n"
      "s_waitcnt lgkmcnt(8)\n"
      "v_add_f32 v197, 1.0, v197\n"
      "v_mfma_f32_16x16x32_bf16 v[96:99], v[48:51], a[224:227], v[96:99]\n"
      "ds_read_b64_tr_b16 v[40:41], v11 offset:4096\n"
      "ds_read_b64_tr_b16 v[42:43], v11 offset:4608\n"
      "v_mfma_f32_16x16x32_bf16 v[96:99], v[52:55], a[228:231], v[96:99]\n"
      "v_rcp_f32 v197, v197\n"
      "v_mfma_f32_16x16x32_bf16 v[100:103], v[48:51], a[232:235], v[100:103]\n"
      "v_mul_f32 v195, v195, v211\n"
      "v_add_f32 v198, 1.0, v198\n"
      "v_mfma_f32_16x16x32_bf16 v[100:103], v[52:55], a[236:239], v[100:103]\n"
      "v_rcp_f32 v198, v198\n"
      "v_mfma_f32_16x16x32_bf16 v[104:107], v[48:51], a[240:243], v[104:107]\n"
      "ds_read_b64_tr_b16 v[44:45], v11 offset:12288\n"
      "ds_read_b64_tr_b16 v[46:47], v11 offset:12800\n"
      "v_mfma_f32_16x16x32_bf16 v[104:107], v[52:55], a[244:247], v[104:107]\n"
      "buffer_load_dwordx4 a[156:159], v1, s[36:39], s93 offen offset:3072 sc1\n"
      "v_mul_f32 v196, v196, v212\n"
      "v_mfma_f32_16x16x32_bf16 v[108:111], v[48:51], a[248:251], v[108:111]\n"
      "v_exp_f32 v200, -v200\n"
      "v_mfma_f32_16x16x32_bf16 v[108:111], v[52:55], a[252:255], v[108:111]\n"
      "s_waitcnt lgkmcnt(8)\n"
      "v_add_f32 v199, 1.0, v199\n"
      "v_mfma_f32_16x16x32_bf16 v[112:115], v[56:59], a[224:227], v[112:115]\n"
      "s_barrier\n"
      "ds_read_b128 v[48:51], v1 offset:16384\n"
      "v_mfma_f32_16x16x32_bf16 v[112:115], v[60:63], a[228:231], v[112:115]\n"
      "s_add_u32 m0, s81, 98304\n"
      "s_add_u32 s96, s89, 81920\n"
      "v_mfma_f32_16x16x32_bf16 v[116:119], v[56:59], a[232:235], v[116:119]\n"
      "buffer_load_dwordx4 v1, s[36:39], s96 offen sc1 lds\n"
      "v_mul_f32 v197, v197, v213\n"
      "v_mfma_f32_16x16x32_bf16 v[116:119], v[60:63], a[236:239], v[116:119]\n"
      "v_rcp_f32 v199, v199\n"
      "v_mfma_f32_16x16x32_bf16 v[120:123], v[56:59], a[240:243], v[120:123]\n"
      "ds_read_b128 v[52:55], v1 offset:17408\n"
      "v_mul_f32 v198, v198, v214\n"
      "v_mfma_f32_16x16x32_bf16 v[120:123], v[60:63], a[244:247], v[120:123]\n"
      "v_exp_f32 v201, -v201\n"
      "v_mfma_f32_16x16x32_bf16 v[124:127], v[56:59], a[248:251], v[124:127]\n"
      "buffer_load_dwordx4 v1, s[36:39], s96 offen offset:1024 sc1 lds\n"
      "v_mul_f32 v199, v199, v215\n"
      "v_mfma_f32_16x16x32_bf16 v[124:127], v[60:63], a[252:255], v[124:127]\n"
      "s_waitcnt lgkmcnt(6)\n"
      "v_cvt_pk_bf16_f32 v208, v192, v193\n"
      "v_mfma_f32_16x16x32_bf16 v[96:99], v[32:35], v[224:227], v[96:99]\n"
      "ds_read_b128 v[56:59], v1 offset:18432\n"
      "v_cvt_pk_bf16_f32 v210, v196, v197\n"
      "v_mfma_f32_16x16x32_bf16 v[96:99], v[36:39], v[228:231], v[96:99]\n"
      "v_exp_f32 v202, -v202\n"
      "v_mfma_f32_16x16x32_bf16 v[100:103], v[32:35], v[232:235], v[100:103]\n"
      "v_exp_f32 v203, -v203\n"
      "v_mfma_f32_16x16x32_bf16 v[100:103], v[36:39], v[236:239], v[100:103]\n"
      "buffer_load_dwordx4 v1, s[36:39], s96 offen offset:2048 sc1 lds\n"
      "v_cvt_pk_bf16_f32 v209, v194, v195\n"
      "v_mfma_f32_16x16x32_bf16 v[104:107], v[32:35], v[240:243], v[104:107]\n"
      "ds_read_b128 v[60:63], v1 offset:19456\n"
      "v_cvt_pk_bf16_f32 v211, v198, v199\n"
      "v_mfma_f32_16x16x32_bf16 v[104:107], v[36:39], v[244:247], v[104:107]\n"
      "v_exp_f32 v204, -v204\n"
      "v_mfma_f32_16x16x32_bf16 v[108:111], v[32:35], v[248:251], v[108:111]\n"
      "v_exp_f32 v205, -v205\n"
      "v_mfma_f32_16x16x32_bf16 v[108:111], v[36:39], v[252:255], v[108:111]\n"
      "s_waitcnt lgkmcnt(4)\n"
      "v_permlane16_swap_b32 v208, v210\n"
      "v_mfma_f32_16x16x32_bf16 v[112:115], v[40:43], v[224:227], v[112:115]\n"
      "ds_read_b128 v[32:35], v1 offset:20480\n"
      "v_permlane16_swap_b32 v209, v211\n"
      "v_mfma_f32_16x16x32_bf16 v[112:115], v[44:47], v[228:231], v[112:115]\n"
      "buffer_load_dwordx4 v1, s[36:39], s96 offen offset:3072 sc1 lds\n"
      "v_add_f32 v200, 1.0, v200\n"
      "v_mfma_f32_16x16x32_bf16 v[116:119], v[40:43], v[232:235], v[116:119]\n"
      "s_add_u32 s95, s35, 57344\n"
      "buffer_store_dwordx4 v[208:211], v72, s[36:39], s95 offen sc1\n"
      "v_mfma_f32_16x16x32_bf16 v[116:119], v[44:47], v[236:239], v[116:119]\n"
      "v_rcp_f32 v200, v200\n"
      "v_mfma_f32_16x16x32_bf16 v[120:123], v[40:43], v[240:243], v[120:123]\n"
      "ds_read_b128 v[36:39], v1 offset:21504\n"
      "v_add_f32 v201, 1.0, v201\n"
      "v_mfma_f32_16x16x32_bf16 v[120:123], v[44:47], v[244:247], v[120:123]\n"
      "s_add_u32 m0, s81, 114688\n"
      "s_add_u32 s96, s89, 98304\n"
      "v_mfma_f32_16x16x32_bf16 v[124:127], v[40:43], v[248:251], v[124:127]\n"
      "buffer_load_dwordx4 v1, s[36:39], s96 offen sc1 lds\n"
      "v_add_f32 v202, 1.0, v202\n"
      "v_mfma_f32_16x16x32_bf16 v[124:127], v[44:47], v[252:255], v[124:127]\n"
      "s_waitcnt lgkmcnt(4)\n"
      "v_mul_f32 v200, v200, v216\n"
      "v_mfma_f32_16x16x32_bf16 v[128:131], v[48:51], a[0:3], 0\n"
      "ds_read_b128 v[40:43], v1 offset:22528\n"
      "ds_read_b128 v[44:47], v1 offset:23552\n"
      "v_mfma_f32_16x16x32_bf16 v[128:131], v[52:55], a[4:7], v[128:131]\n"
      "v_rcp_f32 v201, v201\n"
      "v_mfma_f32_16x16x32_bf16 v[132:135], v[48:51], a[8:11], 0\n"
      "v_rcp_f32 v202, v202\n"
      "v_mfma_f32_16x16x32_bf16 v[132:135], v[52:55], a[12:15], v[132:135]\n"
      "s_waitcnt lgkmcnt(4)\n"
      "buffer_load_dwordx4 v1, s[36:39], s96 offen offset:1024 sc1 lds\n"
      "v_mfma_f32_16x16x32_bf16 v[136:139], v[56:59], a[0:3], 0\n"
      "ds_read_b128 v[48:51], v1 offset:24576\n"
      "ds_read_b128 v[52:55], v1 offset:25600\n"
      "v_mfma_f32_16x16x32_bf16 v[136:139], v[60:63], a[4:7], v[136:139]\n"
      "v_exp_f32 v206, -v206\n"
      "v_mfma_f32_16x16x32_bf16 v[140:143], v[56:59], a[8:11], 0\n"
      "v_exp_f32 v207, -v207\n"
      "v_mfma_f32_16x16x32_bf16 v[140:143], v[60:63], a[12:15], v[140:143]\n"
      "s_waitcnt lgkmcnt(4)\n"
      "v_add_f32 v203, 1.0, v203\n"
      "v_mfma_f32_16x16x32_bf16 v[144:147], v[32:35], a[0:3], 0\n"
      "ds_read_b128 v[56:59], v1 offset:26624\n"
      "ds_read_b128 v[60:63], v1 offset:27648\n"
      "v_mfma_f32_16x16x32_bf16 v[144:147], v[36:39], a[4:7], v[144:147]\n"
      "buffer_load_dwordx4 v1, s[36:39], s96 offen offset:2048 sc1 lds\n"
      "v_mul_f32 v201, v201, v217\n"
      "v_mfma_f32_16x16x32_bf16 v[148:151], v[32:35], a[8:11], 0\n"
      "v_rcp_f32 v203, v203\n"
      "v_mfma_f32_16x16x32_bf16 v[148:151], v[36:39], a[12:15], v[148:151]\n"
      "s_waitcnt lgkmcnt(4)\n"
      "v_add_f32 v204, 1.0, v204\n"
      "v_mfma_f32_16x16x32_bf16 v[152:155], v[40:43], a[0:3], 0\n"
      "ds_read_b128 v[32:35], v1 offset:28672\n"
      "ds_read_b128 v[36:39], v1 offset:29696\n"
      "v_mfma_f32_16x16x32_bf16 v[152:155], v[44:47], a[4:7], v[152:155]\n"
      "v_rcp_f32 v204, v204\n"
      "v_mfma_f32_16x16x32_bf16 v[156:159], v[40:43], a[8:11], 0\n"
      "buffer_load_dwordx4 v1, s[36:39], s96 offen offset:3072 sc1 lds\n"
      "v_mul_f32 v202, v202, v218\n"
      "v_mfma_f32_16x16x32_bf16 v[156:159], v[44:47], a[12:15], v[156:159]\n"
      "s_waitcnt lgkmcnt(4)\n"
      "v_add_f32 v205, 1.0, v205\n"
      "v_mfma_f32_16x16x32_bf16 v[160:163], v[48:51], a[0:3], 0\n"
      "ds_read_b128 v[40:43], v1 offset:30720\n"
      "ds_read_b128 v[44:47], v1 offset:31744\n"
      "v_mfma_f32_16x16x32_bf16 v[160:163], v[52:55], a[4:7], v[160:163]\n"
      "s_add_u32 m0, s81, 131072\n"
      "s_add_u32 s96, s89, 114688\n"
      "v_mfma_f32_16x16x32_bf16 v[164:167], v[48:51], a[8:11], 0\n"
      "v_rcp_f32 v205, v205\n"
      "v_mfma_f32_16x16x32_bf16 v[164:167], v[52:55], a[12:15], v[164:167]\n"
      "s_waitcnt lgkmcnt(4)\n"
      "buffer_load_dwordx4 v1, s[36:39], s96 offen sc1 lds\n"
      "v_mfma_f32_16x16x32_bf16 v[168:171], v[56:59], a[0:3], 0\n"
      "ds_read_b128 v[48:51], v1 offset:32768\n"
      "ds_read_b128 v[52:55], v1 offset:33792\n"
      "v_mfma_f32_16x16x32_bf16 v[168:171], v[60:63], a[4:7], v[168:171]\n"
      "v_mul_f32 v203, v203, v219\n"
      "v_add_f32 v206, 1.0, v206\n"
      "v_mfma_f32_16x16x32_bf16 v[172:175], v[56:59], a[8:11], 0\n"
      "v_rcp_f32 v206, v206\n"
      "v_mfma_f32_16x16x32_bf16 v[172:175], v[60:63], a[12:15], v[172:175]\n"
      "s_waitcnt lgkmcnt(4)\n"
      "v_mul_f32 v204, v204, v220\n"
      "v_mfma_f32_16x16x32_bf16 v[128:131], v[32:35], a[16:19], v[128:131]\n"
      "ds_read_b128 v[56:59], v1 offset:34816\n"
      "ds_read_b128 v[60:63], v1 offset:35840\n"
      "v_mfma_f32_16x16x32_bf16 v[128:131], v[36:39], a[20:23], v[128:131]\n"
      "s_waitcnt vmcnt(15)\n"
      "buffer_store_dwordx2 v[74:75], v76, s[36:39], s58 offen offset:640 sc1\n"
      "v_mfma_f32_16x16x32_bf16 v[132:135], v[32:35], a[24:27], v[132:135]\n"
      "buffer_load_dwordx4 v1, s[36:39], s96 offen offset:1024 sc1 lds\n"
      "v_add_f32 v207, 1.0, v207\n"
      "v_mfma_f32_16x16x32_bf16 v[132:135], v[36:39], a[28:31], v[132:135]\n"
      "s_waitcnt lgkmcnt(4)\n"
      "v_mul_f32 v205, v205, v221\n"
      "v_mfma_f32_16x16x32_bf16 v[136:139], v[40:43], a[16:19], v[136:139]\n"
      "ds_read_b128 v[32:35], v1 offset:36864\n"
      "ds_read_b128 v[36:39], v1 offset:37888\n"
      "v_mfma_f32_16x16x32_bf16 v[136:139], v[44:47], a[20:23], v[136:139]\n"
      "buffer_load_dwordx4 v1, s[36:39], s96 offen offset:2048 sc1 lds\n"
      "v_mul_f32 v206, v206, v222\n"
      "v_mfma_f32_16x16x32_bf16 v[140:143], v[40:43], a[24:27], v[140:143]\n"
      "v_rcp_f32 v207, v207\n"
      "v_mfma_f32_16x16x32_bf16 v[140:143], v[44:47], a[28:31], v[140:143]\n"
      "s_waitcnt lgkmcnt(4)\n"
      "v_mul_f32 v207, v207, v223\n"
      "v_mfma_f32_16x16x32_bf16 v[144:147], v[48:51], a[16:19], v[144:147]\n"
      "ds_read_b128 v[40:43], v1 offset:38912\n"
      "ds_read_b128 v[44:47], v1 offset:39936\n"
      "v_mfma_f32_16x16x32_bf16 v[144:147], v[52:55], a[20:23], v[144:147]\n"
      "v_exp_f32 v96, -v96\n"
      "v_mfma_f32_16x16x32_bf16 v[148:151], v[48:51], a[24:27], v[148:151]\n"
      "v_exp_f32 v97, -v97\n"
      "v_mfma_f32_16x16x32_bf16 v[148:151], v[52:55], a[28:31], v[148:151]\n"
      "s_waitcnt lgkmcnt(4)\n"
      "buffer_load_dwordx4 v1, s[36:39], s96 offen offset:3072 sc1 lds\n"
      "v_mfma_f32_16x16x32_bf16 v[152:155], v[56:59], a[16:19], v[152:155]\n"
      "ds_read_b128 v[48:51], v1 offset:40960\n"
      "ds_read_b128 v[52:55], v1 offset:41984\n"
      "v_mfma_f32_16x16x32_bf16 v[152:155], v[60:63], a[20:23], v[152:155]\n"
      "v_exp_f32 v98, -v98\n"
      "v_mfma_f32_16x16x32_bf16 v[156:159], v[56:59], a[24:27], v[156:159]\n"
      "s_add_u32 m0, s81, 147456\n"
      "s_add_u32 s96, s89, 131072\n"
      "v_mfma_f32_16x16x32_bf16 v[156:159], v[60:63], a[28:31], v[156:159]\n"
      "s_waitcnt lgkmcnt(4)\n"
      "v_cvt_pk_bf16_f32 v216, v200, v201\n"
      "v_mfma_f32_16x16x32_bf16 v[160:163], v[32:35], a[16:19], v[160:163]\n"
      "ds_read_b128 v[56:59], v1 offset:43008\n"
      "ds_read_b128 v[60:63], v1 offset:44032\n"
      "v_mfma_f32_16x16x32_bf16 v[160:163], v[36:39], a[20:23], v[160:163]\n"
      "buffer_load_dwordx4 v1, s[36:39], s96 offen sc1 lds\n"
      "v_cvt_pk_bf16_f32 v218, v204, v205\n"
      "v_mfma_f32_16x16x32_bf16 v[164:167], v[32:35], a[24:27], v[164:167]\n"
      "v_exp_f32 v99, -v99\n"
      "v_mfma_f32_16x16x32_bf16 v[164:167], v[36:39], a[28:31], v[164:167]\n"
      "s_waitcnt lgkmcnt(4)\n"
      "v_cvt_pk_bf16_f32 v217, v202, v203\n"
      "v_mfma_f32_16x16x32_bf16 v[168:171], v[40:43], a[16:19], v[168:171]\n"
      "ds_read_b128 v[32:35], v1 offset:45056\n"
      "ds_read_b128 v[36:39], v1 offset:46080\n"
      "v_mfma_f32_16x16x32_bf16 v[168:171], v[44:47], a[20:23], v[168:171]\n"
      "buffer_load_dwordx4 v1, s[36:39], s96 offen offset:1024 sc1 lds\n"
      "v_cvt_pk_bf16_f32 v219, v206, v207\n"
      "v_mfma_f32_16x16x32_bf16 v[172:175], v[40:43], a[24:27], v[172:175]\n"
      "v_exp_f32 v100, -v100\n"
      "v_mfma_f32_16x16x32_bf16 v[172:175], v[44:47], a[28:31], v[172:175]\n"
      "s_waitcnt lgkmcnt(4)\n"
      "v_permlane16_swap_b32 v216, v218\n"
      "v_mfma_f32_16x16x32_bf16 v[128:131], v[48:51], a[32:35], v[128:131]\n"
      "ds_read_b128 v[40:43], v1 offset:47104\n"
      "ds_read_b128 v[44:47], v1 offset:48128\n"
      "v_mfma_f32_16x16x32_bf16 v[128:131], v[52:55], a[36:39], v[128:131]\n"
      "v_exp_f32 v101, -v101\n"
      "v_mfma_f32_16x16x32_bf16 v[132:135], v[48:51], a[40:43], v[132:135]\n"
      "buffer_load_dwordx4 v1, s[36:39], s96 offen offset:2048 sc1 lds\n"
      "v_permlane16_swap_b32 v217, v219\n"
      "v_mfma_f32_16x16x32_bf16 v[132:135], v[52:55], a[44:47], v[132:135]\n"
      "s_waitcnt lgkmcnt(4)\n"
      "v_add_f32 v96, 1.0, v96\n"
      "v_mfma_f32_16x16x32_bf16 v[136:139], v[56:59], a[32:35], v[136:139]\n"
      "s_barrier\n"
      "ds_read_b128 v[48:51], v1 offset:49152\n"
      "ds_read_b128 v[52:55], v1 offset:50176\n"
      "v_mfma_f32_16x16x32_bf16 v[136:139], v[60:63], a[36:39], v[136:139]\n"
      "s_add_u32 s95, s35, 352256\n"
      "buffer_store_dwordx4 v[216:219], v72, s[36:39], s95 offen sc1\n"
      "v_mfma_f32_16x16x32_bf16 v[140:143], v[56:59], a[40:43], v[140:143]\n"
      "v_rcp_f32 v96, v96\n"
      "v_mfma_f32_16x16x32_bf16 v[140:143], v[60:63], a[44:47], v[140:143]\n"
      "s_waitcnt lgkmcnt(4)\n"
      "v_add_f32 v97, 1.0, v97\n"
      "v_mfma_f32_16x16x32_bf16 v[144:147], v[32:35], a[32:35], v[144:147]\n"
      "ds_read_b128 v[56:59], v1 offset:51200\n"
      "ds_read_b128 v[60:63], v1 offset:52224\n"
      "v_mfma_f32_16x16x32_bf16 v[144:147], v[36:39], a[36:39], v[144:147]\n"
      "buffer_load_dwordx4 v1, s[36:39], s96 offen offset:3072 sc1 lds\n"
      "v_add_f32 v98, 1.0, v98\n"
      "v_mfma_f32_16x16x32_bf16 v[148:151], v[32:35], a[40:43], v[148:151]\n"
      "v_rcp_f32 v97, v97\n"
      "v_mfma_f32_16x16x32_bf16 v[148:151], v[36:39], a[44:47], v[148:151]\n"
      "s_waitcnt lgkmcnt(4)\n"
      "v_mul_f32 v96, v96, v112\n"
      "v_mfma_f32_16x16x32_bf16 v[152:155], v[40:43], a[32:35], v[152:155]\n"
      "ds_read_b128 v[32:35], v1 offset:53248\n"
      "ds_read_b128 v[36:39], v1 offset:54272\n"
      "v_mfma_f32_16x16x32_bf16 v[152:155], v[44:47], a[36:39], v[152:155]\n"
      "s_add_u32 m0, s81, 0\n"
      "s_add_u32 s96, s89, 147456\n"
      "v_mfma_f32_16x16x32_bf16 v[156:159], v[40:43], a[40:43], v[156:159]\n"
      "buffer_load_dwordx4 v1, s[36:39], s96 offen sc1 lds\n"
      "v_add_f32 v99, 1.0, v99\n"
      "v_mfma_f32_16x16x32_bf16 v[156:159], v[44:47], a[44:47], v[156:159]\n"
      "s_waitcnt lgkmcnt(4)\n"
      "v_mul_f32 v97, v97, v113\n"
      "v_mfma_f32_16x16x32_bf16 v[160:163], v[48:51], a[32:35], v[160:163]\n"
      "ds_read_b128 v[40:43], v1 offset:55296\n"
      "ds_read_b128 v[44:47], v1 offset:56320\n"
      "v_mfma_f32_16x16x32_bf16 v[160:163], v[52:55], a[36:39], v[160:163]\n"
      "v_rcp_f32 v98, v98\n"
      "v_mfma_f32_16x16x32_bf16 v[164:167], v[48:51], a[40:43], v[164:167]\n"
      "buffer_load_dwordx4 v1, s[36:39], s96 offen offset:1024 sc1 lds\n"
      "v_add_f32 v100, 1.0, v100\n"
      "v_mfma_f32_16x16x32_bf16 v[164:167], v[52:55], a[44:47], v[164:167]\n"
      "s_waitcnt lgkmcnt(4)\n"
      "v_mul_f32 v98, v98, v114\n"
      "v_mfma_f32_16x16x32_bf16 v[168:171], v[56:59], a[32:35], v[168:171]\n"
      "ds_read_b128 v[48:51], v1 offset:57344\n"
      "ds_read_b128 v[52:55], v1 offset:58368\n"
      "v_mfma_f32_16x16x32_bf16 v[168:171], v[60:63], a[36:39], v[168:171]\n"
      "v_rcp_f32 v99, v99\n"
      "v_mfma_f32_16x16x32_bf16 v[172:175], v[56:59], a[40:43], v[172:175]\n"
      "v_exp_f32 v102, -v102\n"
      "v_mfma_f32_16x16x32_bf16 v[172:175], v[60:63], a[44:47], v[172:175]\n"
      "s_waitcnt lgkmcnt(4)\n"
      "buffer_load_dwordx4 v1, s[36:39], s96 offen offset:2048 sc1 lds\n"
      "v_mfma_f32_16x16x32_bf16 v[128:131], v[32:35], a[48:51], v[128:131]\n"
      "ds_read_b128 v[56:59], v1 offset:59392\n"
      "ds_read_b128 v[60:63], v1 offset:60416\n"
      "v_mfma_f32_16x16x32_bf16 v[128:131], v[36:39], a[52:55], v[128:131]\n"
      "v_rcp_f32 v100, v100\n"
      "v_mfma_f32_16x16x32_bf16 v[132:135], v[32:35], a[56:59], v[132:135]\n"
      "v_exp_f32 v103, -v103\n"
      "v_mfma_f32_16x16x32_bf16 v[132:135], v[36:39], a[60:63], v[132:135]\n"
      "s_waitcnt lgkmcnt(4)\n"
      "v_add_f32 v101, 1.0, v101\n"
      "v_mfma_f32_16x16x32_bf16 v[136:139], v[40:43], a[48:51], v[136:139]\n"
      "ds_read_b128 v[32:35], v1 offset:61440\n"
      "ds_read_b128 v[36:39], v1 offset:62464\n"
      "v_mfma_f32_16x16x32_bf16 v[136:139], v[44:47], a[52:55], v[136:139]\n"
      "buffer_load_dwordx4 v1, s[36:39], s96 offen offset:3072 sc1 lds\n"
      "v_mul_f32 v99, v99, v115\n"
      "v_mfma_f32_16x16x32_bf16 v[140:143], v[40:43], a[56:59], v[140:143]\n"
      "v_rcp_f32 v101, v101\n"
      "v_mfma_f32_16x16x32_bf16 v[140:143], v[44:47], a[60:63], v[140:143]\n"
      "s_waitcnt lgkmcnt(4)\n"
      "v_add_f32 v102, 1.0, v102\n"
      "v_mfma_f32_16x16x32_bf16 v[144:147], v[48:51], a[48:51], v[144:147]\n"
      "ds_read_b128 v[40:43], v1 offset:63488\n"
      "ds_read_b128 v[44:47], v1 offset:64512\n"
      "v_mfma_f32_16x16x32_bf16 v[144:147], v[52:55], a[52:55], v[144:147]\n"
      "s_add_u32 m0, s81, 16384\n"
      "s_add_u32 s96, s89, 163840\n"
      "v_mfma_f32_16x16x32_bf16 v[148:151], v[48:51], a[56:59], v[148:151]\n"
      "buffer_load_dwordx4 v1, s[36:39], s96 offen sc1 lds\n"
      "v_mul_f32 v100, v100, v116\n"
      "v_mfma_f32_16x16x32_bf16 v[148:151], v[52:55], a[60:63], v[148:151]\n"
      "s_waitcnt lgkmcnt(4)\n"
      "v_add_f32 v103, 1.0, v103\n"
      "v_mfma_f32_16x16x32_bf16 v[152:155], v[56:59], a[48:51], v[152:155]\n"
      "ds_read_b128 v[48:51], v28\n"
      "ds_read_b128 v[52:55], v28 offset:1024\n"
      "v_mfma_f32_16x16x32_bf16 v[152:155], v[60:63], a[52:55], v[152:155]\n"
      "v_rcp_f32 v102, v102\n"
      "v_mfma_f32_16x16x32_bf16 v[156:159], v[56:59], a[56:59], v[156:159]\n"
      "v_rcp_f32 v103, v103\n"
      "v_mfma_f32_16x16x32_bf16 v[156:159], v[60:63], a[60:63], v[156:159]\n"
      "s_waitcnt lgkmcnt(4)\n"
      "buffer_load_dwordx4 v1, s[36:39], s96 offen offset:1024 sc1 lds\n"
      "v_mfma_f32_16x16x32_bf16 v[160:163], v[32:35], a[48:51], v[160:163]\n"
      "ds_read_b128 v[56:59], v28 offset:2048\n"
      "ds_read_b128 v[60:63], v28 offset:3072\n"
      "v_mfma_f32_16x16x32_bf16 v[160:163], v[36:39], a[52:55], v[160:163]\n"
      "v_exp_f32 v104, -v104\n"
      "v_mfma_f32_16x16x32_bf16 v[164:167], v[32:35], a[56:59], v[164:167]\n"
      "v_exp_f32 v105, -v105\n"
      "v_mfma_f32_16x16x32_bf16 v[164:167], v[36:39], a[60:63], v[164:167]\n"
      "s_waitcnt lgkmcnt(4)\n"
      "v_mul_f32 v101, v101, v117\n"
      "v_mfma_f32_16x16x32_bf16 v[168:171], v[40:43], a[48:51], v[168:171]\n"
      "ds_read_b128 v[32:35], v28 offset:4096\n"
      "ds_read_b128 v[36:39], v28 offset:5120\n"
      "v_mfma_f32_16x16x32_bf16 v[168:171], v[44:47], a[52:55], v[168:171]\n"
      "buffer_load_dwordx4 v1, s[36:39], s96 offen offset:2048 sc1 lds\n"
      "v_mul_f32 v102, v102, v118\n"
      "v_mfma_f32_16x16x32_bf16 v[172:175], v[40:43], a[56:59], v[172:175]\n"
      "v_exp_f32 v106, -v106\n"
      "v_mfma_f32_16x16x32_bf16 v[172:175], v[44:47], a[60:63], v[172:175]\n"
      "s_waitcnt lgkmcnt(4)\n"
      "v_mul_f32 v103, v103, v119\n"
      "v_mfma_f32_16x16x32_bf16 v[128:131], v[48:51], a[64:67], v[128:131]\n"
      "ds_read_b128 v[40:43], v28 offset:6144\n"
      "ds_read_b128 v[44:47], v28 offset:7168\n"
      "v_mfma_f32_16x16x32_bf16 v[128:131], v[52:55], a[68:71], v[128:131]\n"
      "v_exp_f32 v107, -v107\n"
      "v_mfma_f32_16x16x32_bf16 v[132:135], v[48:51], a[72:75], v[132:135]\n"
      "buffer_load_dwordx4 v1, s[36:39], s96 offen offset:3072 sc1 lds\n"
      "v_cvt_pk_bf16_f32 v112, v96, v97\n"
      "v_mfma_f32_16x16x32_bf16 v[132:135], v[52:55], a[76:79], v[132:135]\n"
      "s_waitcnt lgkmcnt(4)\n"
      "v_cvt_pk_bf16_f32 v114, v100, v101\n"
      "v_mfma_f32_16x16x32_bf16 v[136:139], v[56:59], a[64:67], v[136:139]\n"
      "ds_read_b128 v[48:51], v28 offset:8192\n"
      "ds_read_b128 v[52:55], v28 offset:9216\n"
      "v_mfma_f32_16x16x32_bf16 v[136:139], v[60:63], a[68:71], v[136:139]\n"
      "v_exp_f32 v108, -v108\n"
      "v_mfma_f32_16x16x32_bf16 v[140:143], v[56:59], a[72:75], v[140:143]\n"
      "v_cvt_pk_bf16_f32 v113, v98, v99\n"
      "v_cvt_pk_bf16_f32 v115, v102, v103\n"
      "v_mfma_f32_16x16x32_bf16 v[140:143], v[60:63], a[76:79], v[140:143]\n"
      "s_waitcnt lgkmcnt(4)\n"
      "v_permlane16_swap_b32 v112, v114\n"
      "v_mfma_f32_16x16x32_bf16 v[144:147], v[32:35], a[64:67], v[144:147]\n"
      "ds_read_b128 v[56:59], v28 offset:10240\n"
      "ds_read_b128 v[60:63], v28 offset:11264\n"
      "v_mfma_f32_16x16x32_bf16 v[144:147], v[36:39], a[68:71], v[144:147]\n"
      "v_exp_f32 v109, -v109\n"
      "v_mfma_f32_16x16x32_bf16 v[148:151], v[32:35], a[72:75], v[148:151]\n"
      "v_permlane16_swap_b32 v113, v115\n"
      "v_add_f32 v104, 1.0, v104\n"
      "v_mfma_f32_16x16x32_bf16 v[148:151], v[36:39], a[76:79], v[148:151]\n"
      "s_waitcnt lgkmcnt(4)\n"
      "v_add_f32 v105, 1.0, v105\n"
      "v_mfma_f32_16x16x32_bf16 v[152:155], v[40:43], a[64:67], v[152:155]\n"
      "ds_read_b128 v[32:35], v28 offset:12288\n"
      "ds_read_b128 v[36:39], v28 offset:13312\n"
      "v_mfma_f32_16x16x32_bf16 v[152:155], v[44:47], a[68:71], v[152:155]\n"
      "s_add_u32 s95, s35, 65536\n"
      "buffer_store_dwordx4 v[112:115], v72, s[36:39], s95 offen sc1\n"
      "v_mfma_f32_16x16x32_bf16 v[156:159], v[40:43], a[72:75], v[156:159]\n"
      "v_rcp_f32 v104, v104\n"
      "v_mfma_f32_16x16x32_bf16 v[156:159], v[44:47], a[76:79], v[156:159]\n"
      "s_waitcnt lgkmcnt(4)\n"
      "v_add_f32 v106, 1.0, v106\n"
      "v_mfma_f32_16x16x32_bf16 v[160:163], v[48:51], a[64:67], v[160:163]\n"
      "ds_read_b128 v[40:43], v28 offset:14336\n"
      "ds_read_b128 v[44:47], v28 offset:15360\n"
      "v_mfma_f32_16x16x32_bf16 v[160:163], v[52:55], a[68:71], v[160:163]\n"
      "v_rcp_f32 v105, v105\n"
      "v_mfma_f32_16x16x32_bf16 v[164:167], v[48:51], a[72:75], v[164:167]\n"
      "v_rcp_f32 v106, v106\n"
      "v_mfma_f32_16x16x32_bf16 v[164:167], v[52:55], a[76:79], v[164:167]\n"
      "s_waitcnt lgkmcnt(4)\n"
      "v_mul_f32 v104, v104, v120\n"
      "v_mfma_f32_16x16x32_bf16 v[168:171], v[56:59], a[64:67], v[168:171]\n"
      "s_waitcnt vmcnt(24)\n"
      "s_barrier\n"
      "ds_read_b128 v[48:51], v28 offset:16384\n"
      "ds_read_b128 v[52:55], v28 offset:17408\n"
      "v_mfma_f32_16x16x32_bf16 v[168:171], v[60:63], a[68:71], v[168:171]\n"
      "s_add_u32 m0, s81, 32768\n"
      "s_add_u32 s96, s89, 180224\n"
      "v_mfma_f32_16x16x32_bf16 v[172:175], v[56:59], a[72:75], v[172:175]\n"
      "buffer_load_dwordx4 v1, s[36:39], s96 offen sc1 lds\n"
      "v_add_f32 v107, 1.0, v107\n"
      "v_mfma_f32_16x16x32_bf16 v[172:175], v[60:63], a[76:79], v[172:175]\n"
      "s_waitcnt lgkmcnt(4)\n"
      "v_mul_f32 v105, v105, v121\n"
      "v_mfma_f32_16x16x32_bf16 v[128:131], v[32:35], a[80:83], v[128:131]\n"
      "ds_read_b128 v[56:59], v28 offset:18432\n"
      "ds_read_b128 v[60:63], v28 offset:19456\n"
      "v_mfma_f32_16x16x32_bf16 v[128:131], v[36:39], a[84:87], v[128:131]\n"
      "buffer_load_dwordx4 v1, s[36:39], s96 offen offset:1024 sc1 lds\n"
      "v_add_f32 v108, 1.0, v108\n"
      "v_mfma_f32_16x16x32_bf16 v[132:135], v[32:35], a[88:91], v[132:135]\n"
      "v_rcp_f32 v107, v107\n"
      "v_mfma_f32_16x16x32_bf16 v[132:135], v[36:39], a[92:95], v[132:135]\n"
      "s_waitcnt lgkmcnt(4)\n"
      "v_mul_f32 v106, v106, v122\n"
      "v_mfma_f32_16x16x32_bf16 v[136:139], v[40:43], a[80:83], v[136:139]\n"
      "ds_read_b128 v[32:35], v28 offset:20480\n"
      "ds_read_b128 v[36:39], v28 offset:21504\n"
      "v_mfma_f32_16x16x32_bf16 v[136:139], v[44:47], a[84:87], v[136:139]\n"
      "v_exp_f32 v110, -v110\n"
      "v_mfma_f32_16x16x32_bf16 v[140:143], v[40:43], a[88:91], v[140:143]\n"
      "buffer_load_dwordx4 v1, s[36:39], s96 offen offset:2048 sc1 lds\n"
      "v_add_f32 v109, 1.0, v109\n"
      "v_mfma_f32_16x16x32_bf16 v[140:143], v[44:47], a[92:95], v[140:143]\n"
      "s_waitcnt lgkmcnt(4)\n"
      "v_mul_f32 v107, v107, v123\n"
      "v_mfma_f32_16x16x32_bf16 v[144:147], v[48:51], a[80:83], v[144:147]\n"
      "ds_read_b128 v[40:43], v28 offset:22528\n"
      "ds_read_b128 v[44:47], v28 offset:23552\n"
      "v_mfma_f32_16x16x32_bf16 v[144:147], v[52:55], a[84:87], v[144:147]\n"
      "v_rcp_f32 v108, v108\n"
      "v_mfma_f32_16x16x32_bf16 v[148:151], v[48:51], a[88:91], v[148:151]\n"
      "v_exp_f32 v111, -v111\n"
      "v_mfma_f32_16x16x32_bf16 v[148:151], v[52:55], a[92:95], v[148:151]\n"
      "s_waitcnt lgkmcnt(4)\n"
      "v_add_f32 v110, 1.0, v110\n"
      "v_mfma_f32_16x16x32_bf16 v[152:155], v[56:59], a[80:83], v[152:155]\n"
      "ds_read_b128 v[48:51], v28 offset:24576\n"
      "ds_read_b128 v[52:55], v28 offset:25600\n"
      "v_mfma_f32_16x16x32_bf16 v[152:155], v[60:63], a[84:87], v[152:155]\n"
      "buffer_load_dwordx4 v1, s[36:39], s96 offen offset:3072 sc1 lds\n"
      "v_mul_f32 v108, v108, v124\n"
      "v_mfma_f32_16x16x32_bf16 v[156:159], v[56:59], a[88:91], v[156:159]\n"
      "v_rcp_f32 v109, v109\n"
      "v_mfma_f32_16x16x32_bf16 v[156:159], v[60:63], a[92:95], v[156:159]\n"
      "s_waitcnt lgkmcnt(4)\n"
      "v_add_f32 v111, 1.0, v111\n"
      "v_mfma_f32_16x16x32_bf16 v[160:163], v[32:35], a[80:83], v[160:163]\n"
      "ds_read_b128 v[56:59], v28 offset:26624\n"
      "ds_read_b128 v[60:63], v28 offset:27648\n"
      "v_mfma_f32_16x16x32_bf16 v[160:163], v[36:39], a[84:87], v[160:163]\n"
      "s_add_u32 m0, s81, 49152\n"
      "s_add_u32 s96, s89, 196608\n"
      "v_mfma_f32_16x16x32_bf16 v[164:167], v[32:35], a[88:91], v[164:167]\n"
      "buffer_load_dwordx4 v1, s[36:39], s96 offen sc1 lds\n"
      "v_mul_f32 v109, v109, v125\n"
      "v_mfma_f32_16x16x32_bf16 v[164:167], v[36:39], a[92:95], v[164:167]\n"
      "s_waitcnt lgkmcnt(4)\n"
      "v_mfma_f32_16x16x32_bf16 v[168:171], v[40:43], a[80:83], v[168:171]\n"
      "ds_read_b128 v[32:35], v28 offset:28672\n"
      "ds_read_b128 v[36:39], v28 offset:29696\n"
      "v_mfma_f32_16x16x32_bf16 v[168:171], v[44:47], a[84:87], v[168:171]\n"
      "v_rcp_f32 v110, v110\n"
      "v_mfma_f32_16x16x32_bf16 v[172:175], v[40:43], a[88:91], v[172:175]\n"
      "buffer_load_dwordx4 v1, s[36:39], s96 offen offset:1024 sc1 lds\n"
      "v_mul_f32 v110, v110, v126\n"
      "v_mfma_f32_16x16x32_bf16 v[172:175], v[44:47], a[92:95], v[172:175]\n"
      "s_waitcnt lgkmcnt(4)\n"
      "v_mfma_f32_16x16x32_bf16 v[128:131], v[48:51], a[96:99], v[128:131]\n"
      "ds_read_b128 v[40:43], v28 offset:30720\n"
      "ds_read_b128 v[44:47], v28 offset:31744\n"
      "v_mfma_f32_16x16x32_bf16 v[128:131], v[52:55], a[100:103], v[128:131]\n"
      "buffer_load_dwordx2 v[70:71], v23, s[36:39], 0 offen offset:640 sc1\n"
      "v_mfma_f32_16x16x32_bf16 v[132:135], v[48:51], a[104:107], v[132:135]\n"
      "v_rcp_f32 v111, v111\n"
      "v_mfma_f32_16x16x32_bf16 v[132:135], v[52:55], a[108:111], v[132:135]\n"
      "s_waitcnt lgkmcnt(4)\n"
      "buffer_load_dwordx4 v1, s[36:39], s96 offen offset:2048 sc1 lds\n"
      "v_mfma_f32_16x16x32_bf16 v[136:139], v[56:59], a[96:99], v[136:139]\n"
      "ds_read_b128 v[48:51], v28 offset:32768\n"
      "ds_read_b128 v[52:55], v28 offset:33792\n"
      "v_mfma_f32_16x16x32_bf16 v[136:139], v[60:63], a[100:103], v[136:139]\n"
      "v_mul_f32 v111, v111, v127\n"
      "v_mfma_f32_16x16x32_bf16 v[140:143], v[56:59], a[104:107], v[140:143]\n"
      "v_mfma_f32_16x16x32_bf16 v[140:143], v[60:63], a[108:111], v[140:143]\n"
      "s_waitcnt lgkmcnt(4)\n"
      "v_cvt_pk_bf16_f32 v120, v104, v105\n"
      "v_mfma_f32_16x16x32_bf16 v[144:147], v[32:35], a[96:99], v[144:147]\n"
      "ds_read_b128 v[56:59], v28 offset:34816\n"
      "ds_read_b128 v[60:63], v28 offset:35840\n"
      "v_mfma_f32_16x16x32_bf16 v[144:147], v[36:39], a[100:103], v[144:147]\n"
      "buffer_load_dwordx4 v1, s[36:39], s96 offen offset:3072 sc1 lds\n"
      "v_cvt_pk_bf16_f32 v122, v108, v109\n"
      "v_mfma_f32_16x16x32_bf16 v[148:151], v[32:35], a[104:107], v[148:151]\n"
      "v_cvt_pk_bf16_f32 v121, v106, v107\n"
      "v_cvt_pk_bf16_f32 v123, v110, v111\n"
      "v_mfma_f32_16x16x32_bf16 v[148:151], v[36:39], a[108:111], v[148:151]\n"
      "s_waitcnt lgkmcnt(4)\n"
      "v_permlane16_swap_b32 v120, v122\n"
      "v_mfma_f32_16x16x32_bf16 v[152:155], v[40:43], a[96:99], v[152:155]\n"
      "ds_read_b128 v[32:35], v28 offset:36864\n"
      "ds_read_b128 v[36:39], v28 offset:37888\n"
      "v_mfma_f32_16x16x32_bf16 v[152:155], v[44:47], a[100:103], v[152:155]\n"
      "v_permlane16_swap_b32 v121, v123\n"
      "v_mfma_f32_16x16x32_bf16 v[156:159], v[40:43], a[104:107], v[156:159]\n"
      "s_add_u32 s95, s35, 360448\n"
      "buffer_store_dwordx4 v[120:123], v72, s[36:39], s95 offen sc1\n"
      "v_mfma_f32_16x16x32_bf16 v[156:159], v[44:47], a[108:111], v[156:159]\n"
      "s_waitcnt lgkmcnt(4)\n"
      "v_mfma_f32_16x16x32_bf16 v[160:163], v[48:51], a[96:99], v[160:163]\n"
      "ds_read_b128 v[40:43], v28 offset:38912\n"
      "ds_read_b128 v[44:47], v28 offset:39936\n"
      "v_mfma_f32_16x16x32_bf16 v[160:163], v[52:55], a[100:103], v[160:163]\n"
      "v_mfma_f32_16x16x32_bf16 v[164:167], v[48:51], a[104:107], v[164:167]\n"
      "v_mfma_f32_16x16x32_bf16 v[164:167], v[52:55], a[108:111], v[164:167]\n"
      "s_waitcnt lgkmcnt(4)\n"
      "v_mfma_f32_16x16x32_bf16 v[168:171], v[56:59], a[96:99], v[168:171]\n"
      "ds_read_b128 v[48:51], v28 offset:40960\n"
      "ds_read_b128 v[52:55], v28 offset:41984\n"
      "v_mfma_f32_16x16x32_bf16 v[168:171], v[60:63], a[100:103], v[168:171]\n"
      "v_mfma_f32_16x16x32_bf16 v[172:175], v[56:59], a[104:107], v[172:175]\n"
      "v_mfma_f32_16x16x32_bf16 v[172:175], v[60:63], a[108:111], v[172:175]\n"
      "s_waitcnt lgkmcnt(4)\n"
      "v_mfma_f32_16x16x32_bf16 v[128:131], v[32:35], a[112:115], v[128:131]\n"
      "ds_read_b128 v[56:59], v28 offset:43008\n"
      "ds_read_b128 v[60:63], v28 offset:44032\n"
      "v_mfma_f32_16x16x32_bf16 v[128:131], v[36:39], a[116:119], v[128:131]\n"
      "v_mfma_f32_16x16x32_bf16 v[132:135], v[32:35], a[120:123], v[132:135]\n"
      "v_mfma_f32_16x16x32_bf16 v[132:135], v[36:39], a[124:127], v[132:135]\n"
      "s_waitcnt lgkmcnt(4)\n"
      "v_mfma_f32_16x16x32_bf16 v[136:139], v[40:43], a[112:115], v[136:139]\n"
      "ds_read_b128 v[32:35], v28 offset:45056\n"
      "ds_read_b128 v[36:39], v28 offset:46080\n"
      "v_mfma_f32_16x16x32_bf16 v[136:139], v[44:47], a[116:119], v[136:139]\n"
      "v_mfma_f32_16x16x32_bf16 v[140:143], v[40:43], a[120:123], v[140:143]\n"
      "v_mfma_f32_16x16x32_bf16 v[140:143], v[44:47], a[124:127], v[140:143]\n"
      "s_waitcnt lgkmcnt(4)\n"
      "v_mfma_f32_16x16x32_bf16 v[144:147], v[48:51], a[112:115], v[144:147]\n"
      "ds_read_b128 v[40:43], v28 offset:47104\n"
      "ds_read_b128 v[44:47], v28 offset:48128\n"
      "v_mfma_f32_16x16x32_bf16 v[144:147], v[52:55], a[116:119], v[144:147]\n"
      "v_mfma_f32_16x16x32_bf16 v[148:151], v[48:51], a[120:123], v[148:151]\n"
      "v_mfma_f32_16x16x32_bf16 v[148:151], v[52:55], a[124:127], v[148:151]\n"
      "s_waitcnt lgkmcnt(4)\n"
      "v_mfma_f32_16x16x32_bf16 v[152:155], v[56:59], a[112:115], v[152:155]\n"
      "s_waitcnt vmcnt(24)\n"
      "s_barrier\n"
      "ds_read_b128 v[48:51], v28 offset:49152\n"
      "Lpad12_%=:\n"
      "s_cbranch_execz Lpad13_%=\n"
      "ds_read_b128 v[52:55], v28 offset:50176\n"
      "v_mfma_f32_16x16x32_bf16 v[152:155], v[60:63], a[116:119], v[152:155]\n"
      "s_add_u32 m0, s81, 65536\n"
      "s_add_u32 s96, s89, 212992\n"
      "v_mfma_f32_16x16x32_bf16 v[156:159], v[56:59], a[120:123], v[156:159]\n"
      "buffer_load_dwordx4 v1, s[36:39], s96 offen sc1 lds\n"
      "v_mfma_f32_16x16x32_bf16 v[156:159], v[60:63], a[124:127], v[156:159]\n"
      "s_waitcnt lgkmcnt(4)\n"
      "v_mfma_f32_16x16x32_bf16 v[160:163], v[32:35], a[112:115], v[160:163]\n"
      "ds_read_b128 v[56:59], v28 offset:51200\n"
      "ds_read_b128 v[60:63], v28 offset:52224\n"
      "v_mfma_f32_16x16x32_bf16 v[160:163], v[36:39], a[116:119], v[160:163]\n"
      "buffer_load_dwordx4 v1, s[36:39], s96 offen offset:1024 sc1 lds\n"
      "v_mfma_f32_16x16x32_bf16 v[164:167], v[32:35], a[120:123], v[164:167]\n"
      "v_mfma_f32_16x16x32_bf16 v[164:167], v[36:39], a[124:127], v[164:167]\n"
      "s_waitcnt lgkmcnt(4)\n"
      "v_mfma_f32_16x16x32_bf16 v[168:171], v[40:43], a[112:115], v[168:171]\n"
      "ds_read_b128 v[32:35], v28 offset:53248\n"
      "ds_read_b128 v[36:39], v28 offset:54272\n"
      "v_mfma_f32_16x16x32_bf16 v[168:171], v[44:47], a[116:119], v[168:171]\n"
      "v_mfma_f32_16x16x32_bf16 v[172:175], v[40:43], a[120:123], v[172:175]\n"
      "buffer_load_dwordx4 v1, s[36:39], s96 offen offset:2048 sc1 lds\n"
      "v_mfma_f32_16x16x32_bf16 v[172:175], v[44:47], a[124:127], v[172:175]\n"
      "s_waitcnt lgkmcnt(4)\n"
      "v_mfma_f32_16x16x32_bf16 v[128:131], v[48:51], a[128:131], v[128:131]\n"
      "ds_read_b128 v[40:43], v28 offset:55296\n"
      "ds_read_b128 v[44:47], v28 offset:56320\n"
      "v_mfma_f32_16x16x32_bf16 v[128:131], v[52:55], a[132:135], v[128:131]\n"
      "v_mfma_f32_16x16x32_bf16 v[132:135], v[48:51], a[136:139], v[132:135]\n"
      "v_mfma_f32_16x16x32_bf16 v[132:135], v[52:55], a[140:143], v[132:135]\n"
      "s_waitcnt lgkmcnt(4)\n"
      "v_mfma_f32_16x16x32_bf16 v[136:139], v[56:59], a[128:131], v[136:139]\n"
      "ds_read_b128 v[48:51], v28 offset:57344\n"
      "ds_read_b128 v[52:55], v28 offset:58368\n"
      "v_mfma_f32_16x16x32_bf16 v[136:139], v[60:63], a[132:135], v[136:139]\n"
      "buffer_load_dwordx4 v1, s[36:39], s96 offen offset:3072 sc1 lds\n"
      "v_mfma_f32_16x16x32_bf16 v[140:143], v[56:59], a[136:139], v[140:143]\n"
      "v_mfma_f32_16x16x32_bf16 v[140:143], v[60:63], a[140:143], v[140:143]\n"
      "s_waitcnt lgkmcnt(4)\n"
      "v_mfma_f32_16x16x32_bf16 v[144:147], v[32:35], a[128:131], v[144:147]\n"
      "ds_read_b128 v[56:59], v28 offset:59392\n"
      "ds_read_b128 v[60:63], v28 offset:60416\n"
      "v_mfma_f32_16x16x32_bf16 v[144:147], v[36:39], a[132:135], v[144:147]\n"
      "s_add_u32 m0, s81, 81920\n"
      "s_add_u32 s96, s89, 229376\n"
      "v_mfma_f32_16x16x32_bf16 v[148:151], v[32:35], a[136:139], v[148:151]\n"
      "buffer_load_dwordx4 v1, s[36:39], s96 offen sc1 lds\n"
      "v_mfma_f32_16x16x32_bf16 v[148:151], v[36:39], a[140:143], v[148:151]\n"
      "s_waitcnt lgkmcnt(4)\n"
      "v_mfma_f32_16x16x32_bf16 v[152:155], v[40:43], a[128:131], v[152:155]\n"
      "ds_read_b128 v[32:35], v28 offset:61440\n"
      "ds_read_b128 v[36:39], v28 offset:62464\n"
      "v_mfma_f32_16x16x32_bf16 v[152:155], v[44:47], a[132:135], v[152:155]\n"
      "v_mfma_f32_16x16x32_bf16 v[156:159], v[40:43], a[136:139], v[156:159]\n"
      "buffer_load_dwordx4 v1, s[36:39], s96 offen offset:1024 sc1 lds\n"
      "v_mfma_f32_16x16x32_bf16 v[156:159], v[44:47], a[140:143], v[156:159]\n"
      "s_waitcnt lgkmcnt(4)\n"
      "v_mfma_f32_16x16x32_bf16 v[160:163], v[48:51], a[128:131], v[160:163]\n"
      "ds_read_b128 v[40:43], v28 offset:63488\n"
      "ds_read_b128 v[44:47], v28 offset:64512\n"
      "v_mfma_f32_16x16x32_bf16 v[160:163], v[52:55], a[132:135], v[160:163]\n"
      "s_waitcnt vmcnt(9)\n"
      "v_cmp_ne_u64 vcc, s[76:77], v[70:71]\n"
      "v_mfma_f32_16x16x32_bf16 v[164:167], v[48:51], a[136:139], v[164:167]\n"
      "s_cbranch_vccnz Lsl_33_%=\n"
      "Lbk_34_%=:\n"
      "v_mfma_f32_16x16x32_bf16 v[164:167], v[52:55], a[140:143], v[164:167]\n"
      "s_waitcnt lgkmcnt(4)\n"
      "buffer_load_dwordx4 v1, s[36:39], s96 offen offset:2048 sc1 lds\n"
      "v_mfma_f32_16x16x32_bf16 v[168:171], v[56:59], a[128:131], v[168:171]\n"
      "ds_read_b128 v[48:51], v29\n"
      "ds_read_b128 v[52:55], v29 offset:1024\n"
      "v_mfma_f32_16x16x32_bf16 v[168:171], v[60:63], a[132:135], v[168:171]\n"
      "v_mfma_f32_16x16x32_bf16 v[172:175], v[56:59], a[136:139], v[172:175]\n"
      "v_mfma_f32_16x16x32_bf16 v[172:175], v[60:63], a[140:143], v[172:175]\n"
      "s_waitcnt lgkmcnt(4)\n"
      "v_mfma_f32_16x16x32_bf16 v[128:131], v[32:35], a[144:147], v[128:131]\n"
      "ds_read_b128 v[56:59], v29 offset:2048\n"
      "ds_read_b128 v[60:63], v29 offset:3072\n"
      "v_mfma_f32_16x16x32_bf16 v[128:131], v[36:39], a[148:151], v[128:131]\n"
      "buffer_load_dwordx4 v1, s[36:39], s96 offen offset:3072 sc1 lds\n"
      "v_mfma_f32_16x16x32_bf16 v[132:135], v[32:35], a[152:155], v[132:135]\n"
      "v_mfma_f32_16x16x32_bf16 v[132:135], v[36:39], a[156:159], v[132:135]\n"
      "s_waitcnt lgkmcnt(4)\n"
      "v_mfma_f32_16x16x32_bf16 v[136:139], v[40:43], a[144:147], v[136:139]\n"
      "ds_read_b128 v[32:35], v29 offset:4096\n"
      "ds_read_b128 v[36:39], v29 offset:5120\n"
      "v_mfma_f32_16x16x32_bf16 v[136:139], v[44:47], a[148:151], v[136:139]\n"
      "s_add_u32 s93, s59, 40960\n"
      "v_mfma_f32_16x16x32_bf16 v[140:143], v[40:43], a[152:155], v[140:143]\n"
      "buffer_load_dwordx4 a[160:163], v1, s[36:39], s93 offen sc1\n"
      "v_mfma_f32_16x16x32_bf16 v[140:143], v[44:47], a[156:159], v[140:143]\n"
      "s_waitcnt lgkmcnt(4)\n"
      "v_mfma_f32_16x16x32_bf16 v[144:147], v[48:51], a[144:147], v[144:147]\n"
      "ds_read_b128 v[40:43], v29 offset:6144\n"
      "ds_read_b128 v[44:47], v29 offset:7168\n"
      "v_mfma_f32_16x16x32_bf16 v[144:147], v[52:55], a[148:151], v[144:147]\n"
      "v_mfma_f32_16x16x32_bf16 v[148:151], v[48:51], a[152:155], v[148:151]\n"
      "v_mfma_f32_16x16x32_bf16 v[148:151], v[52:55], a[156:159], v[148:151]\n"
      "s_waitcnt lgkmcnt(4)\n"
      "buffer_load_dwordx4 a[164:167], v1, s[36:39], s93 offen offset:1024 sc1\n"
      "v_mfma_f32_16x16x32_bf16 v[152:155], v[56:59], a[144:147], v[152:155]\n"
      "ds_read_b128 v[48:51], v29 offset:8192\n"
      "ds_read_b128 v[52:55], v29 offset:9216\n"
      "v_mfma_f32_16x16x32_bf16 v[152:155], v[60:63], a[148:151], v[152:155]\n"
      "v_mfma_f32_16x16x32_bf16 v[156:159], v[56:59], a[152:155], v[156:159]\n"
      "v_mfma_f32_16x16x32_bf16 v[156:159], v[60:63], a[156:159], v[156:159]\n"
      "s_waitcnt lgkmcnt(4)\n"
      "v_mfma_f32_16x16x32_bf16 v[160:163], v[32:35], a[144:147], v[160:163]\n"
      "ds_read_b128 v[56:59], v29 offset:10240\n"
      "ds_read_b128 v[60:63], v29 offset:11264\n"
      "v_mfma_f32_16x16x32_bf16 v[160:163], v[36:39], a[148:151], v[160:163]\n"
      "buffer_load_dwordx4 a[168:171], v1, s[36:39], s93 offen offset:2048 sc1\n"
      "v_mfma_f32_16x16x32_bf16 v[164:167], v[32:35], a[152:155], v[164:167]\n"
      "v_mfma_f32_16x16x32_bf16 v[164:167], v[36:39], a[156:159], v[164:167]\n"
      "s_waitcnt lgkmcnt(4)\n"
      "v_mfma_f32_16x16x32_bf16 v[168:171], v[40:43], a[144:147], v[168:171]\n"
      "ds_read_b128 v[32:35], v29 offset:12288\n"
      "ds_read_b128 v[36:39], v29 offset:13312\n"
      "v_mfma_f32_16x16x32_bf16 v[168:171], v[44:47], a[148:151], v[168:171]\n"
      "v_mfma_f32_16x16x32_bf16 v[172:175], v[40:43], a[152:155], v[172:175]\n"
      "buffer_load_dwordx4 a[172:175], v1, s[36:39], s93 offen offset:3072 sc1\n"
      "v_mfma_f32_16x16x32_bf16 v[172:175], v[44:47], a[156:159], v[172:175]\n"
      "s_waitcnt lgkmcnt(4)\n"
      "v_mfma_f32_16x16x32_bf16 v[176:179], v[48:51], a[0:3], 0\n"
      "ds_read_b128 v[40:43], v29 offset:14336\n"
      "v_mfma_f32_16x16x32_bf16 v[176:179], v[52:55], a[4:7], v[176:179]\n"
      "s_waitcnt vmcnt(12)\n"
      "buffer_store_dwordx2 v[74:75], v76, s[36:39], s58 offen offset:896 sc1\n"
      "v_mfma_f32_16x16x32_bf16 v[180:183], v[48:51], a[8:11], 0\n"
      "ds_read_b128 v[44:47], v29 offset:15360\n"
      "s_add_u32 s93, s59, 45056\n"
      "v_mfma_f32_16x16x32_bf16 v[180:183], v[52:55], a[12:15], v[180:183]\n"
      "s_waitcnt lgkmcnt(4)\n"
      "buffer_load_dwordx4 a[176:179], v1, s[36:39], s93 offen sc1\n"
      "v_mfma_f32_16x16x32_bf16 v[176:179], v[56:59], a[16:19], v[176:179]\n"
      "s_barrier\n"
      "ds_read_b128 v[48:51], v29 offset:16384\n"
      "v_mfma_f32_16x16x32_bf16 v[176:179], v[60:63], a[20:23], v[176:179]\n"
      "v_mfma_f32_16x16x32_bf16 v[180:183], v[56:59], a[24:27], v[180:183]\n"
      "ds_read_b128 v[52:55], v29 offset:17408\n"
      "v_mfma_f32_16x16x32_bf16 v[180:183], v[60:63], a[28:31], v[180:183]\n"
      "s_waitcnt lgkmcnt(4)\n"
      "v_mfma_f32_16x16x32_bf16 v[176:179], v[32:35], a[32:35], v[176:179]\n"
      "ds_read_b128 v[56:59], v29 offset:18432\n"
      "buffer_load_dwordx4 a[180:183], v1, s[36:39], s93 offen offset:1024 sc1\n"
      "v_mfma_f32_16x16x32_bf16 v[176:179], v[36:39], a[36:39], v[176:179]\n"
      "v_mfma_f32_16x16x32_bf16 v[180:183], v[32:35], a[40:43], v[180:183]\n"
      "ds_read_b128 v[60:63], v29 offset:19456\n"
      "v_mfma_f32_16x16x32_bf16 v[180:183], v[36:39], a[44:47], v[180:183]\n"
      "s_waitcnt lgkmcnt(4)\n"
      "v_mfma_f32_16x16x32_bf16 v[176:179], v[40:43], a[48:51], v[176:179]\n"
      "ds_read_b128 v[32:35], v29 offset:20480\n"
      "v_mfma_f32_16x16x32_bf16 v[176:179], v[44:47], a[52:55], v[176:179]\n"
      "buffer_load_dwordx4 a[184:187], v1, s[36:39], s93 offen offset:2048 sc1\n"
      "v_mfma_f32_16x16x32_bf16 v[180:183], v[40:43], a[56:59], v[180:183]\n"
      "ds_read_b128 v[36:39], v29 offset:21504\n"
      "v_mfma_f32_16x16x32_bf16 v[180:183], v[44:47], a[60:63], v[180:183]\n"
      "s_waitcnt lgkmcnt(4)\n"
      "v_mfma_f32_16x16x32_bf16 v[176:179], v[48:51], a[64:67], v[176:179]\n"
      "ds_read_b128 v[40:43], v29 offset:22528\n"
      "v_mfma_f32_16x16x32_bf16 v[176:179], v[52:55], a[68:71], v[176:179]\n"
      "v_mfma_f32_16x16x32_bf16 v[180:183], v[48:51], a[72:75], v[180:183]\n"
      "ds_read_b128 v[44:47], v29 offset:23552\n"
      "v_mfma_f32_16x16x32_bf16 v[180:183], v[52:55], a[76:79], v[180:183]\n"
      "s_waitcnt lgkmcnt(4)\n"
      "buffer_load_dwordx4 a[188:191], v1, s[36:39], s93 offen offset:3072 sc1\n"
      "v_mfma_f32_16x16x32_bf16 v[176:179], v[56:59], a[80:83], v[176:179]\n"
      "ds_read_b128 v[48:51], v29 offset:24576\n"
      "v_mfma_f32_16x16x32_bf16 v[176:179], v[60:63], a[84:87], v[176:179]\n"
      "v_mfma_f32_16x16x32_bf16 v[180:183], v[56:59], a[88:91], v[180:183]\n"
      "ds_read_b128 v[52:55], v29 offset:25600\n"
      "s_add_u32 s93, s59, 49152\n"
      "v_mfma_f32_16x16x32_bf16 v[180:183], v[60:63], a[92:95], v[180:183]\n"
      "s_waitcnt lgkmcnt(4)\n"
      "v_mfma_f32_16x16x32_bf16 v[176:179], v[32:35], a[96:99], v[176:179]\n"
      "ds_read_b128 v[56:59], v29 offset:26624\n"
      "buffer_load_dwordx4 a[192:195], v1, s[36:39], s93 offen sc1\n"
      "v_mfma_f32_16x16x32_bf16 v[176:179], v[36:39], a[100:103], v[176:179]\n"
      "v_mfma_f32_16x16x32_bf16 v[180:183], v[32:35], a[104:107], v[180:183]\n"
      "ds_read_b128 v[60:63], v29 offset:27648\n"
      "v_mfma_f32_16x16x32_bf16 v[180:183], v[36:39], a[108:111], v[180:183]\n"
      "s_waitcnt lgkmcnt(4)\n"
      "v_mfma_f32_16x16x32_bf16 v[176:179], v[40:43], a[112:115], v[176:179]\n"
      "ds_read_b128 v[32:35], v29 offset:28672\n"
      "v_mfma_f32_16x16x32_bf16 v[176:179], v[44:47], a[116:119], v[176:179]\n"
      "buffer_load_dwordx4 a[196:199], v1, s[36:39], s93 offen offset:1024 sc1\n"
      "v_mfma_f32_16x16x32_bf16 v[180:183], v[40:43], a[120:123], v[180:183]\n"
      "ds_read_b128 v[36:39], v29 offset:29696\n"
      "v_mfma_f32_16x16x32_bf16 v[180:183], v[44:47], a[124:127], v[180:183]\n"
      "s_waitcnt lgkmcnt(4)\n"
      "v_mfma_f32_16x16x32_bf16 v[176:179], v[48:51], a[128:131], v[176:179]\n"
      "ds_read_b128 v[40:43], v29 offset:30720\n"
      "v_mfma_f32_16x16x32_bf16 v[176:179], v[52:55], a[132:135], v[176:179]\n"
      "v_mfma_f32_16x16x32_bf16 v[180:183], v[48:51], a[136:139], v[180:183]\n"
      "ds_read_b128 v[44:47], v29 offset:31744\n"
      "buffer_load_dwordx4 a[200:203], v1, s[36:39], s93 offen offset:2048 sc1\n"
      "v_mfma_f32_16x16x32_bf16 v[180:183], v[52:55], a[140:143], v[180:183]\n"
      "s_waitcnt lgkmcnt(4)\n"
      "v_mfma_f32_16x16x32_bf16 v[176:179], v[56:59], a[144:147], v[176:179]\n"
      "ds_read_b128 v[48:51], v1\n"
      "v_mfma_f32_16x16x32_bf16 v[176:179], v[60:63], a[148:151], v[176:179]\n"
      "v_mfma_f32_16x16x32_bf16 v[180:183], v[56:59], a[152:155], v[180:183]\n"
      "ds_read_b128 v[52:55], v1 offset:1024\n"
      "v_mfma_f32_16x16x32_bf16 v[180:183], v[60:63], a[156:159], v[180:183]\n"
      "s_waitcnt lgkmcnt(4)\n"
      "v_mfma_f32_16x16x32_bf16 v[184:187], v[32:35], a[0:3], 0\n"
      "ds_read_b128 v[56:59], v1 offset:2048\n"
      "buffer_load_dwordx4 a[204:207], v1, s[36:39], s93 offen offset:3072 sc1\n"
      "v_mfma_f32_16x16x32_bf16 v[184:187], v[36:39], a[4:7], v[184:187]\n"
      "v_mfma_f32_16x16x32_bf16 v[188:191], v[32:35], a[8:11], 0\n"
      "ds_read_b128 v[60:63], v1 offset:3072\n"
      "v_mfma_f32_16x16x32_bf16 v[188:191], v[36:39], a[12:15], v[188:191]\n"
      "s_waitcnt lgkmcnt(4)\n"
      "v_mfma_f32_16x16x32_bf16 v[184:187], v[40:43], a[16:19], v[184:187]\n"
      "ds_read_b128 v[32:35], v1 offset:4096\n"
      "v_mfma_f32_16x16x32_bf16 v[184:187], v[44:47], a[20:23], v[184:187]\n"
      "s_add_u32 m0, s81, 98304\n"
      "s_add_u32 s96, s89, 245760\n"
      "v_mfma_f32_16x16x32_bf16 v[188:191], v[40:43], a[24:27], v[188:191]\n"
      "ds_read_b128 v[36:39], v1 offset:5120\n"
      "buffer_load_dwordx4 v1, s[36:39], s96 offen sc1 lds\n"
      "v_mfma_f32_16x16x32_bf16 v[188:191], v[44:47], a[28:31], v[188:191]\n"
      "s_waitcnt lgkmcnt(4)\n"
      "v_mfma_f32_16x16x32_bf16 v[184:187], v[48:51], a[32:35], v[184:187]\n"
      "ds_read_b128 v[40:43], v1 offset:6144\n"
      "v_mfma_f32_16x16x32_bf16 v[184:187], v[52:55], a[36:39], v[184:187]\n"
      "v_mfma_f32_16x16x32_bf16 v[188:191], v[48:51], a[40:43], v[188:191]\n"
      "ds_read_b128 v[44:47], v1 offset:7168\n"
      "buffer_load_dwordx4 v1, s[36:39], s96 offen offset:1024 sc1 lds\n"
      "v_mfma_f32_16x16x32_bf16 v[188:191], v[52:55], a[44:47], v[188:191]\n"
      "s_waitcnt lgkmcnt(4)\n"
      "v_mfma_f32_16x16x32_bf16 v[184:187], v[56:59], a[48:51], v[184:187]\n"
      "ds_read_b128 v[48:51], v1 offset:8192\n"
      "v_mfma_f32_16x16x32_bf16 v[184:187], v[60:63], a[52:55], v[184:187]\n"
      "v_mfma_f32_16x16x32_bf16 v[188:191], v[56:59], a[56:59], v[188:191]\n"
      "ds_read_b128 v[52:55], v1 offset:9216\n"
      "v_mfma_f32_16x16x32_bf16 v[188:191], v[60:63], a[60:63], v[188:191]\n"
      "s_waitcnt lgkmcnt(4)\n"
      "buffer_load_dwordx4 v1, s[36:39], s96 offen offset:2048 sc1 lds\n"
      "v_mfma_f32_16x16x32_bf16 v[184:187], v[32:35], a[64:67], v[184:187]\n"
      "ds_read_b128 v[56:59], v1 offset:10240\n"
      "v_mfma_f32_16x16x32_bf16 v[184:187], v[36:39], a[68:71], v[184:187]\n"
      "v_mfma_f32_16x16x32_bf16 v[188:191], v[32:35], a[72:75], v[188:191]\n"
      "ds_read_b128 v[60:63], v1 offset:11264\n"
      "v_mfma_f32_16x16x32_bf16 v[188:191], v[36:39], a[76:79], v[188:191]\n"
      "s_waitcnt lgkmcnt(4)\n"
      "v_mfma_f32_16x16x32_bf16 v[184:187], v[40:43], a[80:83], v[184:187]\n"
      "ds_read_b128 v[32:35], v1 offset:12288\n"
      "v_mfma_f32_16x16x32_bf16 v[184:187], v[44:47], a[84:87], v[184:187]\n"
      "buffer_load_dwordx4 v1, s[36:39], s96 offen offset:3072 sc1 lds\n"
      "v_mfma_f32_16x16x32_bf16 v[188:191], v[40:43], a[88:91], v[188:191]\n"
      "ds_read_b128 v[36:39], v1 offset:13312\n"
      "v_mfma_f32_16x16x32_bf16 v[188:191], v[44:47], a[92:95], v[188:191]\n"
      "s_waitcnt lgkmcnt(4)\n"
      "v_mfma_f32_16x16x32_bf16 v[184:187], v[48:51], a[96:99], v[184:187]\n"
      "ds_read_b128 v[40:43], v1 offset:14336\n"
      "s_add_u32 s93, s59, 53248\n"
      "v_mfma_f32_16x16x32_bf16 v[184:187], v[52:55], a[100:103], v[184:187]\n"
      "v_mfma_f32_16x16x32_bf16 v[188:191], v[48:51], a[104:107], v[188:191]\n"
      "ds_read_b128 v[44:47], v1 offset:15360\n"
      "buffer_load_dwordx4 a[208:211], v1, s[36:39], s93 offen sc1\n"
      "v_mfma_f32_16x16x32_bf16 v[188:191], v[52:55], a[108:111], v[188:191]\n"
      "s_waitcnt lgkmcnt(4)\n"
      "v_mfma_f32_16x16x32_bf16 v[184:187], v[56:59], a[112:115], v[184:187]\n"
      "s_barrier\n"
      "ds_read_b128 v[48:51], v1 offset:16384\n"
      "v_mfma_f32_16x16x32_bf16 v[184:187], v[60:63], a[116:119], v[184:187]\n"
      "v_mfma_f32_16x16x32_bf16 v[188:191], v[56:59], a[120:123], v[188:191]\n"
      "ds_read_b128 v[52:55], v1 offset:17408\n"
      "v_mfma_f32_16x16x32_bf16 v[188:191], v[60:63], a[124:127], v[188:191]\n"
      "s_waitcnt lgkmcnt(4)\n"
      "buffer_load_dwordx4 a[212:215], v1, s[36:39], s93 offen offset:1024 sc1\n"
      "v_mfma_f32_16x16x32_bf16 v[184:187], v[32:35], a[128:131], v[184:187]\n"
      "ds_read_b128 v[56:59], v1 offset:18432\n"
      "v_mfma_f32_16x16x32_bf16 v[184:187], v[36:39], a[132:135], v[184:187]\n"
      "v_mfma_f32_16x16x32_bf16 v[188:191], v[32:35], a[136:139], v[188:191]\n"
      "ds_read_b128 v[60:63], v1 offset:19456\n"
      "v_mfma_f32_16x16x32_bf16 v[188:191], v[36:39], a[140:143], v[188:191]\n"
      "s_waitcnt lgkmcnt(4)\n"
      "v_mfma_f32_16x16x32_bf16 v[184:187], v[40:43], a[144:147], v[184:187]\n"
      "ds_read_b128 v[32:35], v1 offset:20480\n"
      "buffer_load_dwordx4 a[216:219], v1, s[36:39], s93 offen offset:2048 sc1\n"
      "v_mfma_f32_16x16x32_bf16 v[184:187], v[44:47], a[148:151], v[184:187]\n"
      "v_mfma_f32_16x16x32_bf16 v[188:191], v[40:43], a[152:155], v[188:191]\n"
      "ds_read_b128 v[36:39], v1 offset:21504\n"
      "v_mfma_f32_16x16x32_bf16 v[188:191], v[44:47], a[156:159], v[188:191]\n"
      "s_waitcnt lgkmcnt(4)\n"
      "v_mfma_f32_16x16x32_bf16 v[80:83], v[48:51], a[0:3], 0\n"
      "ds_read_b128 v[40:43], v1 offset:22528\n"
      "v_mfma_f32_16x16x32_bf16 v[80:83], v[52:55], a[4:7], v[80:83]\n"
      "v_mfma_f32_16x16x32_bf16 v[84:87], v[48:51], a[8:11], 0\n"
      "ds_read_b128 v[44:47], v1 offset:23552\n"
      "buffer_load_dwordx4 a[220:223], v1, s[36:39], s93 offen offset:3072 sc1\n"
      "v_mfma_f32_16x16x32_bf16 v[84:87], v[52:55], a[12:15], v[84:87]\n"
      "s_waitcnt lgkmcnt(4)\n"
      "v_mfma_f32_16x16x32_bf16 v[80:83], v[56:59], a[16:19], v[80:83]\n"
      "ds_read_b128 v[48:51], v1 offset:24576\n"
      "v_mfma_f32_16x16x32_bf16 v[80:83], v[60:63], a[20:23], v[80:83]\n"
      "s_add_u32 m0, s81, 114688\n"
      "s_add_u32 s96, s89, 262144\n"
      "v_mfma_f32_16x16x32_bf16 v[84:87], v[56:59], a[24:27], v[84:87]\n"
      "ds_read_b128 v[52:55], v1 offset:25600\n"
      "v_mfma_f32_16x16x32_bf16 v[84:87], v[60:63], a[28:31], v[84:87]\n"
      "s_waitcnt lgkmcnt(4)\n"
      "buffer_load_dwordx4 v1, s[36:39], s96 offen sc1 lds\n"
      "v_mfma_f32_16x16x32_bf16 v[80:83], v[32:35], a[32:35], v[80:83]\n"
      "ds_read_b128 v[56:59], v1 offset:26624\n"
      "v_mfma_f32_16x16x32_bf16 v[80:83], v[36:39], a[36:39], v[80:83]\n"
      "v_mfma_f32_16x16x32_bf16 v[84:87], v[32:35], a[40:43], v[84:87]\n"
      "ds_read_b128 v[60:63], v1 offset:27648\n"
      "v_mfma_f32_16x16x32_bf16 v[84:87], v[36:39], a[44:47], v[84:87]\n"
      "s_waitcnt lgkmcnt(4)\n"
      "v_mfma_f32_16x16x32_bf16 v[80:83], v[40:43], a[48:51], v[80:83]\n"
      "ds_read_b128 v[32:35], v1 offset:28672\n"
      "buffer_load_dwordx4 v1, s[36:39], s96 offen offset:1024 sc1 lds\n"
      "v_mfma_f32_16x16x32_bf16 v[80:83], v[44:47], a[52:55], v[80:83]\n"
      "v_mfma_f32_16x16x32_bf16 v[84:87], v[40:43], a[56:59], v[84:87]\n"
      "ds_read_b128 v[36:39], v1 offset:29696\n"
      "v_mfma_f32_16x16x32_bf16 v[84:87], v[44:47], a[60:63], v[84:87]\n"
      "s_waitcnt lgkmcnt(4)\n"
      "v_mfma_f32_16x16x32_bf16 v[80:83], v[48:51], a[64:67], v[80:83]\n"
      "ds_read_b128 v[40:43], v1 offset:30720\n"
      "v_mfma_f32_16x16x32_bf16 v[80:83], v[52:55], a[68:71], v[80:83]\n"
      "buffer_load_dwordx4 v1, s[36:39], s96 offen offset:2048 sc1 lds\n"
      "v_mfma_f32_16x16x32_bf16 v[84:87], v[48:51], a[72:75], v[84:87]\n"
      "ds_read_b128 v[44:47], v1 offset:31744\n"
      "v_mfma_f32_16x16x32_bf16 v[84:87], v[52:55], a[76:79], v[84:87]\n"
      "s_waitcnt lgkmcnt(4)\n"
      "v_mfma_f32_16x16x32_bf16 v[80:83], v[56:59], a[80:83], v[80:83]\n"
      "ds_read_b128 v[48:51], v1 offset:32768\n"
      "v_mfma_f32_16x16x32_bf16 v[80:83], v[60:63], a[84:87], v[80:83]\n"
      "v_mfma_f32_16x16x32_bf16 v[84:87], v[56:59], a[88:91], v[84:87]\n"
      "ds_read_b128 v[52:55], v1 offset:33792\n"
      "v_mfma_f32_16x16x32_bf16 v[84:87], v[60:63], a[92:95], v[84:87]\n"
      "s_waitcnt lgkmcnt(4)\n"
      "buffer_load_dwordx4 v1, s[36:39], s96 offen offset:3072 sc1 lds\n"
      "v_mfma_f32_16x16x32_bf16 v[80:83], v[32:35], a[96:99], v[80:83]\n"
      "ds_read_b128 v[56:59], v1 offset:34816\n"
      "v_mfma_f32_16x16x32_bf16 v[80:83], v[36:39], a[100:103], v[80:83]\n"
      "v_mfma_f32_16x16x32_bf16 v[84:87], v[32:35], a[104:107], v[84:87]\n"
      "ds_read_b128 v[60:63], v1 offset:35840\n"
      "v_mfma_f32_16x16x32_bf16 v[84:87], v[36:39], a[108:111], v[84:87]\n"
      "s_waitcnt lgkmcnt(4)\n"
      "v_mfma_f32_16x16x32_bf16 v[80:83], v[40:43], a[112:115], v[80:83]\n"
      "ds_read_b128 v[32:35], v1 offset:36864\n"
      "v_mfma_f32_16x16x32_bf16 v[80:83], v[44:47], a[116:119], v[80:83]\n"
      "s_add_u32 m0, s81, 131072\n"
      "s_add_u32 s96, s89, 278528\n"
      "v_mfma_f32_16x16x32_bf16 v[84:87], v[40:43], a[120:123], v[84:87]\n"
      "ds_read_b128 v[36:39], v1 offset:37888\n"
      "buffer_load_dwordx4 v1, s[36:39], s96 offen sc1 lds\n"
      "v_mfma_f32_16x16x32_bf16 v[84:87], v[44:47], a[124:127], v[84:87]\n"
      "s_waitcnt lgkmcnt(4)\n"
      "v_mfma_f32_16x16x32_bf16 v[80:83], v[48:51], a[128:131], v[80:83]\n"
      "ds_read_b128 v[40:43], v1 offset:38912\n"
      "v_mfma_f32_16x16x32_bf16 v[80:83], v[52:55], a[132:135], v[80:83]\n"
      "buffer_load_dwordx4 v1, s[36:39], s96 offen offset:1024 sc1 lds\n"
      "v_mfma_f32_16x16x32_bf16 v[84:87], v[48:51], a[136:139], v[84:87]\n"
      "ds_read_b128 v[44:47], v1 offset:39936\n"
      "v_mfma_f32_16x16x32_bf16 v[84:87], v[52:55], a[140:143], v[84:87]\n"
      "s_waitcnt lgkmcnt(4)\n"
      "v_mfma_f32_16x16x32_bf16 v[80:83], v[56:59], a[144:147], v[80:83]\n"
      "ds_read_b128 v[48:51], v1 offset:40960\n"
      "v_mfma_f32_16x16x32_bf16 v[80:83], v[60:63], a[148:151], v[80:83]\n"
      "v_mfma_f32_16x16x32_bf16 v[84:87], v[56:59], a[152:155], v[84:87]\n"
      "ds_read_b128 v[52:55], v1 offset:41984\n"
      "buffer_load_dwordx4 v1, s[36:39], s96 offen offset:2048 sc1 lds\n"
      "v_mfma_f32_16x16x32_bf16 v[84:87], v[60:63], a[156:159], v[84:87]\n"
      "s_waitcnt lgkmcnt(4)\n"
      "s_waitcnt vmcnt(24)\n"
      "v_mfma_f32_16x16x32_bf16 v[128:131], v[32:35], a[160:163], v[128:131]\n"
      "ds_read_b128 v[56:59], v1 offset:43008\n"
      "buffer_load_dwordx2 v[70:71], v23, s[36:39], 0 offen offset:896 sc1\n"
      "v_mfma_f32_16x16x32_bf16 v[128:131], v[36:39], a[164:167], v[128:131]\n"
      "v_mfma_f32_16x16x32_bf16 v[132:135], v[32:35], a[168:171], v[132:135]\n"
      "ds_read_b128 v[60:63], v1 offset:44032\n"
      "v_mfma_f32_16x16x32_bf16 v[132:135], v[36:39], a[172:175], v[132:135]\n"
      "s_waitcnt lgkmcnt(4)\n"
      "v_mfma_f32_16x16x32_bf16 v[136:139], v[40:43], a[160:163], v[136:139]\n"
      "ds_read_b128 v[32:35], v1 offset:45056\n"
      "buffer_load_dwordx4 v1, s[36:39], s96 offen offset:3072 sc1 lds\n"
      "v_mfma_f32_16x16x32_bf16 v[136:139], v[44:47], a[164:167], v[136:139]\n"
      "v_mfma_f32_16x16x32_bf16 v[140:143], v[40:43], a[168:171], v[140:143]\n"
      "ds_read_b128 v[36:39], v1 offset:46080\n"
      "v_mfma_f32_16x16x32_bf16 v[140:143], v[44:47], a[172:175], v[140:143]\n"
      "s_waitcnt lgkmcnt(4)\n"
      "v_mfma_f32_16x16x32_bf16 v[144:147], v[48:51], a[160:163], v[144:147]\n"
      "ds_read_b128 v[40:43], v1 offset:47104\n"
      "v_mfma_f32_16x16x32_bf16 v[144:147], v[52:55], a[164:167], v[144:147]\n"
      "s_add_u32 m0, s81, 147456\n"
      "s_add_u32 s96, s89, 294912\n"
      "v_mfma_f32_16x16x32_bf16 v[148:151], v[48:51], a[168:171], v[148:151]\n"
      "ds_read_b128 v[44:47], v1 offset:48128\n"
      "buffer_load_dwordx4 v1, s[36:39], s96 offen sc1 lds\n"
      "v_mfma_f32_16x16x32_bf16 v[148:151], v[52:55], a[172:175], v[148:151]\n"
      "s_waitcnt lgkmcnt(4)\n"
      "v_mfma_f32_16x16x32_bf16 v[152:155], v[56:59], a[160:163], v[152:155]\n"
      "s_barrier\n"
      "ds_read_b128 v[48:51], v1 offset:49152\n"
      "v_mfma_f32_16x16x32_bf16 v[152:155], v[60:63], a[164:167], v[152:155]\n"
      "v_mfma_f32_16x16x32_bf16 v[156:159], v[56:59], a[168:171], v[156:159]\n"
      "ds_read_b128 v[52:55], v1 offset:50176\n"
      "buffer_load_dwordx4 v1, s[36:39], s96 offen offset:1024 sc1 lds\n"
      "v_mfma_f32_16x16x32_bf16 v[156:159], v[60:63], a[172:175], v[156:159]\n"
      "s_waitcnt lgkmcnt(4)\n"
      "v_mfma_f32_16x16x32_bf16 v[160:163], v[32:35], a[160:163], v[160:163]\n"
      "ds_read_b128 v[56:59], v1 offset:51200\n"
      "v_mfma_f32_16x16x32_bf16 v[160:163], v[36:39], a[164:167], v[160:163]\n"
      "v_mfma_f32_16x16x32_bf16 v[164:167], v[32:35], a[168:171], v[164:167]\n"
      "ds_read_b128 v[60:63], v1 offset:52224\n"
      "v_mfma_f32_16x16x32_bf16 v[164:167], v[36:39], a[172:175], v[164:167]\n"
      "s_waitcnt lgkmcnt(4)\n"
      "buffer_load_dwordx4 v1, s[36:39], s96 offen offset:2048 sc1 lds\n"
      "v_mfma_f32_16x16x32_bf16 v[168:171], v[40:43], a[160:163], v[168:171]\n"
      "ds_read_b128 v[32:35], v1 offset:53248\n"
      "v_mfma_f32_16x16x32_bf16 v[168:171], v[44:47], a[164:167], v[168:171]\n"
      "v_mfma_f32_16x16x32_bf16 v[172:175], v[40:43], a[168:171], v[172:175]\n"
      "ds_read_b128 v[36:39], v1 offset:54272\n"
      "v_mfma_f32_16x16x32_bf16 v[172:175], v[44:47], a[172:175], v[172:175]\n"
      "s_waitcnt lgkmcnt(4)\n"
      "v_mfma_f32_16x16x32_bf16 v[176:179], v[48:51], a[160:163], v[176:179]\n"
      "ds_read_b128 v[40:43], v1 offset:55296\n"
      "v_mfma_f32_16x16x32_bf16 v[176:179], v[52:55], a[164:167], v[176:179]\n"
      "buffer_load_dwordx4 v1, s[36:39], s96 offen offset:3072 sc1 lds\n"
      "v_mfma_f32_16x16x32_bf16 v[180:183], v[48:51], a[168:171], v[180:183]\n"
      "ds_read_b128 v[44:47], v1 offset:56320\n"
      "v_mfma_f32_16x16x32_bf16 v[180:183], v[52:55], a[172:175], v[180:183]\n"
      "s_waitcnt lgkmcnt(4)\n"
      "v_mfma_f32_16x16x32_bf16 v[184:187], v[56:59], a[160:163], v[184:187]\n"
      "ds_read_b128 v[48:51], v1 offset:57344\n"
      "v_mfma_f32_16x16x32_bf16 v[184:187], v[60:63], a[164:167], v[184:187]\n"
      "s_add_u32 m0, s81, 0\n"
      "s_add_u32 s96, s89, 311296\n"
      "v_mfma_f32_16x16x32_bf16 v[188:191], v[56:59], a[168:171], v[188:191]\n"
      "ds_read_b128 v[52:55], v1 offset:58368\n"
      "buffer_load_dwordx4 v1, s[36:39], s96 offen sc1 lds\n"
      "v_mfma_f32_16x16x32_bf16 v[188:191], v[60:63], a[172:175], v[188:191]\n"
      "s_waitcnt lgkmcnt(4)\n"
      "v_mfma_f32_16x16x32_bf16 v[80:83], v[32:35], a[160:163], v[80:83]\n"
      "ds_read_b128 v[56:59], v1 offset:59392\n"
      "v_mfma_f32_16x16x32_bf16 v[80:83], v[36:39], a[164:167], v[80:83]\n"
      "v_mfma_f32_16x16x32_bf16 v[84:87], v[32:35], a[168:171], v[84:87]\n"
      "ds_read_b128 v[60:63], v1 offset:60416\n"
      "v_mfma_f32_16x16x32_bf16 v[84:87], v[36:39], a[172:175], v[84:87]\n"
      "s_waitcnt lgkmcnt(4)\n"
      "buffer_load_dwordx4 v1, s[36:39], s96 offen offset:1024 sc1 lds\n"
      "s_waitcnt vmcnt(27)\n"
      "v_mfma_f32_16x16x32_bf16 v[128:131], v[40:43], a[176:179], v[128:131]\n"
      "ds_read_b128 v[32:35], v1 offset:61440\n"
      "v_mfma_f32_16x16x32_bf16 v[128:131], v[44:47], a[180:183], v[128:131]\n"
      "v_mfma_f32_16x16x32_bf16 v[132:135], v[40:43], a[184:187], v[132:135]\n"
      "ds_read_b128 v[36:39], v1 offset:62464\n"
      "v_mfma_f32_16x16x32_bf16 v[132:135], v[44:47], a[188:191], v[132:135]\n"
      "s_waitcnt lgkmcnt(4)\n"
      "v_mfma_f32_16x16x32_bf16 v[136:139], v[48:51], a[176:179], v[136:139]\n"
      "ds_read_b128 v[40:43], v1 offset:63488\n"
      "buffer_load_dwordx4 v1, s[36:39], s96 offen offset:2048 sc1 lds\n"
      "v_mfma_f32_16x16x32_bf16 v[136:139], v[52:55], a[180:183], v[136:139]\n"
      "v_mfma_f32_16x16x32_bf16 v[140:143], v[48:51], a[184:187], v[140:143]\n"
      "ds_read_b128 v[44:47], v1 offset:64512\n"
      "v_mfma_f32_16x16x32_bf16 v[140:143], v[52:55], a[188:191], v[140:143]\n"
      "s_waitcnt lgkmcnt(4)\n"
      "v_mfma_f32_16x16x32_bf16 v[144:147], v[56:59], a[176:179], v[144:147]\n"
      "ds_read_b128 v[48:51], v28\n"
      "v_mfma_f32_16x16x32_bf16 v[144:147], v[60:63], a[180:183], v[144:147]\n"
      "v_mfma_f32_16x16x32_bf16 v[148:151], v[56:59], a[184:187], v[148:151]\n"
      "ds_read_b128 v[52:55], v28 offset:1024\n"
      "buffer_load_dwordx4 v1, s[36:39], s96 offen offset:3072 sc1 lds\n"
      "v_mfma_f32_16x16x32_bf16 v[148:151], v[60:63], a[188:191], v[148:151]\n"
      "s_waitcnt lgkmcnt(4)\n"
      "v_mfma_f32_16x16x32_bf16 v[152:155], v[32:35], a[176:179], v[152:155]\n"
      "ds_read_b128 v[56:59], v28 offset:2048\n"
      "v_mfma_f32_16x16x32_bf16 v[152:155], v[36:39], a[180:183], v[152:155]\n"
      "s_add_u32 m0, s78, 16384\n"
      "s_add_u32 s96, s86, 327680\n"
      "v_mfma_f32_16x16x32_bf16 v[156:159], v[32:35], a[184:187], v[156:159]\n"
      "ds_read_b128 v[60:63], v28 offset:3072\n"
      "v_mfma_f32_16x16x32_bf16 v[156:159], v[36:39], a[188:191], v[156:159]\n"
      "s_waitcnt lgkmcnt(4)\n"
      "buffer_load_dwordx4 v1, s[36:39], s96 offen sc1 lds\n"
      "v_mfma_f32_16x16x32_bf16 v[160:163], v[40:43], a[176:179], v[160:163]\n"
      "ds_read_b128 v[32:35], v28 offset:4096\n"
      "v_mfma_f32_16x16x32_bf16 v[160:163], v[44:47], a[180:183], v[160:163]\n"
      "v_mfma_f32_16x16x32_bf16 v[164:167], v[40:43], a[184:187], v[164:167]\n"
      "ds_read_b128 v[36:39], v28 offset:5120\n"
      "v_mfma_f32_16x16x32_bf16 v[164:167], v[44:47], a[188:191], v[164:167]\n"
      "s_waitcnt lgkmcnt(4)\n"
      "v_mfma_f32_16x16x32_bf16 v[168:171], v[48:51], a[176:179], v[168:171]\n"
      "ds_read_b128 v[40:43], v28 offset:6144\n"
      "v_mfma_f32_16x16x32_bf16 v[168:171], v[52:55], a[180:183], v[168:171]\n"
      "s_waitcnt vmcnt(10)\n"
      "v_cmp_ne_u64 vcc, s[76:77], v[70:71]\n"
      "v_mfma_f32_16x16x32_bf16 v[172:175], v[48:51], a[184:187], v[172:175]\n"
      "ds_read_b128 v[44:47], v28 offset:7168\n"
      "s_cbranch_vccnz Lsl_35_%=\n"
      "Lbk_36_%=:\n"
      "v_mfma_f32_16x16x32_bf16 v[172:175], v[52:55], a[188:191], v[172:175]\n"
      "s_waitcnt lgkmcnt(4)\n"
      "s_add_u32 s93, s59, 57344\n"
      "v_mfma_f32_16x16x32_bf16 v[176:179], v[56:59], a[176:179], v[176:179]\n"
      "ds_read_b128 v[48:51], v28 offset:8192\n"
      "buffer_load_dwordx4 a[224:227], v1, s[36:39], s93 offen sc1\n"
      "v_mfma_f32_16x16x32_bf16 v[176:179], v[60:63], a[180:183], v[176:179]\n"
      "v_mfma_f32_16x16x32_bf16 v[180:183], v[56:59], a[184:187], v[180:183]\n"
      "ds_read_b128 v[52:55], v28 offset:9216\n"
      "v_mfma_f32_16x16x32_bf16 v[180:183], v[60:63], a[188:191], v[180:183]\n"
      "s_waitcnt lgkmcnt(4)\n"
      "buffer_load_dwordx4 a[228:231], v1, s[36:39], s93 offen offset:1024 sc1\n"
      "v_mfma_f32_16x16x32_bf16 v[184:187], v[32:35], a[176:179], v[184:187]\n"
      "ds_read_b128 v[56:59], v28 offset:10240\n"
      "v_mfma_f32_16x16x32_bf16 v[184:187], v[36:39], a[180:183], v[184:187]\n"
      "v_mfma_f32_16x16x32_bf16 v[188:191], v[32:35], a[184:187], v[188:191]\n"
      "ds_read_b128 v[60:63], v28 offset:11264\n"
      "v_mfma_f32_16x16x32_bf16 v[188:191], v[36:39], a[188:191], v[188:191]\n"
      "s_waitcnt lgkmcnt(4)\n"
      "v_mfma_f32_16x16x32_bf16 v[80:83], v[40:43], a[176:179], v[80:83]\n"
      "ds_read_b128 v[32:35], v28 offset:12288\n"
      "buffer_load_dwordx4 a[232:235], v1, s[36:39], s93 offen offset:2048 sc1\n"
      "v_mfma_f32_16x16x32_bf16 v[80:83], v[44:47], a[180:183], v[80:83]\n"
      "v_mfma_f32_16x16x32_bf16 v[84:87], v[40:43], a[184:187], v[84:87]\n"
      "ds_read_b128 v[36:39], v28 offset:13312\n"
      "v_mfma_f32_16x16x32_bf16 v[84:87], v[44:47], a[188:191], v[84:87]\n"
      "s_waitcnt lgkmcnt(4)\n"
      "v_mfma_f32_16x16x32_bf16 v[128:131], v[48:51], a[192:195], v[128:131]\n"
      "ds_read_b128 v[40:43], v28 offset:14336\n"
      "v_mfma_f32_16x16x32_bf16 v[128:131], v[52:55], a[196:199], v[128:131]\n"
      "v_mfma_f32_16x16x32_bf16 v[132:135], v[48:51], a[200:203], v[132:135]\n"
      "ds_read_b128 v[44:47], v28 offset:15360\n"
      "buffer_load_dwordx4 a[236:239], v1, s[36:39], s93 offen offset:3072 sc1\n"
      "v_mfma_f32_16x16x32_bf16 v[132:135], v[52:55], a[204:207], v[132:135]\n"
      "s_waitcnt lgkmcnt(4)\n"
      "v_mfma_f32_16x16x32_bf16 v[136:139], v[56:59], a[192:195], v[136:139]\n"
      "s_barrier\n"
      "ds_read_b128 v[48:51], v28 offset:16384\n"
      "v_mfma_f32_16x16x32_bf16 v[136:139], v[60:63], a[196:199], v[136:139]\n"
      "s_add_u32 s93, s59, 61440\n"
      "v_mfma_f32_16x16x32_bf16 v[140:143], v[56:59], a[200:203], v[140:143]\n"
      "ds_read_b128 v[52:55], v28 offset:17408\n"
      "v_mfma_f32_16x16x32_bf16 v[140:143], v[60:63], a[204:207], v[140:143]\n"
      "s_waitcnt lgkmcnt(4)\n"
      "buffer_load_dwordx4 a[240:243], v1, s[36:39], s93 offen sc1\n"
      "v_mfma_f32_16x16x32_bf16 v[144:147], v[32:35], a[192:195], v[144:147]\n"
      "ds_read_b128 v[56:59], v28 offset:18432\n"
      "v_mfma_f32_16x16x32_bf16 v[144:147], v[36:39], a[196:199], v[144:147]\n"
      "v_mfma_f32_16x16x32_bf16 v[148:151], v[32:35], a[200:203], v[148:151]\n"
      "ds_read_b128 v[60:63], v28 offset:19456\n"
      "v_mfma_f32_16x16x32_bf16 v[148:151], v[36:39], a[204:207], v[148:151]\n"
      "s_waitcnt lgkmcnt(4)\n"
      "v_mfma_f32_16x16x32_bf16 v[152:155], v[40:43], a[192:195], v[152:155]\n"
      "ds_read_b128 v[32:35], v28 offset:20480\n"
      "buffer_load_dwordx4 a[244:247], v1, s[36:39], s93 offen offset:1024 sc1\n"
      "v_mfma_f32_16x16x32_bf16 v[152:155], v[44:47], a[196:199], v[152:155]\n"
      "v_mfma_f32_16x16x32_bf16 v[156:159], v[40:43], a[200:203], v[156:159]\n"
      "Lpad13_%=:\n"
      "s_cbranch_execz Lpad14_%=\n"
      "ds_read_b128 v[36:39], v28 offset:21504\n"
      "v_mfma_f32_16x16x32_bf16 v[156:159], v[44:47], a[204:207], v[156:159]\n"
      "s_waitcnt lgkmcnt(4)\n"
      "v_mfma_f32_16x16x32_bf16 v[160:163], v[48:51], a[192:195], v[160:163]\n"
      "ds_read_b128 v[40:43], v28 offset:22528\n"
      "v_mfma_f32_16x16x32_bf16 v[160:163], v[52:55], a[196:199], v[160:163]\n"
      "buffer_load_dwordx4 a[248:251], v1, s[36:39], s93 offen offset:2048 sc1\n"
      "v_mfma_f32_16x16x32_bf16 v[164:167], v[48:51], a[200:203], v[164:167]\n"
      "ds_read_b128 v[44:47], v28 offset:23552\n"
      "v_mfma_f32_16x16x32_bf16 v[164:167], v[52:55], a[204:207], v[164:167]\n"
      "s_waitcnt lgkmcnt(4)\n"
      "v_mfma_f32_16x16x32_bf16 v[168:171], v[56:59], a[192:195], v[168:171]\n"
      "ds_read_b128 v[48:51], v28 offset:24576\n"
      "v_mfma_f32_16x16x32_bf16 v[168:171], v[60:63], a[196:199], v[168:171]\n"
      "v_mfma_f32_16x16x32_bf16 v[172:175], v[56:59], a[200:203], v[172:175]\n"
      "ds_read_b128 v[52:55], v28 offset:25600\n"
      "v_mfma_f32_16x16x32_bf16 v[172:175], v[60:63], a[204:207], v[172:175]\n"
      "s_waitcnt lgkmcnt(4)\n"
      "buffer_load_dwordx4 a[252:255], v1, s[36:39], s93 offen offset:3072 sc1\n"
      "v_mfma_f32_16x16x32_bf16 v[176:179], v[32:35], a[192:195], v[176:179]\n"
      "ds_read_b128 v[56:59], v28 offset:26624\n"
      "v_mfma_f32_16x16x32_bf16 v[176:179], v[36:39], a[196:199], v[176:179]\n"
      "v_mfma_f32_16x16x32_bf16 v[180:183], v[32:35], a[200:203], v[180:183]\n"
      "ds_read_b128 v[60:63], v28 offset:27648\n"
      "s_add_u32 s93, s59, 65536\n"
      "v_mfma_f32_16x16x32_bf16 v[180:183], v[36:39], a[204:207], v[180:183]\n"
      "s_waitcnt lgkmcnt(4)\n"
      "v_mfma_f32_16x16x32_bf16 v[184:187], v[40:43], a[192:195], v[184:187]\n"
      "ds_read_b128 v[32:35], v28 offset:28672\n"
      "buffer_load_dwordx4 v[224:227], v1, s[36:39], s93 offen sc1\n"
      "v_mfma_f32_16x16x32_bf16 v[184:187], v[44:47], a[196:199], v[184:187]\n"
      "v_mfma_f32_16x16x32_bf16 v[188:191], v[40:43], a[200:203], v[188:191]\n"
      "ds_read_b128 v[36:39], v28 offset:29696\n"
      "v_mfma_f32_16x16x32_bf16 v[188:191], v[44:47], a[204:207], v[188:191]\n"
      "s_waitcnt lgkmcnt(4)\n"
      "v_mfma_f32_16x16x32_bf16 v[80:83], v[48:51], a[192:195], v[80:83]\n"
      "ds_read_b128 v[40:43], v28 offset:30720\n"
      "v_mfma_f32_16x16x32_bf16 v[80:83], v[52:55], a[196:199], v[80:83]\n"
      "buffer_load_dwordx4 v[228:231], v1, s[36:39], s93 offen offset:1024 sc1\n"
      "v_mfma_f32_16x16x32_bf16 v[84:87], v[48:51], a[200:203], v[84:87]\n"
      "ds_read_b128 v[44:47], v28 offset:31744\n"
      "v_mfma_f32_16x16x32_bf16 v[84:87], v[52:55], a[204:207], v[84:87]\n"
      "s_waitcnt lgkmcnt(4)\n"
      "v_mfma_f32_16x16x32_bf16 v[128:131], v[56:59], a[208:211], v[128:131]\n"
      "ds_read_b128 v[48:51], v28 offset:32768\n"
      "v_mfma_f32_16x16x32_bf16 v[128:131], v[60:63], a[212:215], v[128:131]\n"
      "v_mfma_f32_16x16x32_bf16 v[132:135], v[56:59], a[216:219], v[132:135]\n"
      "ds_read_b128 v[52:55], v28 offset:33792\n"
      "buffer_load_dwordx4 v[232:235], v1, s[36:39], s93 offen offset:2048 sc1\n"
      "v_mfma_f32_16x16x32_bf16 v[132:135], v[60:63], a[220:223], v[132:135]\n"
      "s_waitcnt lgkmcnt(4)\n"
      "v_mfma_f32_16x16x32_bf16 v[136:139], v[32:35], a[208:211], v[136:139]\n"
      "ds_read_b128 v[56:59], v28 offset:34816\n"
      "v_mfma_f32_16x16x32_bf16 v[136:139], v[36:39], a[212:215], v[136:139]\n"
      "v_mfma_f32_16x16x32_bf16 v[140:143], v[32:35], a[216:219], v[140:143]\n"
      "ds_read_b128 v[60:63], v28 offset:35840\n"
      "v_mfma_f32_16x16x32_bf16 v[140:143], v[36:39], a[220:223], v[140:143]\n"
      "s_waitcnt lgkmcnt(4)\n"
      "v_mfma_f32_16x16x32_bf16 v[144:147], v[40:43], a[208:211], v[144:147]\n"
      "ds_read_b128 v[32:35], v28 offset:36864\n"
      "buffer_load_dwordx4 v[236:239], v1, s[36:39], s93 offen offset:3072 sc1\n"
      "v_mfma_f32_16x16x32_bf16 v[144:147], v[44:47], a[212:215], v[144:147]\n"
      "v_mfma_f32_16x16x32_bf16 v[148:151], v[40:43], a[216:219], v[148:151]\n"
      "ds_read_b128 v[36:39], v28 offset:37888\n"
      "v_mfma_f32_16x16x32_bf16 v[148:151], v[44:47], a[220:223], v[148:151]\n"
      "s_waitcnt lgkmcnt(4)\n"
      "s_add_u32 s93, s59, 69632\n"
      "v_mfma_f32_16x16x32_bf16 v[152:155], v[48:51], a[208:211], v[152:155]\n"
      "ds_read_b128 v[40:43], v28 offset:38912\n"
      "v_mfma_f32_16x16x32_bf16 v[152:155], v[52:55], a[212:215], v[152:155]\n"
      "buffer_load_dwordx4 v[240:243], v1, s[36:39], s93 offen sc1\n"
      "v_mfma_f32_16x16x32_bf16 v[156:159], v[48:51], a[216:219], v[156:159]\n"
      "ds_read_b128 v[44:47], v28 offset:39936\n"
      "v_mfma_f32_16x16x32_bf16 v[156:159], v[52:55], a[220:223], v[156:159]\n"
      "s_waitcnt lgkmcnt(4)\n"
      "v_mfma_f32_16x16x32_bf16 v[160:163], v[56:59], a[208:211], v[160:163]\n"
      "ds_read_b128 v[48:51], v28 offset:40960\n"
      "v_mfma_f32_16x16x32_bf16 v[160:163], v[60:63], a[212:215], v[160:163]\n"
      "v_mfma_f32_16x16x32_bf16 v[164:167], v[56:59], a[216:219], v[164:167]\n"
      "ds_read_b128 v[52:55], v28 offset:41984\n"
      "buffer_load_dwordx4 v[244:247], v1, s[36:39], s93 offen offset:1024 sc1\n"
      "v_mfma_f32_16x16x32_bf16 v[164:167], v[60:63], a[220:223], v[164:167]\n"
      "s_waitcnt lgkmcnt(4)\n"
      "v_mfma_f32_16x16x32_bf16 v[168:171], v[32:35], a[208:211], v[168:171]\n"
      "ds_read_b128 v[56:59], v28 offset:43008\n"
      "v_mfma_f32_16x16x32_bf16 v[168:171], v[36:39], a[212:215], v[168:171]\n"
      "v_mfma_f32_16x16x32_bf16 v[172:175], v[32:35], a[216:219], v[172:175]\n"
      "ds_read_b128 v[60:63], v28 offset:44032\n"
      "v_mfma_f32_16x16x32_bf16 v[172:175], v[36:39], a[220:223], v[172:175]\n"
      "s_waitcnt lgkmcnt(4)\n"
      "buffer_load_dwordx4 v[248:251], v1, s[36:39], s93 offen offset:2048 sc1\n"
      "v_mfma_f32_16x16x32_bf16 v[176:179], v[40:43], a[208:211], v[176:179]\n"
      "ds_read_b128 v[32:35], v28 offset:45056\n"
      "v_mfma_f32_16x16x32_bf16 v[176:179], v[44:47], a[212:215], v[176:179]\n"
      "v_mfma_f32_16x16x32_bf16 v[180:183], v[40:43], a[216:219], v[180:183]\n"
      "ds_read_b128 v[36:39], v28 offset:46080\n"
      "v_mfma_f32_16x16x32_bf16 v[180:183], v[44:47], a[220:223], v[180:183]\n"
      "s_waitcnt lgkmcnt(4)\n"
      "v_mfma_f32_16x16x32_bf16 v[184:187], v[48:51], a[208:211], v[184:187]\n"
      "ds_read_b128 v[40:43], v28 offset:47104\n"
      "v_mfma_f32_16x16x32_bf16 v[184:187], v[52:55], a[212:215], v[184:187]\n"
      "buffer_load_dwordx4 v[252:255], v1, s[36:39], s93 offen offset:3072 sc1\n"
      "v_mfma_f32_16x16x32_bf16 v[188:191], v[48:51], a[216:219], v[188:191]\n"
      "ds_read_b128 v[44:47], v28 offset:48128\n"
      "v_mfma_f32_16x16x32_bf16 v[188:191], v[52:55], a[220:223], v[188:191]\n"
      "s_waitcnt lgkmcnt(4)\n"
      "v_mfma_f32_16x16x32_bf16 v[80:83], v[56:59], a[208:211], v[80:83]\n"
      "s_waitcnt vmcnt(25)\n"
      "s_barrier\n"
      "ds_read_b128 v[48:51], v28 offset:49152\n"
      "v_mfma_f32_16x16x32_bf16 v[80:83], v[60:63], a[212:215], v[80:83]\n"
      "v_mfma_f32_16x16x32_bf16 v[84:87], v[56:59], a[216:219], v[84:87]\n"
      "ds_read_b128 v[52:55], v28 offset:50176\n"
      "v_mfma_f32_16x16x32_bf16 v[84:87], v[60:63], a[220:223], v[84:87]\n"
      "s_waitcnt lgkmcnt(4)\n"
      "s_waitcnt vmcnt(12)\n"
      "v_mfma_f32_16x16x32_bf16 v[128:131], v[32:35], a[224:227], v[128:131]\n"
      "ds_read_b128 v[56:59], v28 offset:51200\n"
      "v_mfma_f32_16x16x32_bf16 v[128:131], v[36:39], a[228:231], v[128:131]\n"
      "v_mfma_f32_16x16x32_bf16 v[132:135], v[32:35], a[232:235], v[132:135]\n"
      "ds_read_b128 v[60:63], v28 offset:52224\n"
      "v_mfma_f32_16x16x32_bf16 v[132:135], v[36:39], a[236:239], v[132:135]\n"
      "s_waitcnt lgkmcnt(4)\n"
      "s_waitcnt vmcnt(8)\n"
      "v_mfma_f32_16x16x32_bf16 v[128:131], v[40:43], a[240:243], v[128:131]\n"
      "ds_read_b128 v[32:35], v28 offset:53248\n"
      "v_mfma_f32_16x16x32_bf16 v[128:131], v[44:47], a[244:247], v[128:131]\n"
      "v_mfma_f32_16x16x32_bf16 v[132:135], v[40:43], a[248:251], v[132:135]\n"
      "ds_read_b128 v[36:39], v28 offset:54272\n"
      "v_mfma_f32_16x16x32_bf16 v[132:135], v[44:47], a[252:255], v[132:135]\n"
      "s_waitcnt lgkmcnt(4)\n"
      "s_waitcnt vmcnt(4)\n"
      "v_mfma_f32_16x16x32_bf16 v[128:131], v[48:51], v[224:227], v[128:131]\n"
      "ds_read_b128 v[40:43], v28 offset:55296\n"
      "v_mfma_f32_16x16x32_bf16 v[128:131], v[52:55], v[228:231], v[128:131]\n"
      "v_mfma_f32_16x16x32_bf16 v[132:135], v[48:51], v[232:235], v[132:135]\n"
      "ds_read_b128 v[44:47], v28 offset:56320\n"
      "v_mfma_f32_16x16x32_bf16 v[132:135], v[52:55], v[236:239], v[132:135]\n"
      "s_waitcnt lgkmcnt(4)\n"
      "s_waitcnt vmcnt(0)\n"
      "v_mfma_f32_16x16x32_bf16 v[128:131], v[56:59], v[240:243], v[128:131]\n"
      "ds_read_b128 v[48:51], v28 offset:57344\n"
      "v_mfma_f32_16x16x32_bf16 v[128:131], v[60:63], v[244:247], v[128:131]\n"
      "v_mfma_f32_16x16x32_bf16 v[132:135], v[56:59], v[248:251], v[132:135]\n"
      "ds_read_b128 v[52:55], v28 offset:58368\n"
      "v_mfma_f32_16x16x32_bf16 v[132:135], v[60:63], v[252:255], v[132:135]\n"
      "s_waitcnt lgkmcnt(4)\n"
      "v_mfma_f32_16x16x32_bf16 v[136:139], v[32:35], a[224:227], v[136:139]\n"
      "ds_read_b128 v[56:59], v28 offset:59392\n"
      "v_mfma_f32_16x16x32_bf16 v[136:139], v[36:39], a[228:231], v[136:139]\n"
      "v_mfma_f32_16x16x32_bf16 v[140:143], v[32:35], a[232:235], v[140:143]\n"
      "ds_read_b128 v[60:63], v28 offset:60416\n"
      "v_mfma_f32_16x16x32_bf16 v[140:143], v[36:39], a[236:239], v[140:143]\n"
      "s_waitcnt lgkmcnt(4)\n"
      "v_mfma_f32_16x16x32_bf16 v[136:139], v[40:43], a[240:243], v[136:139]\n"
      "ds_read_b128 v[32:35], v28 offset:61440\n"
      "v_cvt_pk_bf16_f32 v96, v128, v129\n"
      "v_mfma_f32_16x16x32_bf16 v[136:139], v[44:47], a[244:247], v[136:139]\n"
      "v_cvt_pk_bf16_f32 v98, v132, v133\n"
      "v_cvt_pk_bf16_f32 v97, v130, v131\n"
      "v_mfma_f32_16x16x32_bf16 v[140:143], v[40:43], a[248:251], v[140:143]\n"
      "ds_read_b128 v[36:39], v28 offset:62464\n"
      "v_cvt_pk_bf16_f32 v99, v134, v135\n"
      "v_mfma_f32_16x16x32_bf16 v[140:143], v[44:47], a[252:255], v[140:143]\n"
      "s_waitcnt lgkmcnt(4)\n"
      "v_permlane16_swap_b32 v96, v98\n"
      "v_mfma_f32_16x16x32_bf16 v[136:139], v[48:51], v[224:227], v[136:139]\n"
      "ds_read_b128 v[40:43], v28 offset:63488\n"
      "v_permlane16_swap_b32 v97, v99\n"
      "v_mfma_f32_16x16x32_bf16 v[136:139], v[52:55], v[228:231], v[136:139]\n"
      "buffer_store_dwordx4 v[96:99], v73, s[36:39], s33 offen sc1\n"
      "v_mfma_f32_16x16x32_bf16 v[140:143], v[48:51], v[232:235], v[140:143]\n"
      "ds_read_b128 v[44:47], v28 offset:64512\n"
      "v_mfma_f32_16x16x32_bf16 v[140:143], v[52:55], v[236:239], v[140:143]\n"
      "s_waitcnt lgkmcnt(4)\n"
      "v_mfma_f32_16x16x32_bf16 v[136:139], v[56:59], v[240:243], v[136:139]\n"
      "ds_read_b128 v[48:51], v29\n"
      "v_mfma_f32_16x16x32_bf16 v[136:139], v[60:63], v[244:247], v[136:139]\n"
      "v_mfma_f32_16x16x32_bf16 v[140:143], v[56:59], v[248:251], v[140:143]\n"
      "ds_read_b128 v[52:55], v29 offset:1024\n"
      "v_mfma_f32_16x16x32_bf16 v[140:143], v[60:63], v[252:255], v[140:143]\n"
      "s_waitcnt lgkmcnt(4)\n"
      "v_mfma_f32_16x16x32_bf16 v[144:147], v[32:35], a[224:227], v[144:147]\n"
      "ds_read_b128 v[56:59], v29 offset:2048\n"
      "v_mfma_f32_16x16x32_bf16 v[144:147], v[36:39], a[228:231], v[144:147]\n"
      "v_mfma_f32_16x16x32_bf16 v[148:151], v[32:35], a[232:235], v[148:151]\n"
      "ds_read_b128 v[60:63], v29 offset:3072\n"
      "v_mfma_f32_16x16x32_bf16 v[148:151], v[36:39], a[236:239], v[148:151]\n"
      "s_waitcnt lgkmcnt(4)\n"
      "v_mfma_f32_16x16x32_bf16 v[144:147], v[40:43], a[240:243], v[144:147]\n"
      "ds_read_b128 v[32:35], v29 offset:4096\n"
      "v_cvt_pk_bf16_f32 v100, v136, v137\n"
      "v_mfma_f32_16x16x32_bf16 v[144:147], v[44:47], a[244:247], v[144:147]\n"
      "v_cvt_pk_bf16_f32 v102, v140, v141\n"
      "v_cvt_pk_bf16_f32 v101, v138, v139\n"
      "v_mfma_f32_16x16x32_bf16 v[148:151], v[40:43], a[248:251], v[148:151]\n"
      "ds_read_b128 v[36:39], v29 offset:5120\n"
      "v_cvt_pk_bf16_f32 v103, v142, v143\n"
      "v_mfma_f32_16x16x32_bf16 v[148:151], v[44:47], a[252:255], v[148:151]\n"
      "s_waitcnt lgkmcnt(4)\n"
      "v_permlane16_swap_b32 v100, v102\n"
      "v_mfma_f32_16x16x32_bf16 v[144:147], v[48:51], v[224:227], v[144:147]\n"
      "ds_read_b128 v[40:43], v29 offset:6144\n"
      "v_permlane16_swap_b32 v101, v103\n"
      "v_mfma_f32_16x16x32_bf16 v[144:147], v[52:55], v[228:231], v[144:147]\n"
      "buffer_store_dwordx4 v[100:103], v73, s[36:39], s33 offen offset:1024 sc1\n"
      "v_mfma_f32_16x16x32_bf16 v[148:151], v[48:51], v[232:235], v[148:151]\n"
      "ds_read_b128 v[44:47], v29 offset:7168\n"
      "v_mfma_f32_16x16x32_bf16 v[148:151], v[52:55], v[236:239], v[148:151]\n"
      "s_waitcnt lgkmcnt(4)\n"
      "v_mfma_f32_16x16x32_bf16 v[144:147], v[56:59], v[240:243], v[144:147]\n"
      "ds_read_b128 v[48:51], v29 offset:8192\n"
      "v_mfma_f32_16x16x32_bf16 v[144:147], v[60:63], v[244:247], v[144:147]\n"
      "v_mfma_f32_16x16x32_bf16 v[148:151], v[56:59], v[248:251], v[148:151]\n"
      "ds_read_b128 v[52:55], v29 offset:9216\n"
      "v_mfma_f32_16x16x32_bf16 v[148:151], v[60:63], v[252:255], v[148:151]\n"
      "s_waitcnt lgkmcnt(4)\n"
      "v_mfma_f32_16x16x32_bf16 v[152:155], v[32:35], a[224:227], v[152:155]\n"
      "ds_read_b128 v[56:59], v29 offset:10240\n"
      "v_mfma_f32_16x16x32_bf16 v[152:155], v[36:39], a[228:231], v[152:155]\n"
      "v_mfma_f32_16x16x32_bf16 v[156:159], v[32:35], a[232:235], v[156:159]\n"
      "ds_read_b128 v[60:63], v29 offset:11264\n"
      "v_mfma_f32_16x16x32_bf16 v[156:159], v[36:39], a[236:239], v[156:159]\n"
      "s_waitcnt lgkmcnt(4)\n"
      "v_mfma_f32_16x16x32_bf16 v[152:155], v[40:43], a[240:243], v[152:155]\n"
      "ds_read_b128 v[32:35], v29 offset:12288\n"
      "v_cvt_pk_bf16_f32 v104, v144, v145\n"
      "v_mfma_f32_16x16x32_bf16 v[152:155], v[44:47], a[244:247], v[152:155]\n"
      "v_cvt_pk_bf16_f32 v106, v148, v149\n"
      "v_cvt_pk_bf16_f32 v105, v146, v147\n"
      "v_mfma_f32_16x16x32_bf16 v[156:159], v[40:43], a[248:251], v[156:159]\n"
      "ds_read_b128 v[36:39], v29 offset:13312\n"
      "v_cvt_pk_bf16_f32 v107, v150, v151\n"
      "v_mfma_f32_16x16x32_bf16 v[156:159], v[44:47], a[252:255], v[156:159]\n"
      "s_waitcnt lgkmcnt(4)\n"
      "v_permlane16_swap_b32 v104, v106\n"
      "v_mfma_f32_16x16x32_bf16 v[152:155], v[48:51], v[224:227], v[152:155]\n"
      "ds_read_b128 v[40:43], v29 offset:14336\n"
      "v_permlane16_swap_b32 v105, v107\n"
      "v_mfma_f32_16x16x32_bf16 v[152:155], v[52:55], v[228:231], v[152:155]\n"
      "buffer_store_dwordx4 v[104:107], v73, s[36:39], s33 offen offset:2048 sc1\n"
      "v_mfma_f32_16x16x32_bf16 v[156:159], v[48:51], v[232:235], v[156:159]\n"
      "ds_read_b128 v[44:47], v29 offset:15360\n"
      "v_mfma_f32_16x16x32_bf16 v[156:159], v[52:55], v[236:239], v[156:159]\n"
      "s_waitcnt lgkmcnt(4)\n"
      "v_mfma_f32_16x16x32_bf16 v[152:155], v[56:59], v[240:243], v[152:155]\n"
      "s_barrier\n"
      "ds_read_b128 v[48:51], v29 offset:16384\n"
      "v_mfma_f32_16x16x32_bf16 v[152:155], v[60:63], v[244:247], v[152:155]\n"
      "v_mfma_f32_16x16x32_bf16 v[156:159], v[56:59], v[248:251], v[156:159]\n"
      "ds_read_b128 v[52:55], v29 offset:17408\n"
      "v_mfma_f32_16x16x32_bf16 v[156:159], v[60:63], v[252:255], v[156:159]\n"
      "s_waitcnt lgkmcnt(4)\n"
      "v_mfma_f32_16x16x32_bf16 v[160:163], v[32:35], a[224:227], v[160:163]\n"
      "ds_read_b128 v[56:59], v29 offset:18432\n"
      "v_mfma_f32_16x16x32_bf16 v[160:163], v[36:39], a[228:231], v[160:163]\n"
      "v_mfma_f32_16x16x32_bf16 v[164:167], v[32:35], a[232:235], v[164:167]\n"
      "ds_read_b128 v[60:63], v29 offset:19456\n"
      "v_mfma_f32_16x16x32_bf16 v[164:167], v[36:39], a[236:239], v[164:167]\n"
      "s_waitcnt lgkmcnt(4)\n"
      "v_mfma_f32_16x16x32_bf16 v[160:163], v[40:43], a[240:243], v[160:163]\n"
      "ds_read_b128 v[32:35], v29 offset:20480\n"
      "v_cvt_pk_bf16_f32 v108, v152, v153\n"
      "v_mfma_f32_16x16x32_bf16 v[160:163], v[44:47], a[244:247], v[160:163]\n"
      "v_cvt_pk_bf16_f32 v110, v156, v157\n"
      "v_cvt_pk_bf16_f32 v109, v154, v155\n"
      "v_mfma_f32_16x16x32_bf16 v[164:167], v[40:43], a[248:251], v[164:167]\n"
      "ds_read_b128 v[36:39], v29 offset:21504\n"
      "v_cvt_pk_bf16_f32 v111, v158, v159\n"
      "v_mfma_f32_16x16x32_bf16 v[164:167], v[44:47], a[252:255], v[164:167]\n"
      "s_waitcnt lgkmcnt(4)\n"
      "v_permlane16_swap_b32 v108, v110\n"
      "v_mfma_f32_16x16x32_bf16 v[160:163], v[48:51], v[224:227], v[160:163]\n"
      "ds_read_b128 v[40:43], v29 offset:22528\n"
      "v_permlane16_swap_b32 v109, v111\n"
      "v_mfma_f32_16x16x32_bf16 v[160:163], v[52:55], v[228:231], v[160:163]\n"
      "buffer_store_dwordx4 v[108:111], v73, s[36:39], s33 offen offset:3072 sc1\n"
      "v_mfma_f32_16x16x32_bf16 v[164:167], v[48:51], v[232:235], v[164:167]\n"
      "ds_read_b128 v[44:47], v29 offset:23552\n"
      "v_mfma_f32_16x16x32_bf16 v[164:167], v[52:55], v[236:239], v[164:167]\n"
      "s_waitcnt lgkmcnt(4)\n"
      "v_mfma_f32_16x16x32_bf16 v[160:163], v[56:59], v[240:243], v[160:163]\n"
      "ds_read_b128 v[48:51], v29 offset:24576\n"
      "v_mfma_f32_16x16x32_bf16 v[160:163], v[60:63], v[244:247], v[160:163]\n"
      "v_mfma_f32_16x16x32_bf16 v[164:167], v[56:59], v[248:251], v[164:167]\n"
      "ds_read_b128 v[52:55], v29 offset:25600\n"
      "v_mfma_f32_16x16x32_bf16 v[164:167], v[60:63], v[252:255], v[164:167]\n"
      "s_waitcnt lgkmcnt(4)\n"
      "v_mfma_f32_16x16x32_bf16 v[168:171], v[32:35], a[224:227], v[168:171]\n"
      "ds_read_b128 v[56:59], v29 offset:26624\n"
      "v_mfma_f32_16x16x32_bf16 v[168:171], v[36:39], a[228:231], v[168:171]\n"
      "v_mfma_f32_16x16x32_bf16 v[172:175], v[32:35], a[232:235], v[172:175]\n"
      "ds_read_b128 v[60:63], v29 offset:27648\n"
      "v_mfma_f32_16x16x32_bf16 v[172:175], v[36:39], a[236:239], v[172:175]\n"
      "s_waitcnt lgkmcnt(4)\n"
      "v_mfma_f32_16x16x32_bf16 v[168:171], v[40:43], a[240:243], v[168:171]\n"
      "ds_read_b128 v[32:35], v29 offset:28672\n"
      "v_cvt_pk_bf16_f32 v96, v160, v161\n"
      "v_mfma_f32_16x16x32_bf16 v[168:171], v[44:47], a[244:247], v[168:171]\n"
      "v_cvt_pk_bf16_f32 v98, v164, v165\n"
      "v_cvt_pk_bf16_f32 v97, v162, v163\n"
      "v_mfma_f32_16x16x32_bf16 v[172:175], v[40:43], a[248:251], v[172:175]\n"
      "ds_read_b128 v[36:39], v29 offset:29696\n"
      "v_cvt_pk_bf16_f32 v99, v166, v167\n"
      "v_mfma_f32_16x16x32_bf16 v[172:175], v[44:47], a[252:255], v[172:175]\n"
      "s_waitcnt lgkmcnt(4)\n"
      "v_permlane16_swap_b32 v96, v98\n"
      "v_mfma_f32_16x16x32_bf16 v[168:171], v[48:51], v[224:227], v[168:171]\n"
      "ds_read_b128 v[40:43], v29 offset:30720\n"
      "v_permlane16_swap_b32 v97, v99\n"
      "v_mfma_f32_16x16x32_bf16 v[168:171], v[52:55], v[228:231], v[168:171]\n"
      "buffer_store_dwordx4 v[96:99], v73, s[36:39], s18 offen sc1\n"
      "v_mfma_f32_16x16x32_bf16 v[172:175], v[48:51], v[232:235], v[172:175]\n"
      "ds_read_b128 v[44:47], v29 offset:31744\n"
      "v_mfma_f32_16x16x32_bf16 v[172:175], v[52:55], v[236:239], v[172:175]\n"
      "s_waitcnt lgkmcnt(4)\n"
      "v_mfma_f32_16x16x32_bf16 v[168:171], v[56:59], v[240:243], v[168:171]\n"
      "ds_read_b128 v[48:51], v1\n"
      "v_mfma_f32_16x16x32_bf16 v[168:171], v[60:63], v[244:247], v[168:171]\n"
      "v_mfma_f32_16x16x32_bf16 v[172:175], v[56:59], v[248:251], v[172:175]\n"
      "ds_read_b128 v[52:55], v1 offset:1024\n"
      "v_mfma_f32_16x16x32_bf16 v[172:175], v[60:63], v[252:255], v[172:175]\n"
      "s_waitcnt lgkmcnt(4)\n"
      "v_mfma_f32_16x16x32_bf16 v[176:179], v[32:35], a[224:227], v[176:179]\n"
      "ds_read_b128 v[56:59], v1 offset:2048\n"
      "v_mfma_f32_16x16x32_bf16 v[176:179], v[36:39], a[228:231], v[176:179]\n"
      "v_mfma_f32_16x16x32_bf16 v[180:183], v[32:35], a[232:235], v[180:183]\n"
      "ds_read_b128 v[60:63], v1 offset:3072\n"
      "v_mfma_f32_16x16x32_bf16 v[180:183], v[36:39], a[236:239], v[180:183]\n"
      "s_waitcnt lgkmcnt(4)\n"
      "v_mfma_f32_16x16x32_bf16 v[176:179], v[40:43], a[240:243], v[176:179]\n"
      "ds_read_b128 v[32:35], v1 offset:4096\n"
      "v_cvt_pk_bf16_f32 v100, v168, v169\n"
      "v_mfma_f32_16x16x32_bf16 v[176:179], v[44:47], a[244:247], v[176:179]\n"
      "v_cvt_pk_bf16_f32 v102, v172, v173\n"
      "v_cvt_pk_bf16_f32 v101, v170, v171\n"
      "v_mfma_f32_16x16x32_bf16 v[180:183], v[40:43], a[248:251], v[180:183]\n"
      "ds_read_b128 v[36:39], v1 offset:5120\n"
      "v_cvt_pk_bf16_f32 v103, v174, v175\n"
      "v_mfma_f32_16x16x32_bf16 v[180:183], v[44:47], a[252:255], v[180:183]\n"
      "s_waitcnt lgkmcnt(4)\n"
      "v_permlane16_swap_b32 v100, v102\n"
      "v_mfma_f32_16x16x32_bf16 v[176:179], v[48:51], v[224:227], v[176:179]\n"
      "ds_read_b128 v[40:43], v1 offset:6144\n"
      "v_permlane16_swap_b32 v101, v103\n"
      "v_mfma_f32_16x16x32_bf16 v[176:179], v[52:55], v[228:231], v[176:179]\n"
      "buffer_store_dwordx4 v[100:103], v73, s[36:39], s18 offen offset:1024 sc1\n"
      "v_mfma_f32_16x16x32_bf16 v[180:183], v[48:51], v[232:235], v[180:183]\n"
      "ds_read_b128 v[44:47], v1 offset:7168\n"
      "v_mfma_f32_16x16x32_bf16 v[180:183], v[52:55], v[236:239], v[180:183]\n"
      "s_waitcnt lgkmcnt(4)\n"
      "v_mfma_f32_16x16x32_bf16 v[176:179], v[56:59], v[240:243], v[176:179]\n"
      "ds_read_b128 v[48:51], v1 offset:8192\n"
      "v_mfma_f32_16x16x32_bf16 v[176:179], v[60:63], v[244:247], v[176:179]\n"
      "v_mfma_f32_16x16x32_bf16 v[180:183], v[56:59], v[248:251], v[180:183]\n"
      "ds_read_b128 v[52:55], v1 offset:9216\n"
      "v_mfma_f32_16x16x32_bf16 v[180:183], v[60:63], v[252:255], v[180:183]\n"
      "s_waitcnt lgkmcnt(4)\n"
      "v_mfma_f32_16x16x32_bf16 v[184:187], v[32:35], a[224:227], v[184:187]\n"
      "ds_read_b128 v[56:59], v1 offset:10240\n"
      "v_mfma_f32_16x16x32_bf16 v[184:187], v[36:39], a[228:231], v[184:187]\n"
      "v_mfma_f32_16x16x32_bf16 v[188:191], v[32:35], a[232:235], v[188:191]\n"
      "ds_read_b128 v[60:63], v1 offset:11264\n"
      "v_mfma_f32_16x16x32_bf16 v[188:191], v[36:39], a[236:239], v[188:191]\n"
      "s_waitcnt lgkmcnt(4)\n"
      "v_mfma_f32_16x16x32_bf16 v[184:187], v[40:43], a[240:243], v[184:187]\n"
      "ds_read_b128 v[32:35], v1 offset:12288\n"
      "v_cvt_pk_bf16_f32 v104, v176, v177\n"
      "v_mfma_f32_16x16x32_bf16 v[184:187], v[44:47], a[244:247], v[184:187]\n"
      "v_cvt_pk_bf16_f32 v106, v180, v181\n"
      "v_cvt_pk_bf16_f32 v105, v178, v179\n"
      "v_mfma_f32_16x16x32_bf16 v[188:191], v[40:43], a[248:251], v[188:191]\n"
      "ds_read_b128 v[36:39], v1 offset:13312\n"
      "v_cvt_pk_bf16_f32 v107, v182, v183\n"
      "v_mfma_f32_16x16x32_bf16 v[188:191], v[44:47], a[252:255], v[188:191]\n"
      "s_waitcnt lgkmcnt(4)\n"
      "v_permlane16_swap_b32 v104, v106\n"
      "v_mfma_f32_16x16x32_bf16 v[184:187], v[48:51], v[224:227], v[184:187]\n"
      "ds_read_b128 v[40:43], v1 offset:14336\n"
      "v_permlane16_swap_b32 v105, v107\n"
      "v_mfma_f32_16x16x32_bf16 v[184:187], v[52:55], v[228:231], v[184:187]\n"
      "buffer_store_dwordx4 v[104:107], v73, s[36:39], s18 offen offset:2048 sc1\n"
      "v_mfma_f32_16x16x32_bf16 v[188:191], v[48:51], v[232:235], v[188:191]\n"
      "ds_read_b128 v[44:47], v1 offset:15360\n"
      "v_mfma_f32_16x16x32_bf16 v[188:191], v[52:55], v[236:239], v[188:191]\n"
      "s_waitcnt lgkmcnt(4)\n"
      "v_mfma_f32_16x16x32_bf16 v[184:187], v[56:59], v[240:243], v[184:187]\n"
      "s_barrier\n"
      "ds_read_b128 v[48:51], v1 offset:16384\n"
      "v_mfma_f32_16x16x32_bf16 v[184:187], v[60:63], v[244:247], v[184:187]\n"
      "v_mfma_f32_16x16x32_bf16 v[188:191], v[56:59], v[248:251], v[188:191]\n"
      "ds_read_b128 v[52:55], v1 offset:17408\n"
      "v_mfma_f32_16x16x32_bf16 v[188:191], v[60:63], v[252:255], v[188:191]\n"
      "s_waitcnt lgkmcnt(4)\n"
      "v_mfma_f32_16x16x32_bf16 v[80:83], v[32:35], a[224:227], v[80:83]\n"
      "ds_read_b128 v[56:59], v1 offset:18432\n"
      "v_mfma_f32_16x16x32_bf16 v[80:83], v[36:39], a[228:231], v[80:83]\n"
      "v_mfma_f32_16x16x32_bf16 v[84:87], v[32:35], a[232:235], v[84:87]\n"
      "ds_read_b128 v[60:63], v1 offset:19456\n"
      "v_mfma_f32_16x16x32_bf16 v[84:87], v[36:39], a[236:239], v[84:87]\n"
      "s_waitcnt lgkmcnt(4)\n"
      "v_mfma_f32_16x16x32_bf16 v[80:83], v[40:43], a[240:243], v[80:83]\n"
      "v_cvt_pk_bf16_f32 v108, v184, v185\n"
      "v_cvt_pk_bf16_f32 v110, v188, v189\n"
      "v_mfma_f32_16x16x32_bf16 v[80:83], v[44:47], a[244:247], v[80:83]\n"
      "v_cvt_pk_bf16_f32 v109, v186, v187\n"
      "v_cvt_pk_bf16_f32 v111, v190, v191\n"
      "v_mfma_f32_16x16x32_bf16 v[84:87], v[40:43], a[248:251], v[84:87]\n"
      "v_permlane16_swap_b32 v108, v110\n"
      "v_permlane16_swap_b32 v109, v111\n"
      "v_mfma_f32_16x16x32_bf16 v[84:87], v[44:47], a[252:255], v[84:87]\n"
      "s_waitcnt lgkmcnt(2)\n"
      "buffer_store_dwordx4 v[108:111], v73, s[36:39], s18 offen offset:3072 sc1\n"
      "v_mfma_f32_16x16x32_bf16 v[80:83], v[48:51], v[224:227], v[80:83]\n"
      "v_mfma_f32_16x16x32_bf16 v[80:83], v[52:55], v[228:231], v[80:83]\n"
      "v_mfma_f32_16x16x32_bf16 v[84:87], v[48:51], v[232:235], v[84:87]\n"
      "v_mfma_f32_16x16x32_bf16 v[84:87], v[52:55], v[236:239], v[84:87]\n"
      "s_waitcnt lgkmcnt(0)\n"
      "v_mfma_f32_16x16x32_bf16 v[80:83], v[56:59], v[240:243], v[80:83]\n"
      "v_mfma_f32_16x16x32_bf16 v[80:83], v[60:63], v[244:247], v[80:83]\n"
      "v_mfma_f32_16x16x32_bf16 v[84:87], v[56:59], v[248:251], v[84:87]\n"
      "v_mfma_f32_16x16x32_bf16 v[84:87], v[60:63], v[252:255], v[84:87]\n"
      "s_nop 5\n"
      "v_cvt_pk_bf16_f32 v96, v80, v81\n"
      "s_nop 0\n"
      "v_cvt_pk_bf16_f32 v98, v84, v85\n"
      "v_cvt_pk_bf16_f32 v97, v82, v83\n"
      "v_cvt_pk_bf16_f32 v99, v86, v87\n"
      "v_permlane16_swap_b32 v96, v98\n"
      "s_nop 0\n"
      "v_permlane16_swap_b32 v97, v99\n"
      "buffer_store_dwordx4 v[96:99], v73, s[36:39], s19 offen sc1\n"
      "s_mov_b32 s64, s24\n"
      "s_and_b32 s65, s25, 0xffff\n"
      "s_mov_b32 s66, 0x240000\n"
      "s_mov_b32 s67, 0x20000\n"
      "v_lshlrev_b32 v144, 16, v24\n"
      "v_and_b32 v156, 0xffff0000, v24\n"
      "v_lshlrev_b32 v145, 16, v25\n"
      "v_and_b32 v157, 0xffff0000, v25\n"
      "v_lshlrev_b32 v146, 16, v26\n"
      "v_and_b32 v158, 0xffff0000, v26\n"
      "v_lshlrev_b32 v147, 16, v27\n"
      "v_and_b32 v159, 0xffff0000, v27\n"
      "v_lshlrev_b32 v148, 16, v8\n"
      "v_and_b32 v160, 0xffff0000, v8\n"
      "v_lshlrev_b32 v149, 16, v10\n"
      "v_and_b32 v161, 0xffff0000, v10\n"
      "v_lshlrev_b32 v150, 16, v12\n"
      "v_and_b32 v162, 0xffff0000, v12\n"
      "v_lshlrev_b32 v151, 16, v14\n"
      "v_and_b32 v163, 0xffff0000, v14\n"
      "v_lshlrev_b32 v152, 16, v15\n"
      "v_and_b32 v164, 0xffff0000, v15\n"
      "v_lshlrev_b32 v153, 16, v64\n"
      "v_and_b32 v165, 0xffff0000, v64\n"
      "v_lshlrev_b32 v154, 16, v65\n"
      "v_and_b32 v166, 0xffff0000, v65\n"
      "v_lshlrev_b32 v155, 16, v30\n"
      "v_and_b32 v167, 0xffff0000, v30\n"
      "v_mov_b32 v66, 0x3f317218\n"
      "s_lshl_b32 s94, s72, 15\n"
      "s_add_u32 s94, s94, s7\n"
      "s_add_u32 s94, s94, 0x80000\n"
      "s_lshl_b32 s20, s30, 15\n"
      "s_add_u32 s20, s20, 0x80000\n"
      "s_waitcnt vmcnt(0)\n"
      "buffer_store_dwordx2 v[74:75], v76, s[36:39], s94 offen sc1\n"
      "s_cmp_eq_u32 s72, 3\n"
      "s_cbranch_scc1 Lend_%=\n"
      "s_sleep 12\n"
      "s_mov_b32 s8, 0\n"
      "Lpoll_37_%=:\n"
      "buffer_load_dwordx2 v[70:71], v68, s[36:39], s20 offen sc1\n"
      "s_add_u32 s8, s8, 1\n"
      "s_waitcnt vmcnt(0) lgkmcnt(0)\n"
      "v_cmp_ne_u64 vcc, s[76:77], v[70:71]\n"
      "s_cbranch_vccz Lpolld_38_%=\n"
      "s_cmp_lt_u32 s8, 40000\n"
      "s_cbranch_scc1 Lpoll_37_%=\n"
      "Lpolld_38_%=:\n"
      "s_add_u32 s95, s12, 0\n"
      "buffer_load_dwordx4 v[96:99], v1, s[36:39], s95 offen sc1\n"
      "s_add_u32 s95, s12, 9216\n"
      "buffer_load_dwordx4 v[100:103], v1, s[36:39], s95 offen sc1\n"
      "s_add_u32 s95, s12, 18432\n"
      "buffer_load_dwordx4 v[104:107], v1, s[36:39], s95 offen sc1\n"
      "s_add_u32 s95, s12, 27648\n"
      "buffer_load_dwordx4 v[108:111], v1, s[36:39], s95 offen sc1\n"
      "s_add_u32 s95, s12, 3072\n"
      "buffer_load_dwordx4 v[112:115], v1, s[36:39], s95 offen sc1\n"
      "s_add_u32 s95, s12, 12288\n"
      "buffer_load_dwordx4 v[116:119], v1, s[36:39], s95 offen sc1\n"
      "s_add_u32 s95, s12, 21504\n"
      "buffer_load_dwordx4 v[120:123], v1, s[36:39], s95 offen sc1\n"
      "s_add_u32 s95, s12, 30720\n"
      "buffer_load_dwordx4 v[124:127], v1, s[36:39], s95 offen sc1\n"
      "s_add_u32 s95, s12, 6144\n"
      "buffer_load_dwordx4 v[128:131], v1, s[36:39], s95 offen sc1\n"
      "s_add_u32 s95, s12, 15360\n"
      "buffer_load_dwordx4 v[132:135], v1, s[36:39], s95 offen sc1\n"
      "s_add_u32 s95, s12, 24576\n"
      "buffer_load_dwordx4 v[136:139], v1, s[36:39], s95 offen sc1\n"
      "s_add_u32 s95, s12, 33792\n"
      "buffer_load_dwordx4 v[140:143], v1, s[36:39], s95 offen sc1\n"
      "s_waitcnt vmcnt(11)\n"
      "v_lshlrev_b32 v168, 16, v96\n"
      "v_lshlrev_b32 v169, 16, v97\n"
      "v_lshlrev_b32 v170, 16, v98\n"
      "v_lshlrev_b32 v171, 16, v99\n"
      "v_and_b32 v96, 0xffff0000, v96\n"
      "v_and_b32 v97, 0xffff0000, v97\n"
      "v_and_b32 v98, 0xffff0000, v98\n"
      "v_and_b32 v99, 0xffff0000, v99\n"
      "v_pk_fma_f32 v[144:145], v[168:169], v[66:67], v[144:145] op_sel_hi:[1,0,1]\n"
      "v_pk_fma_f32 v[156:157], v[96:97], v[66:67], v[156:157] op_sel_hi:[1,0,1]\n"
      "v_pk_fma_f32 v[146:147], v[170:171], v[66:67], v[146:147] op_sel_hi:[1,0,1]\n"
      "v_pk_fma_f32 v[158:159], v[98:99], v[66:67], v[158:159] op_sel_hi:[1,0,1]\n"
      "s_waitcnt vmcnt(10)\n"
      "v_lshlrev_b32 v168, 16, v100\n"
      "v_lshlrev_b32 v169, 16, v101\n"
      "v_lshlrev_b32 v170, 16, v102\n"
      "v_lshlrev_b32 v171, 16, v103\n"
      "v_and_b32 v100, 0xffff0000, v100\n"
      "v_and_b32 v101, 0xffff0000, v101\n"
      "v_and_b32 v102, 0xffff0000, v102\n"
      "v_and_b32 v103, 0xffff0000, v103\n"
      "v_pk_fma_f32 v[144:145], v[168:169], v[66:67], v[144:145] op_sel_hi:[1,0,1]\n"
      "v_pk_fma_f32 v[156:157], v[100:101], v[66:67], v[156:157] op_sel_hi:[1,0,1]\n"
      "v_pk_fma_f32 v[146:147], v[170:171], v[66:67], v[146:147] op_sel_hi:[1,0,1]\n"
      "v_pk_fma_f32 v[158:159], v[102:103], v[66:67], v[158:159] op_sel_hi:[1,0,1]\n"
      "s_waitcnt vmcnt(9)\n"
      "v_lshlrev_b32 v168, 16, v104\n"
      "v_lshlrev_b32 v169, 16, v105\n"
      "v_lshlrev_b32 v170, 16, v106\n"
      "v_lshlrev_b32 v171, 16, v107\n"
      "v_and_b32 v104, 0xffff0000, v104\n"
      "v_and_b32 v105, 0xffff0000, v105\n"
      "v_and_b32 v106, 0xffff0000, v106\n"
      "v_and_b32 v107, 0xffff0000, v107\n"
      "v_pk_fma_f32 v[144:145], v[168:169], v[66:67], v[144:145] op_sel_hi:[1,0,1]\n"
      "v_pk_fma_f32 v[156:157], v[104:105], v[66:67], v[156:157] op_sel_hi:[1,0,1]\n"
      "v_pk_fma_f32 v[146:147], v[170:171], v[66:67], v[146:147] op_sel_hi:[1,0,1]\n"
      "v_pk_fma_f32 v[158:159], v[106:107], v[66:67], v[158:159] op_sel_hi:[1,0,1]\n"
      "s_waitcnt vmcnt(8)\n"
      "v_lshlrev_b32 v168, 16, v108\n"
      "v_lshlrev_b32 v169, 16, v109\n"
      "v_lshlrev_b32 v170, 16, v110\n"
      "v_lshlrev_b32 v171, 16, v111\n"
      "v_and_b32 v108, 0xffff0000, v108\n"
      "v_and_b32 v109, 0xffff0000, v109\n"
      "v_and_b32 v110, 0xffff0000, v110\n"
      "v_and_b32 v111, 0xffff0000, v111\n"
      "v_pk_fma_f32 v[144:145], v[168:169], v[66:67], v[144:145] op_sel_hi:[1,0,1]\n"
      "v_pk_fma_f32 v[156:157], v[108:109], v[66:67], v[156:157] op_sel_hi:[1,0,1]\n"
      "v_pk_fma_f32 v[146:147], v[170:171], v[66:67], v[146:147] op_sel_hi:[1,0,1]\n"
      "v_pk_fma_f32 v[158:159], v[110:111], v[66:67], v[158:159] op_sel_hi:[1,0,1]\n"
      "v_cvt_pk_bf16_f32 v200, v144, v156\n"
      "v_cvt_pk_bf16_f32 v201, v145, v157\n"
      "v_cvt_pk_bf16_f32 v202, v146, v158\n"
      "v_cvt_pk_bf16_f32 v203, v147, v159\n"
      "buffer_store_dwordx4 v[200:203], v31, s[64:67], s34 offen\n"
      "s_waitcnt vmcnt(8)\n"
      "v_lshlrev_b32 v168, 16, v112\n"
      "v_lshlrev_b32 v169, 16, v113\n"
      "v_lshlrev_b32 v170, 16, v114\n"
      "v_lshlrev_b32 v171, 16, v115\n"
      "v_and_b32 v112, 0xffff0000, v112\n"
      "v_and_b32 v113, 0xffff0000, v113\n"
      "v_and_b32 v114, 0xffff0000, v114\n"
      "v_and_b32 v115, 0xffff0000, v115\n"
      "v_pk_fma_f32 v[148:149], v[168:169], v[66:67], v[148:149] op_sel_hi:[1,0,1]\n"
      "v_pk_fma_f32 v[160:161], v[112:113], v[66:67], v[160:161] op_sel_hi:[1,0,1]\n"
      "v_pk_fma_f32 v[150:151], v[170:171], v[66:67], v[150:151] op_sel_hi:[1,0,1]\n"
      "v_pk_fma_f32 v[162:163], v[114:115], v[66:67], v[162:163] op_sel_hi:[1,0,1]\n"
      "s_waitcnt vmcnt(7)\n"
      "v_lshlrev_b32 v168, 16, v116\n"
      "v_lshlrev_b32 v169, 16, v117\n"
      "v_lshlrev_b32 v170, 16, v118\n"
      "v_lshlrev_b32 v171, 16, v119\n"
      "v_and_b32 v116, 0xffff0000, v116\n"
      "Lpad14_%=:\n"
      "s_cbranch_execz Lret_%=\n"
      "v_and_b32 v117, 0xffff0000, v117\n"
      "v_and_b32 v118, 0xffff0000, v118\n"
      "v_and_b32 v119, 0xffff0000, v119\n"
      "v_pk_fma_f32 v[148:149], v[168:169], v[66:67], v[148:149] op_sel_hi:[1,0,1]\n"
      "v_pk_fma_f32 v[160:161], v[116:117], v[66:67], v[160:161] op_sel_hi:[1,0,1]\n"
      "v_pk_fma_f32 v[150:151], v[170:171], v[66:67], v[150:151] op_sel_hi:[1,0,1]\n"
      "v_pk_fma_f32 v[162:163], v[118:119], v[66:67], v[162:163] op_sel_hi:[1,0,1]\n"
      "s_waitcnt vmcnt(6)\n"
      "v_lshlrev_b32 v168, 16, v120\n"
      "v_lshlrev_b32 v169, 16, v121\n"
      "v_lshlrev_b32 v170, 16, v122\n"
      "v_lshlrev_b32 v171, 16, v123\n"
      "v_and_b32 v120, 0xffff0000, v120\n"
      "v_and_b32 v121, 0xffff0000, v121\n"
      "v_and_b32 v122, 0xffff0000, v122\n"
      "v_and_b32 v123, 0xffff0000, v123\n"
      "v_pk_fma_f32 v[148:149], v[168:169], v[66:67], v[148:149] op_sel_hi:[1,0,1]\n"
      "v_pk_fma_f32 v[160:161], v[120:121], v[66:67], v[160:161] op_sel_hi:[1,0,1]\n"
      "v_pk_fma_f32 v[150:151], v[170:171], v[66:67], v[150:151] op_sel_hi:[1,0,1]\n"
      "v_pk_fma_f32 v[162:163], v[122:123], v[66:67], v[162:163] op_sel_hi:[1,0,1]\n"
      "s_waitcnt vmcnt(5)\n"
      "v_lshlrev_b32 v168, 16, v124\n"
      "v_lshlrev_b32 v169, 16, v125\n"
      "v_lshlrev_b32 v170, 16, v126\n"
      "v_lshlrev_b32 v171, 16, v127\n"
      "v_and_b32 v124, 0xffff0000, v124\n"
      "v_and_b32 v125, 0xffff0000, v125\n"
      "v_and_b32 v126, 0xffff0000, v126\n"
      "v_and_b32 v127, 0xffff0000, v127\n"
      "v_pk_fma_f32 v[148:149], v[168:169], v[66:67], v[148:149] op_sel_hi:[1,0,1]\n"
      "v_pk_fma_f32 v[160:161], v[124:125], v[66:67], v[160:161] op_sel_hi:[1,0,1]\n"
      "v_pk_fma_f32 v[150:151], v[170:171], v[66:67], v[150:151] op_sel_hi:[1,0,1]\n"
      "v_pk_fma_f32 v[162:163], v[126:127], v[66:67], v[162:163] op_sel_hi:[1,0,1]\n"
      "v_cvt_pk_bf16_f32 v204, v148, v160\n"
      "v_cvt_pk_bf16_f32 v205, v149, v161\n"
      "v_cvt_pk_bf16_f32 v206, v150, v162\n"
      "v_cvt_pk_bf16_f32 v207, v151, v163\n"
      "buffer_store_dwordx4 v[204:207], v31, s[64:67], s34 offen offset:384\n"
      "s_waitcnt vmcnt(5)\n"
      "v_lshlrev_b32 v168, 16, v128\n"
      "v_lshlrev_b32 v169, 16, v129\n"
      "v_lshlrev_b32 v170, 16, v130\n"
      "v_lshlrev_b32 v171, 16, v131\n"
      "v_and_b32 v128, 0xffff0000, v128\n"
      "v_and_b32 v129, 0xffff0000, v129\n"
      "v_and_b32 v130, 0xffff0000, v130\n"
      "v_and_b32 v131, 0xffff0000, v131\n"
      "v_pk_fma_f32 v[152:153], v[168:169], v[66:67], v[152:153] op_sel_hi:[1,0,1]\n"
      "v_pk_fma_f32 v[164:165], v[128:129], v[66:67], v[164:165] op_sel_hi:[1,0,1]\n"
      "v_pk_fma_f32 v[154:155], v[170:171], v[66:67], v[154:155] op_sel_hi:[1,0,1]\n"
      "v_pk_fma_f32 v[166:167], v[130:131], v[66:67], v[166:167] op_sel_hi:[1,0,1]\n"
      "s_waitcnt vmcnt(4)\n"
      "v_lshlrev_b32 v168, 16, v132\n"
      "v_lshlrev_b32 v169, 16, v133\n"
      "v_lshlrev_b32 v170, 16, v134\n"
      "v_lshlrev_b32 v171, 16, v135\n"
      "v_and_b32 v132, 0xffff0000, v132\n"
      "v_and_b32 v133, 0xffff0000, v133\n"
      "v_and_b32 v134, 0xffff0000, v134\n"
      "v_and_b32 v135, 0xffff0000, v135\n"
      "v_pk_fma_f32 v[152:153], v[168:169], v[66:67], v[152:153] op_sel_hi:[1,0,1]\n"
      "v_pk_fma_f32 v[164:165], v[132:133], v[66:67], v[164:165] op_sel_hi:[1,0,1]\n"
      "v_pk_fma_f32 v[154:155], v[170:171], v[66:67], v[154:155] op_sel_hi:[1,0,1]\n"
      "v_pk_fma_f32 v[166:167], v[134:135], v[66:67], v[166:167] op_sel_hi:[1,0,1]\n"
      "s_waitcnt vmcnt(3)\n"
      "v_lshlrev_b32 v168, 16, v136\n"
      "v_lshlrev_b32 v169, 16, v137\n"
      "v_lshlrev_b32 v170, 16, v138\n"
      "v_lshlrev_b32 v171, 16, v139\n"
      "v_and_b32 v136, 0xffff0000, v136\n"
      "v_and_b32 v137, 0xffff0000, v137\n"
      "v_and_b32 v138, 0xffff0000, v138\n"
      "v_and_b32 v139, 0xffff0000, v139\n"
      "v_pk_fma_f32 v[152:153], v[168:169], v[66:67], v[152:153] op_sel_hi:[1,0,1]\n"
      "v_pk_fma_f32 v[164:165], v[136:137], v[66:67], v[164:165] op_sel_hi:[1,0,1]\n"
      "v_pk_fma_f32 v[154:155], v[170:171], v[66:67], v[154:155] op_sel_hi:[1,0,1]\n"
      "v_pk_fma_f32 v[166:167], v[138:139], v[66:67], v[166:167] op_sel_hi:[1,0,1]\n"
      "s_waitcnt vmcnt(2)\n"
      "v_lshlrev_b32 v168, 16, v140\n"
      "v_lshlrev_b32 v169, 16, v141\n"
      "v_lshlrev_b32 v170, 16, v142\n"
      "v_lshlrev_b32 v171, 16, v143\n"
      "v_and_b32 v140, 0xffff0000, v140\n"
      "v_and_b32 v141, 0xffff0000, v141\n"
      "v_and_b32 v142, 0xffff0000, v142\n"
      "v_and_b32 v143, 0xffff0000, v143\n"
      "v_pk_fma_f32 v[152:153], v[168:169], v[66:67], v[152:153] op_sel_hi:[1,0,1]\n"
      "v_pk_fma_f32 v[164:165], v[140:141], v[66:67], v[164:165] op_sel_hi:[1,0,1]\n"
      "v_pk_fma_f32 v[154:155], v[170:171], v[66:67], v[154:155] op_sel_hi:[1,0,1]\n"
      "v_pk_fma_f32 v[166:167], v[142:143], v[66:67], v[166:167] op_sel_hi:[1,0,1]\n"
      "v_cvt_pk_bf16_f32 v208, v152, v164\n"
      "v_cvt_pk_bf16_f32 v209, v153, v165\n"
      "v_cvt_pk_bf16_f32 v210, v154, v166\n"
      "v_cvt_pk_bf16_f32 v211, v155, v167\n"
      "buffer_store_dwordx4 v[208:211], v31, s[64:67], s34 offen offset:768\n"
      "s_or_b32 s90, s73, s72\n"
      "s_cmp_lg_u32 s90, 0\n"
      "s_cbranch_scc1 Lend_%=\n"
      "v_xor_b32 v75, s3, v75\n"
      "s_mov_b32 s95, 0\n"
      "buffer_store_dwordx2 v[74:75], v76, s[36:39], s95 offen sc1\n"
      "s_branch Lend_%=\n"
      "Lsl_22_%=:\n"
      "s_mov_b32 s8, 0\n"
      "Lpoll_39_%=:\n"
      "buffer_load_dwordx2 v[70:71], v22, s[36:39], 0 offen sc1\n"
      "s_add_u32 s8, s8, 1\n"
      "s_waitcnt vmcnt(0) lgkmcnt(0)\n"
      "v_cmp_ne_u64 vcc, s[76:77], v[70:71]\n"
      "s_cbranch_vccz Lpolld_40_%=\n"
      "s_cmp_lt_u32 s8, 40000\n"
      "s_cbranch_scc1 Lpoll_39_%=\n"
      "Lpolld_40_%=:\n"
      "s_branch Lbk_23_%=\n"
      "Lsl_25_%=:\n"
      "s_mov_b32 s8, 0\n"
      "Lpoll_41_%=:\n"
      "buffer_load_dwordx2 v[70:71], v69, s[36:39], 0 offen sc1\n"
      "s_add_u32 s8, s8, 1\n"
      "s_waitcnt vmcnt(0) lgkmcnt(0)\n"
      "v_cmp_ne_u64 vcc, s[76:77], v[70:71]\n"
      "s_cbranch_vccz Lpolld_42_%=\n"
      "s_cmp_lt_u32 s8, 40000\n"
      "s_cbranch_scc1 Lpoll_41_%=\n"
      "Lpolld_42_%=:\n"
      "s_branch Lbk_26_%=\n"
      "Lsl_27_%=:\n"
      "s_mov_b32 s8, 0\n"
      "Lpoll_43_%=:\n"
      "buffer_load_dwordx2 v[70:71], v23, s[36:39], 0 offen sc1\n"
      "s_add_u32 s8, s8, 1\n"
      "s_waitcnt vmcnt(0) lgkmcnt(0)\n"
      "v_cmp_ne_u64 vcc, s[76:77], v[70:71]\n"
      "s_cbranch_vccz Lpolld_44_%=\n"
      "s_cmp_lt_u32 s8, 40000\n"
      "s_cbranch_scc1 Lpoll_43_%=\n"
      "Lpolld_44_%=:\n"
      "s_branch Lbk_28_%=\n"
      "Lsl_29_%=:\n"
      "s_mov_b32 s8, 0\n"
      "Lpoll_45_%=:\n"
      "buffer_load_dwordx2 v[70:71], v23, s[36:39], 0 offen offset:256 sc1\n"
      "s_add_u32 s8, s8, 1\n"
      "s_waitcnt vmcnt(0) lgkmcnt(0)\n"
      "v_cmp_ne_u64 vcc, s[76:77], v[70:71]\n"
      "s_cbranch_vccz Lpolld_46_%=\n"
      "s_cmp_lt_u32 s8, 40000\n"
      "s_cbranch_scc1 Lpoll_45_%=\n"
      "Lpolld_46_%=:\n"
      "s_branch Lbk_30_%=\n"
      "Lsl_31_%=:\n"
      "s_mov_b32 s8, 0\n"
      "Lpoll_47_%=:\n"
      "buffer_load_dwordx2 v[70:71], v23, s[36:39], 0 offen offset:384 sc1\n"
      "s_add_u32 s8, s8, 1\n"
      "s_waitcnt vmcnt(0) lgkmcnt(0)\n"
      "v_cmp_ne_u64 vcc, s[76:77], v[70:71]\n"
      "s_cbranch_vccz Lpolld_48_%=\n"
      "s_cmp_lt_u32 s8, 40000\n"
      "s_cbranch_scc1 Lpoll_47_%=\n"
      "Lpolld_48_%=:\n"
      "s_branch Lbk_32_%=\n"
      "Lsl_33_%=:\n"
      "s_mov_b32 s8, 0\n"
      "Lpoll_49_%=:\n"
      "buffer_load_dwordx2 v[70:71], v23, s[36:39], 0 offen offset:640 sc1\n"
      "s_add_u32 s8, s8, 1\n"
      "s_waitcnt vmcnt(0) lgkmcnt(0)\n"
      "v_cmp_ne_u64 vcc, s[76:77], v[70:71]\n"
      "s_cbranch_vccz Lpolld_50_%=\n"
      "s_cmp_lt_u32 s8, 40000\n"
      "s_cbranch_scc1 Lpoll_49_%=\n"
      "Lpolld_50_%=:\n"
      "s_branch Lbk_34_%=\n"
      "Lsl_35_%=:\n"
      "s_mov_b32 s8, 0\n"
      "Lpoll_51_%=:\n"
      "buffer_load_dwordx2 v[70:71], v23, s[36:39], 0 offen offset:896 sc1\n"
      "s_add_u32 s8, s8, 1\n"
      "s_waitcnt vmcnt(0) lgkmcnt(0)\n"
      "v_cmp_ne_u64 vcc, s[76:77], v[70:71]\n"
      "s_cbranch_vccz Lpolld_52_%=\n"
      "s_cmp_lt_u32 s8, 40000\n"
      "s_cbranch_scc1 Lpoll_51_%=\n"
      "Lpolld_52_%=:\n"
      "s_branch Lbk_36_%=\n"
      "Lend_%=:\n"
      :
      : "s"(__builtin_amdgcn_kernarg_segment_ptr()), "s"(blockIdx.x), "v"(threadIdx.x), "s"(fnet_dispatch_id())
      : "v1", "v2", "v3", "v4", "v5", "v6", "v7", "v8", "v9", "v10", "v11", "v12", "v13", "v14", "v15", "v16", "v17", "v18", "v19", "v20", "v21", "v22", "v23", "v24", "v25", "v26", "v27", "v28", "v29", "v30", "v31", "v32", "v33", "v34", "v35", "v36", "v37", "v38", "v39", "v40", "v41", "v42", "v43", "v44", "v45", "v46", "v47", "v48", "v49", "v50", "v51", "v52", "v53", "v54", "v55", "v56", "v57", "v58", "v59", "v60", "v61", "v62", "v63", "v64", "v65", "v66", "v67", "v68", "v69", "v70", "v71", "v72", "v73", "v74", "v75", "v76", "v77", "v78", "v79", "v80", "v81", "v82", "v83", "v84", "v85", "v86", "v87", "v88", "v89", "v90", "v91", "v92", "v93", "v94", "v95", "v96", "v97", "v98", "v99", "v100", "v101", "v102", "v103", "v104", "v105", "v106", "v107", "v108", "v109", "v110", "v111", "v112", "v113", "v114", "v115", "v116", "v117", "v118", "v119", "v120", "v121", "v122", "v123", "v124", "v125", "v126", "v127", "v128", "v129", "v130", "v131", "v132", "v133", "v134", "v135", "v136", "v137", "v138", "v139", "v140", "v141", "v142", "v143", "v144", "v145", "v146", "v147", "v148", "v149", "v150", "v151", "v152", "v153", "v154", "v155", "v156", "v157", "v158", "v159", "v160", "v161", "v162", "v163", "v164", "v165", "v166", "v167", "v168", "v169", "v170", "v171", "v172", "v173", "v174", "v175", "v176", "v177", "v178", "v179", "v180", "v181", "v182", "v183", "v184", "v185", "v186", "v187", "v188", "v189", "v190", "v191", "v192", "v193", "v194", "v195", "v196", "v197", "v198", "v199", "v200", "v201", "v202", "v203", "v204", "v205", "v206", "v207", "v208", "v209", "v210", "v211", "v212", "v213", "v214", "v215", "v216", "v217", "v218", "v219", "v220", "v221", "v222", "v223", "v224", "v225", "v226", "v227", "v228", "v229", "v230", "v231", "v232", "v233", "v234", "v235", "v236", "v237", "v238", "v239", "v240", "v241", "v242", "v243", "v244", "v245", "v246", "v247", "v248", "v249", "v250", "v251", "v252", "v253", "v254", "v255", "a0", "a1", "a2", "a3", "a4", "a5", "a6", "a7", "a8", "a9", "a10", "a11", "a12", "a13", "a14", "a15", "a16", "a17", "a18", "a19", "a20", "a21", "a22", "a23", "a24", "a25", "a26", "a27", "a28", "a29", "a30", "a31", "a32", "a33", "a34", "a35", "a36", "a37", "a38", "a39", "a40", "a41", "a42", "a43", "a44", "a45", "a46", "a47", "a48", "a49", "a50", "a51", "a52", "a53", "a54", "a55", "a56", "a57", "a58", "a59", "a60", "a61", "a62", "a63", "a64", "a65", "a66", "a67", "a68", "a69", "a70", "a71", "a72", "a73", "a74", "a75", "a76", "a77", "a78", "a79", "a80", "a81", "a82", "a83", "a84", "a85", "a86", "a87", "a88", "a89", "a90", "a91", "a92", "a93", "a94", "a95", "a96", "a97", "a98", "a99", "a100", "a101", "a102", "a103", "a104", "a105", "a106", "a107", "a108", "a109", "a110", "a111", "a112", "a113", "a114", "a115", "a116", "a117", "a118", "a119", "a120", "a121", "a122", "a123", "a124", "a125", "a126", "a127", "a128", "a129", "a130", "a131", "a132", "a133", "a134", "a135", "a136", "a137", "a138", "a139", "a140", "a141", "a142", "a143", "a144", "a145", "a146", "a147", "a148", "a149", "a150", "a151", "a152", "a153", "a154", "a155", "a156", "a157", "a158", "a159", "a160", "a161", "a162", "a163", "a164", "a165", "a166", "a167", "a168", "a169", "a170", "a171", "a172", "a173", "a174", "a175", "a176", "a177", "a178", "a179", "a180", "a181", "a182", "a183", "a184", "a185", "a186", "a187", "a188", "a189", "a190", "a191", "a192", "a193", "a194", "a195", "a196", "a197", "a198", "a199", "a200", "a201", "a202", "a203", "a204", "a205", "a206", "a207", "a208", "a209", "a210", "a211", "a212", "a213", "a214", "a215", "a216", "a217", "a218", "a219", "a220", "a221", "a222", "a223", "a224", "a225", "a226", "a227", "a228", "a229", "a230", "a231", "a232", "a233", "a234", "a235", "a236", "a237", "a238", "a239", "a240", "a241", "a242", "a243", "a244", "a245", "a246", "a247", "a248", "a249", "a250", "a251", "a252", "a253", "a254", "a255", "s6", "s7", "s8", "s9", "s10", "s11", "s12", "s13", "s14", "s15", "s16", "s17", "s18", "s19", "s20", "s21", "s22", "s23", "s24", "s25", "s26", "s27", "s28", "s29", "s30", "s31", "s33", "s34", "s35", "s36", "s37", "s38", "s39", "s40", "s41", "s42", "s43", "s44", "s45", "s46", "s47", "s48", "s49", "s50", "s51", "s52", "s53", "s54", "s55", "s56", "s57", "s58", "s59", "s60", "s61", "s62", "s63", "s64", "s65", "s66", "s67", "s68", "s69", "s70", "s71", "s72", "s73", "s74", "s75", "s76", "s77", "s78", "s79", "s80", "s81", "s82", "s83", "s84", "s85", "s86", "s87", "s88", "s89", "s90", "s91", "s92", "s93", "s94", "s95", "s96", "s97", "s98", "s99", "vcc", "scc", "memory");
}

extern "C" void kernel_launch(void* const* d_in, const int* in_sizes, int n_in, void* d_out, int out_size, void* d_ws, size_t ws_size,
                              hipStream_t stream) {
  if (ws_size < 0x4000000u) return;
  fnet_glu_kernel<<<256, 256, 0, stream>>>(d_in[0], d_in[1], d_in[2], d_in[3], d_in[4], d_in[6], d_in[7], d_in[8], d_out, d_ws);
}
```
